```python
import torch

def _softmax(x, axis=-1):
    return torch.softmax(x, dim=axis)
def _gelu(x):
    return torch.nn.functional.gelu(x, approximate="tanh")
def _silu(x):
    return torch.nn.functional.silu(x)
def _sigmoid(x):
    return torch.sigmoid(x)
def _layernorm(x, axis=-1, eps=1e-5):
    m = x.mean(dim=axis, keepdim=True)
    v = x.var(dim=axis, keepdim=True, unbiased=False)
    return (x - m) / torch.sqrt(v + eps)
def _scatter_add(tgt, idx, src, axis=0):
    return tgt.clone().index_add_(axis, idx, src)
def _segment_sum(src, seg, n):
    out = torch.zeros((n,) + src.shape[1:], dtype=src.dtype)
    return out.index_add_(0, seg, src)
def _pad(x, pw):
    flat = tuple(v for pair in reversed(pw) for v in pair)
    return torch.nn.functional.pad(x, flat)

kernel_name = 'concat_heads__sliding_block__sliding_local_dilated__prompt'

def setup_inputs():

    _w = (lambda a: (lambda t: t.to(torch.bfloat16) if t.is_floating_point() else t)(torch.as_tensor(a)))
    def _A_setup():
        B, S, D = 1, 2048, 448
        return {
            "x": _w((lambda *s: torch.randn(*s, dtype=torch.bfloat16) * 0.2)(B, S, D)),
            "wqkv": _w((lambda *s: torch.randn(*s, dtype=torch.bfloat16) * 0.4)(D, 3 * D)),
            "wo": _w((lambda *s: torch.randn(*s, dtype=torch.bfloat16) * 0.4)(D, D)),
        }
    _A_in = _A_setup()

    def _B_setup():
        B, S, D = 1, 2048, 448
        return {
            "x": _w((lambda *s: torch.randn(*s, dtype=torch.bfloat16) * 0.2)(B, S, D)),
            "wqkv": _w((lambda *s: torch.randn(*s, dtype=torch.bfloat16) * 0.4)(D, 3 * D)),
            "wo": _w((lambda *s: torch.randn(*s, dtype=torch.bfloat16) * 0.4)(D, D)),
        }
    _B_in = _B_setup()

    return {
        "A_x": _A_in["x"],
        "A_wqkv": _A_in["wqkv"],
        "A_wo": _A_in["wo"],
        "B_x": _B_in["x"],
        "B_wqkv": _B_in["wqkv"],
        "B_wo": _B_in["wo"],
    }

def reference(A_x, A_wqkv, A_wo, B_x, B_wqkv, B_wo):
    def _A_ref(x, wqkv, wo):
        B, S, D = x.shape
        H = 16; Dh = D // H; W = 256
        qkv = (lambda x, *d: x.permute(*d))((x @ wqkv).reshape(B, S, 3, H, Dh), 2, 0, 3, 1, 4)
        q, k, v = qkv[0], qkv[1], qkv[2]
        scores = torch.einsum("bhqd,bhkd->bhqk", q, k) * (Dh ** -0.5)
        idx = torch.arange(S)
        mask = (idx[:, None] - idx[None, :] >= 0) & (idx[:, None] - idx[None, :] < W)
        scores = torch.where(mask[None, None], scores, -1e9)
        p = _softmax(scores, axis=-1)
        o = torch.einsum("bhqk,bhkd->bhqd", p, v)
        return (lambda x, *d: x.permute(*d))(o, 0, 2, 1, 3).reshape(B, S, D) @ wo

    def _B_ref(x, wqkv, wo):
        B, S, D = x.shape
        H = 16; Dh = D // H
        W, dil = 8, 2
        qkv = (lambda x, *d: x.permute(*d))((x @ wqkv).reshape(B, S, 3, H, Dh), 2, 0, 3, 1, 4)
        q, k, v = qkv[0], qkv[1], qkv[2]
        scale = Dh ** -0.5
        pos = torch.arange(S)
        off = torch.arange(W) * dil
        idx = pos[:, None] - off[None, :]
        mask = idx >= 0
        idx_c = torch.where(mask, idx, 0).reshape(-1)
        kg = k[:, :, idx_c, :].reshape(B, H, S, W, Dh)
        vg = v[:, :, idx_c, :].reshape(B, H, S, W, Dh)
        logits = torch.einsum("bhsd,bhswd->bhsw", q, kg) * scale
        logits = torch.where(mask[None, None, :, :], logits, -1e9)
        attn = _softmax(logits, axis=-1)
        o = torch.einsum("bhsw,bhswd->bhsd", attn, vg)
        return (lambda x, *d: x.permute(*d))(o, 0, 2, 1, 3).reshape(B, S, D) @ wo

    a = _A_ref(A_x, A_wqkv, A_wo)
    b = _B_ref(B_x, B_wqkv, B_wo)
    return torch.concatenate([a, b], axis=-1)
```

```cpp
#include <hip/hip_runtime.h>
#include <hip/hip_bf16.h>

typedef __bf16 bf16x8 __attribute__((ext_vector_type(8)));
typedef __bf16 bf16x4 __attribute__((ext_vector_type(4)));
typedef short s16x4 __attribute__((ext_vector_type(4)));
typedef float f32x16 __attribute__((ext_vector_type(16)));
typedef float f32x8 __attribute__((ext_vector_type(8)));
typedef unsigned u32x4 __attribute__((ext_vector_type(4)));
typedef unsigned u32x2 __attribute__((ext_vector_type(2)));
#define LAS __attribute__((address_space(3)))
#define DI __device__ __forceinline__
#define MFMA(a, b, c) __builtin_amdgcn_mfma_f32_32x32x16_bf16((a), (b), (c), 0, 0, 0)

constexpr int S = 2048, D = 448, N3 = 1344;
constexpr int XB = S * D * 2, WB = D * N3 * 2, OB = D * D * 2;
constexpr int QKV_BYTES = S * N3 * 2;
constexpr int O_BYTES = S * D * 2;
constexpr int STAGE = 40960;

DI __amdgpu_buffer_rsrc_t rsrc(const void* p, int bytes) { return __builtin_amdgcn_make_buffer_rsrc((void*)p, 0, bytes, 0x00020000); }
DI void dma16(__amdgpu_buffer_rsrc_t r, char* lds, int voff, int soff) {
    __builtin_amdgcn_raw_ptr_buffer_load_lds(r, (LAS void*)lds, 16, voff, soff, 0, 0);
}
DI bf16x8 tr_frag(const char* a0, const char* a1) {
    s16x4 lo = __builtin_amdgcn_ds_read_tr16_b64_v4i16((LAS s16x4*)a0);
    s16x4 hi = __builtin_amdgcn_ds_read_tr16_b64_v4i16((LAS s16x4*)a1);
    return __builtin_bit_cast(bf16x8, __builtin_shufflevector(lo, hi, 0, 1, 2, 3, 4, 5, 6, 7));
}
DI bf16x8 pack8(const f32x16& v, int s) {
    f32x8 t = {v[8 * s], v[8 * s + 1], v[8 * s + 2], v[8 * s + 3], v[8 * s + 4], v[8 * s + 5], v[8 * s + 6], v[8 * s + 7]};
    return __builtin_convertvector(t, bf16x8);
}
#define WAITV(n) asm volatile("s_waitcnt vmcnt(" #n ")" ::: "memory")
#define WAITL0() asm volatile("s_waitcnt lgkmcnt(0)" ::: "memory")
#define BAR() __builtin_amdgcn_s_barrier()

__global__ void __launch_bounds__(512, 2) k_qkv(const char* xa, const char* wa, const char* xb, const char* wb, char* qkv) {
    extern __shared__ __attribute__((aligned(16))) char smem[];
    const int b = blockIdx.x, xcd = b & 7, idx = b >> 3;
    if (idx >= 28) return;
    const int br = xcd >> 2, jj = idx / 7, g = idx - jj * 7, j = (xcd & 3) * 4 + jj;
    const int wave = __builtin_amdgcn_readfirstlane(threadIdx.x >> 6), lane = threadIdx.x & 63;
    const int rot = (g + 2 * jj) % 7;
    if (wave >= 4) {
        const int lw = wave - 4, r3 = lane >> 3, u = lane & 7;
        const __amdgpu_buffer_rsrc_t rx = rsrc(br ? xb : xa, XB), rw = rsrc(br ? wb : wa, WB);
        const int vx0 = (j * 128 + lw * 32 + r3) * 896 + ((u ^ (r3 >> 1)) << 4);
        const int vx1 = vx0 ^ 64;
        const int vw = (lw * 16 + r3) * 2688 + g * 384 + ((u ^ (((r3 >> 1) & 1) << 2)) << 4);
        auto issue = [&](int c) {
            int ch = c + rot; if (ch >= 7) ch -= 7;
            char* st = smem + (c % 3) * STAGE;
#pragma unroll
            for (int i = 0; i < 4; ++i) dma16(rx, st + (lw * 32 + i * 8) * 128, (i & 1) ? vx1 : vx0, ch * 128 + i * 7168);
#pragma unroll
            for (int cb = 0; cb < 3; ++cb)
#pragma unroll
                for (int i = 0; i < 2; ++i) dma16(rw, st + 16384 + cb * 8192 + (lw * 16 + i * 8) * 128, vw, ch * (64 * 2688) + cb * 128 + i * (8 * 2688));
        };
        issue(0); issue(1); issue(2);
        WAITV(20); BAR();
#pragma unroll
        for (int c = 1; c < 6; ++c) { WAITV(10); BAR(); if (c + 2 < 7) issue(c + 2); }
        WAITV(0); BAR();
        return;
    }
    const int wr = wave >> 1, wc = wave & 1, hh = lane >> 5, t = lane & 31;
    const int blk = (lane >> 4) & 1, q = (lane >> 2) & 3, p = lane & 3;
    int xo[4], wo_[3];
#pragma unroll
    for (int ks = 0; ks < 4; ++ks) xo[ks] = (wr * 64 + t) * 128 + (((2 * ks + hh) ^ ((t >> 1) & 7)) << 4);
#pragma unroll
    for (int i = 0; i < 3; ++i) { const int ct = 3 * wc + i, cb = ct >> 1, half = ct & 1;
        wo_[i] = 16384 + cb * 8192 + (8 * hh + q) * 128 + ((((half ^ (q >> 1)) << 2) + blk * 2 + (p & 1)) << 4) + (p >> 1) * 8; }
    f32x16 acc[2][3] = {};
#pragma unroll 1
    for (int c = 0; c < 7; ++c) {
        BAR();
        const char* st = smem + (c % 3) * STAGE;
#pragma unroll
        for (int ks = 0; ks < 4; ++ks) {
            bf16x8 xf[2], wf[3];
#pragma unroll
            for (int tt = 0; tt < 2; ++tt) xf[tt] = *(const bf16x8*)(st + xo[ks] + tt * 4096);
#pragma unroll
            for (int i = 0; i < 3; ++i) wf[i] = tr_frag(st + wo_[i] + ks * 2048, st + wo_[i] + ks * 2048 + 512);
#pragma unroll
            for (int i = 0; i < 3; ++i)
#pragma unroll
                for (int tt = 0; tt < 2; ++tt) acc[tt][i] = MFMA(wf[i], xf[tt], acc[tt][i]);
        }
        WAITL0();
    }
    char* dst = qkv + br * QKV_BYTES;
    const float qs = 0.18898223650461361f * 1.4426950408889634f;
#pragma unroll
    for (int i = 0; i < 3; ++i) {
        const int cbase = 24 * g + 4 * (3 * wc + i);
        const float sc = cbase < 56 ? qs : 1.0f;
#pragma unroll
        for (int tt = 0; tt < 2; ++tt) {
            const int T = j * 4 + wr * 2 + tt;
            f32x16 v = acc[tt][i] * sc;
#pragma unroll
            for (int s = 0; s < 2; ++s) {
                const int cc = 2 * s + hh;
                *(bf16x8*)(dst + ((T * 168 + cbase + cc) * 32 + (((t >> 2) ^ cc) << 2) + (t & 3)) * 16) = pack8(v, s);
            }
        }
    }
}

__global__ void __launch_bounds__(512, 2) k_attn(const char* qkv, char* ows) {
    __shared__ __attribute__((aligned(16))) char smem[17 * 4096];
    const int b = blockIdx.x, xcd = b & 7, idx = b >> 3;
    const int h = xcd * 2 + (idx & 1), j = idx >> 1;
    const int wave = __builtin_amdgcn_readfirstlane(threadIdx.x >> 6), lane = threadIdx.x & 63;
    const int hh = lane >> 5, t = lane & 31, blk = (lane >> 4) & 1, q = (lane >> 2) & 3, p = lane & 3;
    const int c0 = (7 * h) >> 1, odd = h & 1;
    const int br = wave >> 2, w4 = wave & 3;
    const __amdgpu_buffer_rsrc_t rq = rsrc(qkv, 2 * QKV_BYTES);
    for (int s = wave; s < 17; s += 8) {
        const int sb = s >= 12, T = sb ? 4 * j - 1 + (s - 12) : 4 * j - 8 + s;
        if (T >= 0) {
            const int base = sb * QKV_BYTES + (T * 168 + 56 + c0) * 512;
            dma16(rq, smem + s * 4096, lane * 16, base);
            dma16(rq, smem + s * 4096 + 1024, lane * 16, base + 1024);
            dma16(rq, smem + s * 4096 + 2048, lane * 16, base + 56 * 512);
            dma16(rq, smem + s * 4096 + 3072, lane * 16, base + 56 * 512 + 1024);
        }
    }
    const int T = 4 * j + w4;
    int fo[2];
#pragma unroll
    for (int s = 0; s < 2; ++s) fo[s] = (2 * s + hh) * 512 + ((((t >> 2) ^ ((c0 + 2 * s + hh) & 3)) << 2) + (t & 3)) * 16;
    u32x4 qf[2];
#pragma unroll
    for (int s = 0; s < 2; ++s) qf[s] = *(const u32x4*)(qkv + br * QKV_BYTES + (T * 168 + c0) * 512 + fo[s]);
    if (odd) { if (!hh) { qf[0].x = 0; qf[0].y = 0; } } else { if (hh) { qf[1].z = 0; qf[1].w = 0; } }
    WAITV(0);
    for (int s = wave; s < 17; s += 8) {
        if (lane < 32) *(u32x2*)(smem + s * 4096 + 2048 + (odd ? 0 : 3 * 512 + 8) + lane * 16) = (u32x2){0x00003F80u, 0u};
    }
    __syncthreads();
    const bf16x8 q0 = __builtin_bit_cast(bf16x8, qf[0]), q1 = __builtin_bit_cast(bf16x8, qf[1]);
    int vo[2];
    { const int cc = 2 * blk + (p & 1);
#pragma unroll
      for (int tt = 0; tt < 2; ++tt) vo[tt] = 2048 + cc * 512 + ((((2 * tt + hh) ^ ((c0 + cc) & 3)) << 2) + q) * 16 + (p >> 1) * 8; }
    const int sd = br ? 13 + w4 : 8 + w4;
    const int so = br ? 12 + w4 : w4;
    const bool old_ok = br ? (T >= 1) : (T >= 8);
    const int nmid = br ? 0 : (T >= 7 ? 7 : T);
    auto qk = [&](int slot, const f32x16& c) {
        const char* kb = smem + slot * 4096;
        f32x16 r = MFMA(*(const bf16x8*)(kb + fo[0]), q0, c);
        return MFMA(*(const bf16x8*)(kb + fo[1]), q1, r);
    };
    auto pv = [&](int slot, bf16x8 p0, bf16x8 p1, f32x16& o) {
        const char* vb = smem + slot * 4096;
        o = MFMA(tr_frag(vb + vo[0], vb + vo[1]), p0, o);
        o = MFMA(tr_frag(vb + vo[0] + 256, vb + vo[1] + 256), p1, o);
    };
    const f32x16 zero = {};
    const float NEG = -3.0e38f;
    f32x16 sm;
    {
        const f32x16 sdg = qk(sd, zero), sol = qk(old_ok ? so : sd, zero);
#pragma unroll
        for (int r = 0; r < 16; ++r) {
            const int key = (r & 3) + 8 * (r >> 2) + 4 * hh;
            const int dist = (t - key) & 31;
            const bool lo = key <= t;
            const bool ok = (lo || old_ok) && (!br || (!(dist & 1) && dist <= 14));
            sm[r] = ok ? (lo ? sdg[r] : sol[r]) : NEG;
        }
    }
    float m = sm[0];
#pragma unroll
    for (int r = 1; r < 16; ++r) m = fmaxf(m, sm[r]);
    m = fmaxf(m, __shfl_xor(m, 32));
    f32x16 o; float l;
    for (int pass = 0;; ++pass) {
        o = zero;
        {
            f32x16 e;
#pragma unroll
            for (int r = 0; r < 16; ++r) e[r] = __builtin_amdgcn_exp2f(sm[r] - m);
            f32x16 ed, eo;
#pragma unroll
            for (int r = 0; r < 16; ++r) { const int key = (r & 3) + 8 * (r >> 2) + 4 * hh; ed[r] = key <= t ? e[r] : 0.f; eo[r] = key <= t ? 0.f : e[r]; }
            pv(sd, pack8(ed, 0), pack8(ed, 1), o);
            if (old_ok) pv(so, pack8(eo, 0), pack8(eo, 1), o);
        }
        f32x16 cm;
#pragma unroll
        for (int r = 0; r < 16; ++r) cm[r] = -m;
        for (int i = 1; i <= nmid; ++i) {
            f32x16 s = qk(sd - i, cm);
#pragma unroll
            for (int r = 0; r < 16; ++r) s[r] = __builtin_amdgcn_exp2f(s[r]);
            pv(sd - i, pack8(s, 0), pack8(s, 1), o);
        }
        l = __shfl(odd ? o[0] : o[12], odd ? t : t + 32);
        if (pass || __all(l < 1.0e9f)) break;
        for (int i = 1; i <= nmid; ++i) {
            const f32x16 s = qk(sd - i, zero);
#pragma unroll
            for (int r = 0; r < 16; ++r) m = fmaxf(m, s[r]);
        }
        m = fmaxf(m, __shfl_xor(m, 32));
    }
    const float inv = 1.0f / l;
    o = o * inv;
    char* dst = ows + br * O_BYTES + (T * 56 + c0) * 512 + t * 16;
    const u32x4 o0 = __builtin_bit_cast(u32x4, pack8(o, 0)), o1 = __builtin_bit_cast(u32x4, pack8(o, 1));
    if (odd) {
        if (hh) *(u32x4*)(dst + 512) = o0; else *(u32x2*)(dst + 8) = (u32x2){o0.z, o0.w};
        *(u32x4*)(dst + (2 + hh) * 512) = o1;
    } else {
        *(u32x4*)(dst + hh * 512) = o0;
        if (hh) *(u32x2*)(dst + 3 * 512) = (u32x2){o1.x, o1.y}; else *(u32x4*)(dst + 2 * 512) = o1;
    }
}

__global__ void __launch_bounds__(256, 1) k_out(const char* ows, const char* woa, const char* wob, char* out) {
    __shared__ __attribute__((aligned(16))) char smem[57344 + 4 * 4096];
    const int b = blockIdx.x, xcd = b & 7, idx = b >> 3;
    if (idx >= 28) return;
    const int br = xcd >> 2, jj = idx / 7, g = idx - jj * 7, j = (xcd & 3) * 4 + jj;
    const int wave = __builtin_amdgcn_readfirstlane(threadIdx.x >> 6), lane = threadIdx.x & 63;
    const int hh = lane >> 5, t = lane & 31, blk = (lane >> 4) & 1, q = (lane >> 2) & 3, p = lane & 3;
    const int r3 = lane >> 3, u = lane & 7;
    const __amdgpu_buffer_rsrc_t rw = rsrc(br ? wob : woa, OB);
    const int vw = r3 * 896 + g * 128 + ((u ^ (((r3 >> 1) & 1) << 2)) << 4);
#pragma unroll
    for (int i = 0; i < 14; ++i) dma16(rw, smem + (i * 4 + wave) * 1024, vw, (i * 4 + wave) * (8 * 896));
    const int T = 4 * j + wave;
    const char* src = ows + br * O_BYTES + T * (56 * 512) + lane * 16;
    bf16x8 of[28];
#pragma unroll
    for (int ks = 0; ks < 28; ++ks) of[ks] = *(const bf16x8*)(src + ks * 1024);
    WAITV(0);
    __syncthreads();
    int wo_[2];
#pragma unroll
    for (int i = 0; i < 2; ++i) wo_[i] = (8 * hh + q) * 128 + ((((i ^ (q >> 1)) << 2) + blk * 2 + (p & 1)) << 4) + (p >> 1) * 8;
    f32x16 acc[2] = {};
#pragma unroll
    for (int ks = 0; ks < 28; ++ks)
#pragma unroll
        for (int i = 0; i < 2; ++i) acc[i] = MFMA(tr_frag(smem + wo_[i] + ks * 2048, smem + wo_[i] + ks * 2048 + 512), of[ks], acc[i]);
    char* tb = smem + 57344 + wave * 4096;
#pragma unroll
    for (int i = 0; i < 2; ++i)
#pragma unroll
        for (int s = 0; s < 2; ++s) *(bf16x8*)(tb + t * 128 + (((4 * i + 2 * s + hh) ^ (t & 7)) << 4)) = pack8(acc[i], s);
    WAITL0();
#pragma unroll
    for (int i = 0; i < 4; ++i) {
        const int row = i * 8 + r3;
        const u32x4 v = *(const u32x4*)(tb + row * 128 + ((u ^ (row & 7)) << 4));
        *(u32x4*)(out + (T * 32 + row) * 1792 + br * 896 + g * 128 + u * 16) = v;
    }
}

extern "C" void kernel_launch(void* const* d_in, const int* in_sizes, int n_in, void* d_out, int out_size, void* d_ws, size_t ws_size, hipStream_t stream) {
    char* ws = (char*)d_ws;
    char* qkv = ws; char* ows = ws + 2 * QKV_BYTES;
    (void)hipFuncSetAttribute((const void*)k_qkv, hipFuncAttributeMaxDynamicSharedMemorySize, 3 * STAGE);
    k_qkv<<<256, 512, 3 * STAGE, stream>>>((const char*)d_in[0], (const char*)d_in[1], (const char*)d_in[3], (const char*)d_in[4], qkv);
    k_attn<<<256, 512, 0, stream>>>(qkv, ows);
    k_out<<<256, 256, 0, stream>>>(ows, (const char*)d_in[2], (const char*)d_in[5], (char*)d_out);
}
```

```cpp
#define DIAG 1
#include <hip/hip_runtime.h>
#include <hip/hip_bf16.h>

typedef __bf16 bf16x8 __attribute__((ext_vector_type(8)));
typedef __bf16 bf16x4 __attribute__((ext_vector_type(4)));
typedef short s16x4 __attribute__((ext_vector_type(4)));
typedef float f32x16 __attribute__((ext_vector_type(16)));
typedef float f32x8 __attribute__((ext_vector_type(8)));
typedef unsigned u32x4 __attribute__((ext_vector_type(4)));
typedef unsigned u32x2 __attribute__((ext_vector_type(2)));
#define LAS __attribute__((address_space(3)))
#define DI __device__ __forceinline__
#define MFMA(a, b, c) __builtin_amdgcn_mfma_f32_32x32x16_bf16((a), (b), (c), 0, 0, 0)

constexpr int S = 2048, D = 448, N3 = 1344;
constexpr int XB = S * D * 2, WB = D * N3 * 2, OB = D * D * 2;
constexpr int QKV_BYTES = S * N3 * 2;
constexpr int O_BYTES = S * D * 2;
constexpr int STAGE = 40960;

DI __amdgpu_buffer_rsrc_t rsrc(const void* p, int bytes) { return __builtin_amdgcn_make_buffer_rsrc((void*)p, 0, bytes, 0x00020000); }
DI void dma16(__amdgpu_buffer_rsrc_t r, char* lds, int voff, int soff) {
    __builtin_amdgcn_raw_ptr_buffer_load_lds(r, (LAS void*)lds, 16, voff, soff, 0, 0);
}
DI bf16x8 tr_frag(const char* a0, const char* a1) {
    s16x4 lo = __builtin_amdgcn_ds_read_tr16_b64_v4i16((LAS s16x4*)a0);
    s16x4 hi = __builtin_amdgcn_ds_read_tr16_b64_v4i16((LAS s16x4*)a1);
    return __builtin_bit_cast(bf16x8, __builtin_shufflevector(lo, hi, 0, 1, 2, 3, 4, 5, 6, 7));
}
DI bf16x8 pack8(const f32x16& v, int s) {
    f32x8 t = {v[8 * s], v[8 * s + 1], v[8 * s + 2], v[8 * s + 3], v[8 * s + 4], v[8 * s + 5], v[8 * s + 6], v[8 * s + 7]};
    return __builtin_convertvector(t, bf16x8);
}
#define WAITV(n) asm volatile("s_waitcnt vmcnt(" #n ")" ::: "memory")
#define WAITL0() asm volatile("s_waitcnt lgkmcnt(0)" ::: "memory")
#define BAR() __builtin_amdgcn_s_barrier()

__device__ unsigned long long g_diag[3 * 256 * 8 * 16];
#define STAMP(K, i) do { unsigned long long t_; asm volatile("s_memrealtime %0\n\ts_waitcnt lgkmcnt(0)" : "=s"(t_) :: "memory"); if ((threadIdx.x & 63) == 0) g_diag[(((K) * 256 + blockIdx.x) * 8 + (threadIdx.x >> 6)) * 16 + (i)] = t_; } while (0)
__global__ void __launch_bounds__(512, 2) k_qkv(const char* xa, const char* wa, const char* xb, const char* wb, char* qkv) {
    extern __shared__ __attribute__((aligned(16))) char smem[];
    const int b = blockIdx.x, xcd = b & 7, idx = b >> 3;
    if (idx >= 28) return;
    STAMP(0, 0);
    const int br = xcd >> 2, jj = idx / 7, g = idx - jj * 7, j = (xcd & 3) * 4 + jj;
    const int wave = __builtin_amdgcn_readfirstlane(threadIdx.x >> 6), lane = threadIdx.x & 63;
    const int rot = (g + 2 * jj) % 7;
    if (wave >= 4) {
        const int lw = wave - 4, r3 = lane >> 3, u = lane & 7;
        const __amdgpu_buffer_rsrc_t rx = rsrc(br ? xb : xa, XB), rw = rsrc(br ? wb : wa, WB);
        const int vx0 = (j * 128 + lw * 32 + r3) * 896 + ((u ^ (r3 >> 1)) << 4);
        const int vx1 = vx0 ^ 64;
        const int vw = (lw * 16 + r3) * 2688 + g * 384 + ((u ^ (((r3 >> 1) & 1) << 2)) << 4);
        auto issue = [&](int c) {
            int ch = c + rot; if (ch >= 7) ch -= 7;
            char* st = smem + (c % 3) * STAGE;
#pragma unroll
            for (int i = 0; i < 4; ++i) dma16(rx, st + (lw * 32 + i * 8) * 128, (i & 1) ? vx1 : vx0, ch * 128 + i * 7168);
#pragma unroll
            for (int cb = 0; cb < 3; ++cb)
#pragma unroll
                for (int i = 0; i < 2; ++i) dma16(rw, st + 16384 + cb * 8192 + (lw * 16 + i * 8) * 128, vw, ch * (64 * 2688) + cb * 128 + i * (8 * 2688));
        };
        issue(0); issue(1); issue(2);
        WAITV(20); BAR();
#pragma unroll
        for (int c = 1; c < 6; ++c) { WAITV(10); BAR(); if (c + 2 < 7) issue(c + 2); }
        WAITV(0); BAR();
        return;
    }
    const int wr = wave >> 1, wc = wave & 1, hh = lane >> 5, t = lane & 31;
    const int blk = (lane >> 4) & 1, q = (lane >> 2) & 3, p = lane & 3;
    int xo[4], wo_[3];
#pragma unroll
    for (int ks = 0; ks < 4; ++ks) xo[ks] = (wr * 64 + t) * 128 + (((2 * ks + hh) ^ ((t >> 1) & 7)) << 4);
#pragma unroll
    for (int i = 0; i < 3; ++i) { const int ct = 3 * wc + i, cb = ct >> 1, half = ct & 1;
        wo_[i] = 16384 + cb * 8192 + (8 * hh + q) * 128 + ((((half ^ (q >> 1)) << 2) + blk * 2 + (p & 1)) << 4) + (p >> 1) * 8; }
    f32x16 acc[2][3] = {};
#pragma unroll 1
    for (int c = 0; c < 7; ++c) {
        BAR();
        STAMP(0, 1 + c);
        const char* st = smem + (c % 3) * STAGE;
#pragma unroll
        for (int ks = 0; ks < 4; ++ks) {
            bf16x8 xf[2], wf[3];
#pragma unroll
            for (int tt = 0; tt < 2; ++tt) xf[tt] = *(const bf16x8*)(st + xo[ks] + tt * 4096);
#pragma unroll
            for (int i = 0; i < 3; ++i) wf[i] = tr_frag(st + wo_[i] + ks * 2048, st + wo_[i] + ks * 2048 + 512);
#pragma unroll
            for (int i = 0; i < 3; ++i)
#pragma unroll
                for (int tt = 0; tt < 2; ++tt) acc[tt][i] = MFMA(wf[i], xf[tt], acc[tt][i]);
        }
        WAITL0();
    }
    STAMP(0, 8);
    char* dst = qkv + br * QKV_BYTES;
    const float qs = 0.18898223650461361f * 1.4426950408889634f;
#pragma unroll
    for (int i = 0; i < 3; ++i) {
        const int cbase = 24 * g + 4 * (3 * wc + i);
        const float sc = cbase < 56 ? qs : 1.0f;
#pragma unroll
        for (int tt = 0; tt < 2; ++tt) {
            const int T = j * 4 + wr * 2 + tt;
            f32x16 v = acc[tt][i] * sc;
#pragma unroll
            for (int s = 0; s < 2; ++s) {
                const int cc = 2 * s + hh;
                *(bf16x8*)(dst + ((T * 168 + cbase + cc) * 32 + (((t >> 2) ^ cc) << 2) + (t & 3)) * 16) = pack8(v, s);
            }
        }
    }
    STAMP(0, 9);
    WAITV(0);
    STAMP(0, 10);
}

__global__ void __launch_bounds__(512, 2) k_attn(const char* qkv, char* ows) {
    __shared__ __attribute__((aligned(16))) char smem[17 * 4096];
    const int b = blockIdx.x, xcd = b & 7, idx = b >> 3;
    STAMP(1, 0);
    const int h = xcd * 2 + (idx & 1), j = idx >> 1;
    const int wave = __builtin_amdgcn_readfirstlane(threadIdx.x >> 6), lane = threadIdx.x & 63;
    const int hh = lane >> 5, t = lane & 31, blk = (lane >> 4) & 1, q = (lane >> 2) & 3, p = lane & 3;
    const int c0 = (7 * h) >> 1, odd = h & 1;
    const int br = wave >> 2, w4 = wave & 3;
    const __amdgpu_buffer_rsrc_t rq = rsrc(qkv, 2 * QKV_BYTES);
    for (int s = wave; s < 17; s += 8) {
        const int sb = s >= 12, T = sb ? 4 * j - 1 + (s - 12) : 4 * j - 8 + s;
        if (T >= 0) {
            const int base = sb * QKV_BYTES + (T * 168 + 56 + c0) * 512;
            dma16(rq, smem + s * 4096, lane * 16, base);
            dma16(rq, smem + s * 4096 + 1024, lane * 16, base + 1024);
            dma16(rq, smem + s * 4096 + 2048, lane * 16, base + 56 * 512);
            dma16(rq, smem + s * 4096 + 3072, lane * 16, base + 56 * 512 + 1024);
        }
    }
    const int T = 4 * j + w4;
    int fo[2];
#pragma unroll
    for (int s = 0; s < 2; ++s) fo[s] = (2 * s + hh) * 512 + ((((t >> 2) ^ ((c0 + 2 * s + hh) & 3)) << 2) + (t & 3)) * 16;
    u32x4 qf[2];
#pragma unroll
    for (int s = 0; s < 2; ++s) qf[s] = *(const u32x4*)(qkv + br * QKV_BYTES + (T * 168 + c0) * 512 + fo[s]);
    if (odd) { if (!hh) { qf[0].x = 0; qf[0].y = 0; } } else { if (hh) { qf[1].z = 0; qf[1].w = 0; } }
    WAITV(0);
    STAMP(1, 1);
    for (int s = wave; s < 17; s += 8) {
        if (lane < 32) *(u32x2*)(smem + s * 4096 + 2048 + (odd ? 0 : 3 * 512 + 8) + lane * 16) = (u32x2){0x00003F80u, 0u};
    }
    __syncthreads();
    STAMP(1, 2);
    const bf16x8 q0 = __builtin_bit_cast(bf16x8, qf[0]), q1 = __builtin_bit_cast(bf16x8, qf[1]);
    int vo[2];
    { const int cc = 2 * blk + (p & 1);
#pragma unroll
      for (int tt = 0; tt < 2; ++tt) vo[tt] = 2048 + cc * 512 + ((((2 * tt + hh) ^ ((c0 + cc) & 3)) << 2) + q) * 16 + (p >> 1) * 8; }
    const int sd = br ? 13 + w4 : 8 + w4;
    const int so = br ? 12 + w4 : w4;
    const bool old_ok = br ? (T >= 1) : (T >= 8);
    const int nmid = br ? 0 : (T >= 7 ? 7 : T);
    auto qk = [&](int slot, const f32x16& c) {
        const char* kb = smem + slot * 4096;
        f32x16 r = MFMA(*(const bf16x8*)(kb + fo[0]), q0, c);
        return MFMA(*(const bf16x8*)(kb + fo[1]), q1, r);
    };
    auto pv = [&](int slot, bf16x8 p0, bf16x8 p1, f32x16& o) {
        const char* vb = smem + slot * 4096;
        o = MFMA(tr_frag(vb + vo[0], vb + vo[1]), p0, o);
        o = MFMA(tr_frag(vb + vo[0] + 256, vb + vo[1] + 256), p1, o);
    };
    const f32x16 zero = {};
    const float NEG = -3.0e38f;
    f32x16 sm;
    {
        const f32x16 sdg = qk(sd, zero), sol = qk(old_ok ? so : sd, zero);
#pragma unroll
        for (int r = 0; r < 16; ++r) {
            const int key = (r & 3) + 8 * (r >> 2) + 4 * hh;
            const int dist = (t - key) & 31;
            const bool lo = key <= t;
            const bool ok = (lo || old_ok) && (!br || (!(dist & 1) && dist <= 14));
            sm[r] = ok ? (lo ? sdg[r] : sol[r]) : NEG;
        }
    }
    float m = sm[0];
#pragma unroll
    for (int r = 1; r < 16; ++r) m = fmaxf(m, sm[r]);
    m = fmaxf(m, __shfl_xor(m, 32));
    STAMP(1, 3);
    f32x16 o; float l;
    for (int pass = 0;; ++pass) {
        o = zero;
        {
            f32x16 e;
#pragma unroll
            for (int r = 0; r < 16; ++r) e[r] = __builtin_amdgcn_exp2f(sm[r] - m);
            f32x16 ed, eo;
#pragma unroll
            for (int r = 0; r < 16; ++r) { const int key = (r & 3) + 8 * (r >> 2) + 4 * hh; ed[r] = key <= t ? e[r] : 0.f; eo[r] = key <= t ? 0.f : e[r]; }
            pv(sd, pack8(ed, 0), pack8(ed, 1), o);
            if (old_ok) pv(so, pack8(eo, 0), pack8(eo, 1), o);
        }
        f32x16 cm;
#pragma unroll
        for (int r = 0; r < 16; ++r) cm[r] = -m;
        for (int i = 1; i <= nmid; ++i) {
            f32x16 s = qk(sd - i, cm);
#pragma unroll
            for (int r = 0; r < 16; ++r) s[r] = __builtin_amdgcn_exp2f(s[r]);
            pv(sd - i, pack8(s, 0), pack8(s, 1), o);
        }
        l = __shfl(odd ? o[0] : o[12], odd ? t : t + 32);
        if (pass || __all(l < 1.0e9f)) break;
        for (int i = 1; i <= nmid; ++i) {
            const f32x16 s = qk(sd - i, zero);
#pragma unroll
            for (int r = 0; r < 16; ++r) m = fmaxf(m, s[r]);
        }
        m = fmaxf(m, __shfl_xor(m, 32));
    }
    STAMP(1, 4);
    const float inv = 1.0f / l;
    o = o * inv;
    char* dst = ows + br * O_BYTES + (T * 56 + c0) * 512 + t * 16;
    const u32x4 o0 = __builtin_bit_cast(u32x4, pack8(o, 0)), o1 = __builtin_bit_cast(u32x4, pack8(o, 1));
    if (odd) {
        if (hh) *(u32x4*)(dst + 512) = o0; else *(u32x2*)(dst + 8) = (u32x2){o0.z, o0.w};
        *(u32x4*)(dst + (2 + hh) * 512) = o1;
    } else {
        *(u32x4*)(dst + hh * 512) = o0;
        if (hh) *(u32x2*)(dst + 3 * 512) = (u32x2){o1.x, o1.y}; else *(u32x4*)(dst + 2 * 512) = o1;
    }
    WAITV(0);
    STAMP(1, 5);
}

__global__ void __launch_bounds__(256, 1) k_out(const char* ows, const char* woa, const char* wob, char* out) {
    __shared__ __attribute__((aligned(16))) char smem[57344 + 4 * 4096];
    const int b = blockIdx.x, xcd = b & 7, idx = b >> 3;
    if (idx >= 28) return;
    const int br = xcd >> 2, jj = idx / 7, g = idx - jj * 7, j = (xcd & 3) * 4 + jj;
    const int wave = __builtin_amdgcn_readfirstlane(threadIdx.x >> 6), lane = threadIdx.x & 63;
    const int hh = lane >> 5, t = lane & 31, blk = (lane >> 4) & 1, q = (lane >> 2) & 3, p = lane & 3;
    STAMP(2, 0);
    const int r3 = lane >> 3, u = lane & 7;
    const __amdgpu_buffer_rsrc_t rw = rsrc(br ? wob : woa, OB);
    const int vw = r3 * 896 + g * 128 + ((u ^ (((r3 >> 1) & 1) << 2)) << 4);
#pragma unroll
    for (int i = 0; i < 14; ++i) dma16(rw, smem + (i * 4 + wave) * 1024, vw, (i * 4 + wave) * (8 * 896));
    const int T = 4 * j + wave;
    const char* src = ows + br * O_BYTES + T * (56 * 512) + lane * 16;
    bf16x8 of[28];
#pragma unroll
    for (int ks = 0; ks < 28; ++ks) of[ks] = *(const bf16x8*)(src + ks * 1024);
    WAITV(0);
    STAMP(2, 1);
    __syncthreads();
    STAMP(2, 2);
    int wo_[2];
#pragma unroll
    for (int i = 0; i < 2; ++i) wo_[i] = (8 * hh + q) * 128 + ((((i ^ (q >> 1)) << 2) + blk * 2 + (p & 1)) << 4) + (p >> 1) * 8;
    f32x16 acc[2] = {};
#pragma unroll
    for (int ks = 0; ks < 28; ++ks)
#pragma unroll
        for (int i = 0; i < 2; ++i) acc[i] = MFMA(tr_frag(smem + wo_[i] + ks * 2048, smem + wo_[i] + ks * 2048 + 512), of[ks], acc[i]);
    STAMP(2, 3);
    char* tb = smem + 57344 + wave * 4096;
#pragma unroll
    for (int i = 0; i < 2; ++i)
#pragma unroll
        for (int s = 0; s < 2; ++s) *(bf16x8*)(tb + t * 128 + (((4 * i + 2 * s + hh) ^ (t & 7)) << 4)) = pack8(acc[i], s);
    WAITL0();
#pragma unroll
    for (int i = 0; i < 4; ++i) {
        const int row = i * 8 + r3;
        const u32x4 v = *(const u32x4*)(tb + row * 128 + ((u ^ (row & 7)) << 4));
        *(u32x4*)(out + (T * 32 + row) * 1792 + br * 896 + g * 128 + u * 16) = v;
    }
    WAITV(0);
    STAMP(2, 4);
}

__global__ void k_probe(int ka, int ia, int maxa, int kb, int ib, int maxb, int mult) {
    __shared__ unsigned long long sa[256], sb[256];
    const int b = threadIdx.x;
    unsigned long long t0; asm volatile("s_memrealtime %0\n\ts_waitcnt lgkmcnt(0)" : "=s"(t0) :: "memory");
    const bool use = (b >> 3) < 28;
    unsigned long long a = g_diag[((ka * 256 + b) * 8 + 0) * 16 + ia], c = g_diag[((kb * 256 + b) * 8 + 0) * 16 + ib];
    sa[b] = a; sb[b] = c; __syncthreads();
    if (b == 0) {
        unsigned long long A = sa[0], B = sb[0];
        for (int i = 1; i < 224; ++i) { A = maxa ? (sa[i] > A ? sa[i] : A) : (sa[i] < A ? sa[i] : A); B = maxb ? (sb[i] > B ? sb[i] : B) : (sb[i] < B ? sb[i] : B); }
        unsigned long long x = (B - A) * mult; if (x > 20000) x = 20000;
        unsigned long long t; do { asm volatile("s_memrealtime %0\n\ts_waitcnt lgkmcnt(0)" : "=s"(t) :: "memory"); } while (t - t0 < x + 500);
    }
    (void)use;
}
extern "C" void kernel_launch(void* const* d_in, const int* in_sizes, int n_in, void* d_out, int out_size, void* d_ws, size_t ws_size, hipStream_t stream) {
    char* ws = (char*)d_ws;
    char* qkv = ws; char* ows = ws + 2 * QKV_BYTES;
    (void)hipFuncSetAttribute((const void*)k_qkv, hipFuncAttributeMaxDynamicSharedMemorySize, 3 * STAGE);
    k_qkv<<<256, 512, 3 * STAGE, stream>>>((const char*)d_in[0], (const char*)d_in[1], (const char*)d_in[3], (const char*)d_in[4], qkv);
    k_attn<<<256, 512, 0, stream>>>(qkv, ows);
    k_out<<<256, 256, 0, stream>>>(ows, (const char*)d_in[2], (const char*)d_in[5], (char*)d_out);
    k_probe<<<1, 256, 0, stream>>>(0,0,1,0,1,1,4);
}
```

```cpp
#define DIAG 1
#include <hip/hip_runtime.h>
#include <hip/hip_bf16.h>

typedef __bf16 bf16x8 __attribute__((ext_vector_type(8)));
typedef __bf16 bf16x4 __attribute__((ext_vector_type(4)));
typedef short s16x4 __attribute__((ext_vector_type(4)));
typedef float f32x16 __attribute__((ext_vector_type(16)));
typedef float f32x8 __attribute__((ext_vector_type(8)));
typedef unsigned u32x4 __attribute__((ext_vector_type(4)));
typedef unsigned u32x2 __attribute__((ext_vector_type(2)));
#define LAS __attribute__((address_space(3)))
#define DI __device__ __forceinline__
#define MFMA(a, b, c) __builtin_amdgcn_mfma_f32_32x32x16_bf16((a), (b), (c), 0, 0, 0)

constexpr int S = 2048, D = 448, N3 = 1344;
constexpr int XB = S * D * 2, WB = D * N3 * 2, OB = D * D * 2;
constexpr int QKV_BYTES = S * N3 * 2;
constexpr int O_BYTES = S * D * 2;
constexpr int STAGE = 40960;

DI __amdgpu_buffer_rsrc_t rsrc(const void* p, int bytes) { return __builtin_amdgcn_make_buffer_rsrc((void*)p, 0, bytes, 0x00020000); }
DI void dma16(__amdgpu_buffer_rsrc_t r, char* lds, int voff, int soff) {
    __builtin_amdgcn_raw_ptr_buffer_load_lds(r, (LAS void*)lds, 16, voff, soff, 0, 0);
}
DI bf16x8 tr_frag(const char* a0, const char* a1) {
    s16x4 lo = __builtin_amdgcn_ds_read_tr16_b64_v4i16((LAS s16x4*)a0);
    s16x4 hi = __builtin_amdgcn_ds_read_tr16_b64_v4i16((LAS s16x4*)a1);
    return __builtin_bit_cast(bf16x8, __builtin_shufflevector(lo, hi, 0, 1, 2, 3, 4, 5, 6, 7));
}
DI bf16x8 pack8(const f32x16& v, int s) {
    f32x8 t = {v[8 * s], v[8 * s + 1], v[8 * s + 2], v[8 * s + 3], v[8 * s + 4], v[8 * s + 5], v[8 * s + 6], v[8 * s + 7]};
    return __builtin_convertvector(t, bf16x8);
}
#define WAITV(n) asm volatile("s_waitcnt vmcnt(" #n ")" ::: "memory")
#define WAITL0() asm volatile("s_waitcnt lgkmcnt(0)" ::: "memory")
#define BAR() __builtin_amdgcn_s_barrier()

__device__ unsigned long long g_diag[3 * 256 * 8 * 16];
#define STAMP(K, i) do { unsigned long long t_; asm volatile("s_memrealtime %0\n\ts_waitcnt lgkmcnt(0)" : "=s"(t_) :: "memory"); if ((threadIdx.x & 63) == 0) g_diag[(((K) * 256 + blockIdx.x) * 8 + (threadIdx.x >> 6)) * 16 + (i)] = t_; } while (0)
__global__ void __launch_bounds__(512, 2) k_qkv(const char* xa, const char* wa, const char* xb, const char* wb, char* qkv) {
    extern __shared__ __attribute__((aligned(16))) char smem[];
    const int b = blockIdx.x, xcd = b & 7, idx = b >> 3;
    if (idx >= 28) return;
    STAMP(0, 0);
    const int br = xcd >> 2, jj = idx / 7, g = idx - jj * 7, j = (xcd & 3) * 4 + jj;
    const int wave = __builtin_amdgcn_readfirstlane(threadIdx.x >> 6), lane = threadIdx.x & 63;
    const int rot = (g + 2 * jj) % 7;
    if (wave >= 4) {
        const int lw = wave - 4, r3 = lane >> 3, u = lane & 7;
        const __amdgpu_buffer_rsrc_t rx = rsrc(br ? xb : xa, XB), rw = rsrc(br ? wb : wa, WB);
        const int vx0 = (j * 128 + lw * 32 + r3) * 896 + ((u ^ (r3 >> 1)) << 4);
        const int vx1 = vx0 ^ 64;
        const int vw = (lw * 16 + r3) * 2688 + g * 384 + ((u ^ (((r3 >> 1) & 1) << 2)) << 4);
        auto issue = [&](int c) {
            int ch = c + rot; if (ch >= 7) ch -= 7;
            char* st = smem + (c % 3) * STAGE;
#pragma unroll
            for (int i = 0; i < 4; ++i) dma16(rx, st + (lw * 32 + i * 8) * 128, (i & 1) ? vx1 : vx0, ch * 128 + i * 7168);
#pragma unroll
            for (int cb = 0; cb < 3; ++cb)
#pragma unroll
                for (int i = 0; i < 2; ++i) dma16(rw, st + 16384 + cb * 8192 + (lw * 16 + i * 8) * 128, vw, ch * (64 * 2688) + cb * 128 + i * (8 * 2688));
        };
        issue(0); issue(1); issue(2);
        WAITV(20); BAR();
#pragma unroll
        for (int c = 1; c < 6; ++c) { WAITV(10); BAR(); if (c + 2 < 7) issue(c + 2); }
        WAITV(0); BAR();
        return;
    }
    const int wr = wave >> 1, wc = wave & 1, hh = lane >> 5, t = lane & 31;
    const int blk = (lane >> 4) & 1, q = (lane >> 2) & 3, p = lane & 3;
    int xo[4], wo_[3];
#pragma unroll
    for (int ks = 0; ks < 4; ++ks) xo[ks] = (wr * 64 + t) * 128 + (((2 * ks + hh) ^ ((t >> 1) & 7)) << 4);
#pragma unroll
    for (int i = 0; i < 3; ++i) { const int ct = 3 * wc + i, cb = ct >> 1, half = ct & 1;
        wo_[i] = 16384 + cb * 8192 + (8 * hh + q) * 128 + ((((half ^ (q >> 1)) << 2) + blk * 2 + (p & 1)) << 4) + (p >> 1) * 8; }
    f32x16 acc[2][3] = {};
#pragma unroll 1
    for (int c = 0; c < 7; ++c) {
        BAR();
        STAMP(0, 1 + c);
        const char* st = smem + (c % 3) * STAGE;
#pragma unroll
        for (int ks = 0; ks < 4; ++ks) {
            bf16x8 xf[2], wf[3];
#pragma unroll
            for (int tt = 0; tt < 2; ++tt) xf[tt] = *(const bf16x8*)(st + xo[ks] + tt * 4096);
#pragma unroll
            for (int i = 0; i < 3; ++i) wf[i] = tr_frag(st + wo_[i] + ks * 2048, st + wo_[i] + ks * 2048 + 512);
#pragma unroll
            for (int i = 0; i < 3; ++i)
#pragma unroll
                for (int tt = 0; tt < 2; ++tt) acc[tt][i] = MFMA(wf[i], xf[tt], acc[tt][i]);
        }
        WAITL0();
    }
    STAMP(0, 8);
    char* dst = qkv + br * QKV_BYTES;
    const float qs = 0.18898223650461361f * 1.4426950408889634f;
#pragma unroll
    for (int i = 0; i < 3; ++i) {
        const int cbase = 24 * g + 4 * (3 * wc + i);
        const float sc = cbase < 56 ? qs : 1.0f;
#pragma unroll
        for (int tt = 0; tt < 2; ++tt) {
            const int T = j * 4 + wr * 2 + tt;
            f32x16 v = acc[tt][i] * sc;
#pragma unroll
            for (int s = 0; s < 2; ++s) {
                const int cc = 2 * s + hh;
                *(bf16x8*)(dst + ((T * 168 + cbase + cc) * 32 + (((t >> 2) ^ cc) << 2) + (t & 3)) * 16) = pack8(v, s);
            }
        }
    }
    STAMP(0, 9);
    WAITV(0);
    STAMP(0, 10);
}

__global__ void __launch_bounds__(512, 2) k_attn(const char* qkv, char* ows) {
    __shared__ __attribute__((aligned(16))) char smem[17 * 4096];
    const int b = blockIdx.x, xcd = b & 7, idx = b >> 3;
    STAMP(1, 0);
    const int h = xcd * 2 + (idx & 1), j = idx >> 1;
    const int wave = __builtin_amdgcn_readfirstlane(threadIdx.x >> 6), lane = threadIdx.x & 63;
    const int hh = lane >> 5, t = lane & 31, blk = (lane >> 4) & 1, q = (lane >> 2) & 3, p = lane & 3;
    const int c0 = (7 * h) >> 1, odd = h & 1;
    const int br = wave >> 2, w4 = wave & 3;
    const __amdgpu_buffer_rsrc_t rq = rsrc(qkv, 2 * QKV_BYTES);
    for (int s = wave; s < 17; s += 8) {
        const int sb = s >= 12, T = sb ? 4 * j - 1 + (s - 12) : 4 * j - 8 + s;
        if (T >= 0) {
            const int base = sb * QKV_BYTES + (T * 168 + 56 + c0) * 512;
            dma16(rq, smem + s * 4096, lane * 16, base);
            dma16(rq, smem + s * 4096 + 1024, lane * 16, base + 1024);
            dma16(rq, smem + s * 4096 + 2048, lane * 16, base + 56 * 512);
            dma16(rq, smem + s * 4096 + 3072, lane * 16, base + 56 * 512 + 1024);
        }
    }
    const int T = 4 * j + w4;
    int fo[2];
#pragma unroll
    for (int s = 0; s < 2; ++s) fo[s] = (2 * s + hh) * 512 + ((((t >> 2) ^ ((c0 + 2 * s + hh) & 3)) << 2) + (t & 3)) * 16;
    u32x4 qf[2];
#pragma unroll
    for (int s = 0; s < 2; ++s) qf[s] = *(const u32x4*)(qkv + br * QKV_BYTES + (T * 168 + c0) * 512 + fo[s]);
    if (odd) { if (!hh) { qf[0].x = 0; qf[0].y = 0; } } else { if (hh) { qf[1].z = 0; qf[1].w = 0; } }
    WAITV(0);
    STAMP(1, 1);
    for (int s = wave; s < 17; s += 8) {
        if (lane < 32) *(u32x2*)(smem + s * 4096 + 2048 + (odd ? 0 : 3 * 512 + 8) + lane * 16) = (u32x2){0x00003F80u, 0u};
    }
    __syncthreads();
    STAMP(1, 2);
    const bf16x8 q0 = __builtin_bit_cast(bf16x8, qf[0]), q1 = __builtin_bit_cast(bf16x8, qf[1]);
    int vo[2];
    { const int cc = 2 * blk + (p & 1);
#pragma unroll
      for (int tt = 0; tt < 2; ++tt) vo[tt] = 2048 + cc * 512 + ((((2 * tt + hh) ^ ((c0 + cc) & 3)) << 2) + q) * 16 + (p >> 1) * 8; }
    const int sd = br ? 13 + w4 : 8 + w4;
    const int so = br ? 12 + w4 : w4;
    const bool old_ok = br ? (T >= 1) : (T >= 8);
    const int nmid = br ? 0 : (T >= 7 ? 7 : T);
    auto qk = [&](int slot, const f32x16& c) {
        const char* kb = smem + slot * 4096;
        f32x16 r = MFMA(*(const bf16x8*)(kb + fo[0]), q0, c);
        return MFMA(*(const bf16x8*)(kb + fo[1]), q1, r);
    };
    auto pv = [&](int slot, bf16x8 p0, bf16x8 p1, f32x16& o) {
        const char* vb = smem + slot * 4096;
        o = MFMA(tr_frag(vb + vo[0], vb + vo[1]), p0, o);
        o = MFMA(tr_frag(vb + vo[0] + 256, vb + vo[1] + 256), p1, o);
    };
    const f32x16 zero = {};
    const float NEG = -3.0e38f;
    f32x16 sm;
    {
        const f32x16 sdg = qk(sd, zero), sol = qk(old_ok ? so : sd, zero);
#pragma unroll
        for (int r = 0; r < 16; ++r) {
            const int key = (r & 3) + 8 * (r >> 2) + 4 * hh;
            const int dist = (t - key) & 31;
            const bool lo = key <= t;
            const bool ok = (lo || old_ok) && (!br || (!(dist & 1) && dist <= 14));
            sm[r] = ok ? (lo ? sdg[r] : sol[r]) : NEG;
        }
    }
    float m = sm[0];
#pragma unroll
    for (int r = 1; r < 16; ++r) m = fmaxf(m, sm[r]);
    m = fmaxf(m, __shfl_xor(m, 32));
    STAMP(1, 3);
    f32x16 o; float l;
    for (int pass = 0;; ++pass) {
        o = zero;
        {
            f32x16 e;
#pragma unroll
            for (int r = 0; r < 16; ++r) e[r] = __builtin_amdgcn_exp2f(sm[r] - m);
            f32x16 ed, eo;
#pragma unroll
            for (int r = 0; r < 16; ++r) { const int key = (r & 3) + 8 * (r >> 2) + 4 * hh; ed[r] = key <= t ? e[r] : 0.f; eo[r] = key <= t ? 0.f : e[r]; }
            pv(sd, pack8(ed, 0), pack8(ed, 1), o);
            if (old_ok) pv(so, pack8(eo, 0), pack8(eo, 1), o);
        }
        f32x16 cm;
#pragma unroll
        for (int r = 0; r < 16; ++r) cm[r] = -m;
        for (int i = 1; i <= nmid; ++i) {
            f32x16 s = qk(sd - i, cm);
#pragma unroll
            for (int r = 0; r < 16; ++r) s[r] = __builtin_amdgcn_exp2f(s[r]);
            pv(sd - i, pack8(s, 0), pack8(s, 1), o);
        }
        l = __shfl(odd ? o[0] : o[12], odd ? t : t + 32);
        if (pass || __all(l < 1.0e9f)) break;
        for (int i = 1; i <= nmid; ++i) {
            const f32x16 s = qk(sd - i, zero);
#pragma unroll
            for (int r = 0; r < 16; ++r) m = fmaxf(m, s[r]);
        }
        m = fmaxf(m, __shfl_xor(m, 32));
    }
    STAMP(1, 4);
    const float inv = 1.0f / l;
    o = o * inv;
    char* dst = ows + br * O_BYTES + (T * 56 + c0) * 512 + t * 16;
    const u32x4 o0 = __builtin_bit_cast(u32x4, pack8(o, 0)), o1 = __builtin_bit_cast(u32x4, pack8(o, 1));
    if (odd) {
        if (hh) *(u32x4*)(dst + 512) = o0; else *(u32x2*)(dst + 8) = (u32x2){o0.z, o0.w};
        *(u32x4*)(dst + (2 + hh) * 512) = o1;
    } else {
        *(u32x4*)(dst + hh * 512) = o0;
        if (hh) *(u32x2*)(dst + 3 * 512) = (u32x2){o1.x, o1.y}; else *(u32x4*)(dst + 2 * 512) = o1;
    }
    WAITV(0);
    STAMP(1, 5);
}

__global__ void __launch_bounds__(256, 1) k_out(const char* ows, const char* woa, const char* wob, char* out) {
    __shared__ __attribute__((aligned(16))) char smem[57344 + 4 * 4096];
    const int b = blockIdx.x, xcd = b & 7, idx = b >> 3;
    if (idx >= 28) return;
    const int br = xcd >> 2, jj = idx / 7, g = idx - jj * 7, j = (xcd & 3) * 4 + jj;
    const int wave = __builtin_amdgcn_readfirstlane(threadIdx.x >> 6), lane = threadIdx.x & 63;
    const int hh = lane >> 5, t = lane & 31, blk = (lane >> 4) & 1, q = (lane >> 2) & 3, p = lane & 3;
    STAMP(2, 0);
    const int r3 = lane >> 3, u = lane & 7;
    const __amdgpu_buffer_rsrc_t rw = rsrc(br ? wob : woa, OB);
    const int vw = r3 * 896 + g * 128 + ((u ^ (((r3 >> 1) & 1) << 2)) << 4);
#pragma unroll
    for (int i = 0; i < 14; ++i) dma16(rw, smem + (i * 4 + wave) * 1024, vw, (i * 4 + wave) * (8 * 896));
    const int T = 4 * j + wave;
    const char* src = ows + br * O_BYTES + T * (56 * 512) + lane * 16;
    bf16x8 of[28];
#pragma unroll
    for (int ks = 0; ks < 28; ++ks) of[ks] = *(const bf16x8*)(src + ks * 1024);
    WAITV(0);
    STAMP(2, 1);
    __syncthreads();
    STAMP(2, 2);
    int wo_[2];
#pragma unroll
    for (int i = 0; i < 2; ++i) wo_[i] = (8 * hh + q) * 128 + ((((i ^ (q >> 1)) << 2) + blk * 2 + (p & 1)) << 4) + (p >> 1) * 8;
    f32x16 acc[2] = {};
#pragma unroll
    for (int ks = 0; ks < 28; ++ks)
#pragma unroll
        for (int i = 0; i < 2; ++i) acc[i] = MFMA(tr_frag(smem + wo_[i] + ks * 2048, smem + wo_[i] + ks * 2048 + 512), of[ks], acc[i]);
    STAMP(2, 3);
    char* tb = smem + 57344 + wave * 4096;
#pragma unroll
    for (int i = 0; i < 2; ++i)
#pragma unroll
        for (int s = 0; s < 2; ++s) *(bf16x8*)(tb + t * 128 + (((4 * i + 2 * s + hh) ^ (t & 7)) << 4)) = pack8(acc[i], s);
    WAITL0();
#pragma unroll
    for (int i = 0; i < 4; ++i) {
        const int row = i * 8 + r3;
        const u32x4 v = *(const u32x4*)(tb + row * 128 + ((u ^ (row & 7)) << 4));
        *(u32x4*)(out + (T * 32 + row) * 1792 + br * 896 + g * 128 + u * 16) = v;
    }
    WAITV(0);
    STAMP(2, 4);
}

__global__ void k_first() { STAMP(2, 15); }
__global__ void k_probe(int ka, int ia, int maxa) {
    __shared__ unsigned long long sa[256];
    const int b = threadIdx.x;
    unsigned long long a = g_diag[((ka * 256 + (b < 224 ? b : 0)) * 8 + 0) * 16 + ia];
    sa[b] = a; __syncthreads();
    for (int o = 128; o > 0; o >>= 1) { if (b < o) { unsigned long long x = sa[b], y = sa[b + o]; sa[b] = maxa ? (x > y ? x : y) : (x < y ? x : y); } __syncthreads(); }
    if (b == 0) {
        const unsigned long long A = sa[0];
        unsigned long long t; int n = 0; do { asm volatile("s_memrealtime %0\n\ts_waitcnt lgkmcnt(0)" : "=s"(t) :: "memory"); } while (t - A < 6000 && ++n < 100000);
    }
}
extern "C" void kernel_launch(void* const* d_in, const int* in_sizes, int n_in, void* d_out, int out_size, void* d_ws, size_t ws_size, hipStream_t stream) {
    char* ws = (char*)d_ws;
    char* qkv = ws; char* ows = ws + 2 * QKV_BYTES;
    (void)hipFuncSetAttribute((const void*)k_qkv, hipFuncAttributeMaxDynamicSharedMemorySize, 3 * STAGE);
    k_first<<<256, 64, 0, stream>>>();
    k_qkv<<<256, 512, 3 * STAGE, stream>>>((const char*)d_in[0], (const char*)d_in[1], (const char*)d_in[3], (const char*)d_in[4], qkv);
    k_attn<<<256, 512, 0, stream>>>(qkv, ows);
    k_out<<<256, 256, 0, stream>>>(ows, (const char*)d_in[2], (const char*)d_in[5], (char*)d_out);
    k_probe<<<1, 256, 0, stream>>>(2,15,0);
}
```

```cpp
#include <hip/hip_runtime.h>
#include <hip/hip_bf16.h>

typedef __bf16 bf16x8 __attribute__((ext_vector_type(8)));
typedef short s16x4 __attribute__((ext_vector_type(4)));
typedef float f32x16 __attribute__((ext_vector_type(16)));
typedef float f32x8 __attribute__((ext_vector_type(8)));
typedef float f32x4 __attribute__((ext_vector_type(4)));
typedef unsigned u32x4 __attribute__((ext_vector_type(4)));
typedef unsigned u32x2 __attribute__((ext_vector_type(2)));
typedef unsigned long long u64;
typedef __attribute__((address_space(1))) u64 gu64;
#define LAS __attribute__((address_space(3)))
#define DI __device__ __forceinline__
#define MFMA(a, b, c) __builtin_amdgcn_mfma_f32_32x32x16_bf16((a), (b), (c), 0, 0, 0)
#define RLX __ATOMIC_RELAXED, __HIP_MEMORY_SCOPE_AGENT

constexpr int XB = 2048 * 896, WB = 448 * 2688, OB = 448 * 896;
constexpr int STAGE = 40960, RING = 40960, WO_OFF = 105472, LDS_BYTES = 163840;
constexpr int WS_F1 = 4096, WS_F2 = 65536, WS_X = 1 << 20, X_BYTES = 2 * 16 * 64 * 6144, WS_O = 16 << 20, O_BYTES = 2 * 64 * 16 * 2048;
constexpr int OOB = 0x40000000;
constexpr int CODE_LINES = 126;
constexpr int SPIN_MAX = 1 << 18;

#define STAMP(i)
#define CST(i)
#define STAMP_DECL
#define STAMP_FLUSH()

DI u32x4 srd(const void* p, unsigned bytes) { const u64 a = (u64)p; return (u32x4){(unsigned)a, (unsigned)(a >> 32) & 0xffffu, bytes, 0x00020000u}; }
DI __amdgpu_buffer_rsrc_t rsrc(const void* p, int bytes) { return __builtin_amdgcn_make_buffer_rsrc((void*)p, 0, bytes, 0x00020000); }
DI void dma16(u32x4 r, int lds, int voff, int soff) {
    asm volatile("s_mov_b32 m0, %0\n\ts_nop 0\n\tbuffer_load_dwordx4 %1, %2, %3 offen lds" :: "s"(lds), "v"(voff), "s"(r), "s"(soff) : "memory");
}
DI void dma16_dev(u32x4 r, int lds, int voff, int soff) {
    asm volatile("s_mov_b32 m0, %0\n\ts_nop 0\n\tbuffer_load_dwordx4 %1, %2, %3 offen sc1 lds" :: "s"(lds), "v"(voff), "s"(r), "s"(soff) : "memory");
}
DI void dma64_dev(u32x4 r, int lds, int voff, int soff) {
    asm volatile("s_mov_b32 m0, %0\n\ts_nop 0\n\tbuffer_load_dwordx4 %1, %2, %3 offen sc1 lds\n\tbuffer_load_dwordx4 %1, %2, %3 offen offset:1024 sc1 lds\n\t"
                 "buffer_load_dwordx4 %1, %2, %3 offen offset:2048 sc1 lds\n\tbuffer_load_dwordx4 %1, %2, %3 offen offset:3072 sc1 lds" :: "s"(lds), "v"(voff), "s"(r), "s"(soff) : "memory");
}
DI bf16x8 tr_frag(const char* a0, const char* a1) {
    s16x4 lo = __builtin_amdgcn_ds_read_tr16_b64_v4i16((LAS s16x4*)a0);
    s16x4 hi = __builtin_amdgcn_ds_read_tr16_b64_v4i16((LAS s16x4*)a1);
    return __builtin_bit_cast(bf16x8, __builtin_shufflevector(lo, hi, 0, 1, 2, 3, 4, 5, 6, 7));
}
DI bf16x8 pack8(const f32x16& v, int s) {
    f32x8 t = {v[8 * s], v[8 * s + 1], v[8 * s + 2], v[8 * s + 3], v[8 * s + 4], v[8 * s + 5], v[8 * s + 6], v[8 * s + 7]};
    return __builtin_convertvector(t, bf16x8);
}
DI float xhalf(float v) {
    const unsigned u = __builtin_bit_cast(unsigned, v);
    const auto r = __builtin_amdgcn_permlane32_swap(u, u, false, false);
    return __builtin_bit_cast(float, (threadIdx.x & 32) ? r[0] : r[1]);
}
#define WAITV(n) asm volatile("s_waitcnt vmcnt(" #n ")" ::: "memory")
#define WAITL0() asm volatile("s_waitcnt lgkmcnt(0)" ::: "memory")
#define BAR() do { asm volatile("" ::: "memory"); __builtin_amdgcn_s_barrier(); asm volatile("" ::: "memory"); } while (0)
#define SCHED() __builtin_amdgcn_sched_barrier(0)

#define SG(m, n) __builtin_amdgcn_sched_group_barrier(m, n, 0)
DI float fmx(float a, float b) { return __builtin_elementwise_maximum(a, b); }
typedef int i32x4 __attribute__((ext_vector_type(4)));
#define PIN(x) asm volatile("" : "+v"(x))
DI void poll(const char* p, bool active, u64 tag) {
#pragma unroll 1
    for (int n = 0; n < SPIN_MAX; ++n) {
        u64 v = tag;
        if (active) v = __hip_atomic_load((gu64*)p, RLX);
        if (__all(v == tag)) break;
        __builtin_amdgcn_s_sleep(1);
    }
    asm volatile("" ::: "memory");
}

extern "C" __global__ void __launch_bounds__(512, 2)
fused(const char* xa, const char* wa, const char* woa, const char* xb, const char* wb, const char* wob, char* out, char* ws) {
    __shared__ __attribute__((aligned(16))) char smem[LDS_BYTES];
    STAMP_DECL;
    STAMP(0);
    const int b = blockIdx.x, xcd = b & 7, idx = b >> 3;
    const int br = xcd & 1, jj = idx >> 3, ty = idx & 7, j = (xcd >> 1) * 4 + jj;
    const int wave = __builtin_amdgcn_readfirstlane(threadIdx.x >> 6), lane = threadIdx.x & 63;
    const int r3 = lane >> 3, u = lane & 7;
    const int lbase = (int)(unsigned)(size_t)(LAS char*)smem;
    u64 epoch;
    asm volatile("global_load_dwordx2 %0, %1, %2 sc1" : "=v"(epoch) : "v"(0), "s"(ws) : "memory");
    const int w4 = wave & 3, hs = wave >> 2;
    const int hh = lane >> 5, t = lane & 31, blk = (lane >> 4) & 1, q = (lane >> 2) & 3, p = lane & 3;
    const int ha = (0x4ECA7530u >> (4 * ty)) & 15, hb = (0xBFD98621u >> (4 * ty)) & 15;
    const int rec = ((br * 16) * 64 + 4 * j) * 6144;
    const __amdgpu_buffer_rsrc_t rxs = rsrc(ws + WS_X, X_BYTES);

    if (ty < 7) {
        const u32x4 rx = srd(br ? xb : xa, XB), rw = srd(br ? wb : wa, WB);
        const int vx0 = r3 * 896 + ((u ^ (r3 >> 1)) << 4), vx1 = vx0 ^ 64;
        const int vw = r3 * 2688 + ((u ^ (((r3 >> 1) & 1) << 2)) << 4);
        int rot = ty + 2 * jj; if (rot >= 7) rot -= 7;
        const int sx = j * (128 * 896), sw = ty * 128;
        {
            const int st = lbase + RING + 2 * STAGE;
            dma16(rx, st + wave * 2048, vx0, sx + wave * 14336 + rot * 128);
            dma16(rx, st + wave * 2048 + 1024, vx1, sx + wave * 14336 + 7168 + rot * 128);
#pragma unroll
            for (int cb = 0; cb < 3; ++cb) dma16(rw, st + 16384 + cb * 8192 + wave * 1024, vw, sw + (rot * 64 + wave * 8) * 2688 + cb * 896);
        }
        const int wr = w4 >> 1, wc = w4 & 1;
        const int sp0 = (int)(0x04152630415263ull >> (4 * (2 * ty + wc))) & 15, sp1 = (int)(0x04152630415263ull >> (4 * (2 * ty + (wc ^ 1)))) & 15;
        char* tab = smem + w4 * 7168 + lane * 16;
        if (__builtin_expect(wave >= 4, 1)) {
            int ch = rot, sg = 2;
#pragma unroll 1
            for (int c = 1; c < 7; ++c) {
                ++ch; if (ch == 7) ch = 0;
                ++sg; if (sg == 3) sg = 0;
                const int st = lbase + RING + sg * STAGE;
#pragma unroll
                for (int i = 0; i < 4; ++i) dma16(rx, st + w4 * 4096 + i * 1024, (i & 1) ? vx1 : vx0, sx + (w4 * 4 + i) * 7168 + ch * 128);
#pragma unroll
                for (int cb = 0; cb < 3; ++cb)
#pragma unroll
                    for (int i = 0; i < 2; ++i) dma16(rw, st + 16384 + cb * 8192 + w4 * 2048 + i * 1024, vw, sw + (ch * 64 + w4 * 16 + i * 8) * 2688 + cb * 896);
                WAITV(10); BAR();
            }
            int g[3][2][2], l[3][2], g2[3][2], l2[3];
#pragma unroll
            for (int i = 0; i < 3; ++i) {
                const int sec = (3 * wc + i) >> 1, c0 = 8 * ty + 4 * ((wc + i) & 1), sp = (i & 1) ? sp1 : sp0;
                const int go = rec + wr * 12288 + (sec == 0 ? 4096 : (sec - 1) * 2048);
                const int lo = wr * 8192 + (sec == 0 ? 0 : 30720 + sec * 2048);
#pragma unroll
                for (int s = 0; s < 3; ++s) {
                    const int c = c0 + (s < 2 ? 2 * s + hh : sp), m = (c * 37) >> 8, r = c - 7 * m;
                    const int hd = 2 * m + (s < 2 ? (r > 3) : 1), slot = s < 2 ? (r > 3 ? r - 3 : r) : 0;
                    const int pos = slot * 512 + ((((t >> 2) ^ slot) << 2) + (t & 3)) * 16;
                    const bool on = s < 2 || hh == (sp & 1), own = hd == ha || hd == hb;
                    const int la = ((own && on) ? (hd == hb ? 49152 : 0) : 65536) + lo + pos;
                    if (s < 2) l[i][s] = la; else l2[i] = la;
#pragma unroll
                    for (int tt = 0; tt < 2; ++tt) {
                        const bool pub = sec != 0 && (br == 0 || (wr == 1 && tt == 1));
                        const int ga = (on && (pub || !own)) ? go + tt * 6144 + hd * (64 * 6144) + pos : OOB;
                        if (s < 2) g[i][tt][s] = ga; else g2[i][tt] = ga;
                    }
                }
                *(i32x4*)(tab + i * 2048) = (i32x4){g[i][0][0], g[i][0][1], g[i][1][0], g[i][1][1]};
                *(i32x4*)(tab + i * 2048 + 1024) = (i32x4){l[i][0], l[i][1], g2[i][0], g2[i][1]};
            }
            *(i32x4*)(tab + 6144) = (i32x4){l2[0], l2[1], l2[2], 0};
            asm volatile("s_waitcnt vmcnt(0) lgkmcnt(0)" ::: "memory"); BAR();
            BAR();
            const u32x4 rwo = srd(br ? wob : woa, OB);
            const int vwo = r3 * 896 + ((u ^ (((r3 >> 1) & 1) << 2)) << 4);
#pragma unroll 1
            for (int n = 12 + w4; n < 57; n += 4) dma16(rwo, lbase + WO_OFF + n * 1024, vwo, n * 7168 + ty * 128);
            BAR();
#pragma unroll 1
            for (int n = w4; n < 12; n += 4) dma16(rwo, lbase + WO_OFF + n * 1024, vwo, n * 7168 + ty * 128);
        } else {
            int xo[4], wo_[3];
#pragma unroll
            for (int ks = 0; ks < 4; ++ks) xo[ks] = (wr * 64 + t) * 128 + (((2 * ks + hh) ^ ((t >> 1) & 7)) << 4);
#pragma unroll
            for (int i = 0; i < 3; ++i) { const int ct = 3 * wc + i, cb = ct >> 1, half = ct & 1;
                wo_[i] = 16384 + cb * 8192 + (8 * hh + q) * 128 + ((((half ^ (q >> 1)) << 2) + blk * 2 + (p & 1)) << 4) + (p >> 1) * 8; }
            f32x16 acc[2][3] = {};
#pragma unroll
            for (int i = 0; i < 3; ++i) { PIN(acc[0][i]); PIN(acc[1][i]); }
            bf16x8 xf[2][2], wf[2][3];
            WAITV(0); BAR();
            STAMP(1); CST(20);
            int sg = 2;
            const char* st = smem + RING + 2 * STAGE;
            auto rd = [&](int f, int ks) {
#pragma unroll
                for (int i = 2; i >= 0; --i) {
                    wf[f][i] = tr_frag(st + wo_[i] + ks * 2048, st + wo_[i] + ks * 2048 + 512);
                    if (i == 2) { xf[f][0] = *(const bf16x8*)(st + xo[ks]); xf[f][1] = *(const bf16x8*)(st + xo[ks] + 4096); }
                }
            };
            auto mm = [&](int f) {
#pragma unroll
                for (int i = 2; i >= 0; --i)
#pragma unroll
                    for (int tt = 0; tt < 2; ++tt) acc[tt][i] = MFMA(wf[f][i], xf[f][tt], acc[tt][i]);
            };
#define MIX() do { SG(0x8, 1); SG(0x100, 2); SG(0x8, 1); SG(0x100, 2); SG(0x8, 1); SG(0x100, 2); SG(0x8, 1); SG(0x100, 2); SG(0x8, 2); SCHED(); } while (0)
            rd(0, 0);
            SCHED();
#pragma unroll 1
            for (int c = 0; c < 6; ++c) {
                rd(1, 1); mm(0); MIX();
                rd(0, 2); mm(1); MIX();
                rd(1, 3); mm(0); MIX();
                WAITL0(); BAR();
                ++sg; if (sg == 3) sg = 0;
                st = smem + RING + sg * STAGE;
                SCHED();
                rd(0, 0); mm(1); MIX();
            }
            i32x4 tb[7];
            rd(1, 1); mm(0); MIX();
            rd(0, 2); mm(1); MIX();
            rd(1, 3);
#pragma unroll
            for (int i = 0; i < 7; ++i) tb[i] = *(const i32x4*)(tab + i * 1024);
            mm(0);
            SG(0x8, 1); SG(0x100, 3); SG(0x8, 1); SG(0x100, 3); SG(0x8, 1); SG(0x100, 3); SG(0x8, 1); SG(0x100, 3); SG(0x8, 1); SG(0x100, 3); SG(0x8, 1); SCHED();
            WAITL0(); BAR();
            SCHED();
            mm(1);
            SCHED();
            STAMP(2); CST(21);
            const float qs = 0.18898223650461361f * 1.4426950408889634f;
#pragma unroll
            for (int i = 2; i >= 0; --i) {
                const int sp = (i & 1) ? sp1 : sp0;
                const unsigned padv = (wc && i) ? 0x00003F80u : 0u;
                if (i < 2 && wc == 0) { acc[0][i] *= qs; acc[1][i] *= qs; }
                u32x4 d[2][2];
#pragma unroll
                for (int tt = 0; tt < 2; ++tt)
#pragma unroll
                    for (int s = 0; s < 2; ++s) d[tt][s] = __builtin_bit_cast(u32x4, pack8(acc[tt][i], s));
                auto fix = [&](int s) {
                    const bool sh = hh == (sp & 1);
#pragma unroll
                    for (int tt = 0; tt < 2; ++tt) {
                        const u32x4 d2 = {padv, 0u, d[tt][s].z, d[tt][s].w};
                        __builtin_amdgcn_raw_buffer_store_b128(d2, rxs, tb[2 * i + 1][2 + tt], 0, 16);
                        *(u32x4*)(smem + tb[6][i] + tt * 4096) = d2;
                        d[tt][s].z = sh ? padv : d[tt][s].z; d[tt][s].w = sh ? 0u : d[tt][s].w;
                    }
                };
                if (sp < 2) fix(0); else if (sp < 4) fix(1);
#pragma unroll
                for (int tt = 0; tt < 2; ++tt)
#pragma unroll
                    for (int s = 0; s < 2; ++s) __builtin_amdgcn_raw_buffer_store_b128(d[tt][s], rxs, tb[2 * i][2 * tt + s], 0, 16);
#pragma unroll
                for (int tt = 0; tt < 2; ++tt)
#pragma unroll
                    for (int s = 0; s < 2; ++s) *(u32x4*)(smem + tb[2 * i + 1][s] + tt * 4096) = d[tt][s];
            }
            WAITL0();
            STAMP(3);
            BAR();
        }
    } else {
        u64 pc; unsigned pco;
        asm volatile("s_getpc_b64 %0\n.Lhere%=:\n\ts_mov_b32 %1, .Lhere%=-fused" : "=s"(pc), "=s"(pco));
        unsigned d0 = 0, d1 = 0, d2 = 0;
        if ((int)threadIdx.x < CODE_LINES) asm volatile("global_load_dword %0, %1, %2" : "=v"(d0) : "v"(threadIdx.x * 128), "s"(pc - pco) : "memory");
        const char* wo = br ? wob : woa;
        asm volatile("global_load_dword %0, %1, %2" : "=v"(d1) : "v"((jj * 784 + threadIdx.x) * 128), "s"(wo) : "memory");
        if (threadIdx.x < 272) asm volatile("global_load_dword %0, %1, %2" : "=v"(d2) : "v"((jj * 784 + 512 + threadIdx.x) * 128), "s"(wo) : "memory");
        asm volatile("s_waitcnt vmcnt(0)" : "+v"(d0), "+v"(d1), "+v"(d2) :: "memory");
    }
    STAMP(4);
    asm volatile("s_waitcnt vmcnt(0)" : "+v"(epoch) :: "memory");
    const u64 tag = epoch * 0x9E3779B97F4A7C15ull + 0x7F4A7C159E3779B9ull;
    if (wave < 4 && ty < 7 && lane == 0) __hip_atomic_store((gu64*)(ws + WS_F1 + (((br * 16 + j) * 8 + ty) * 16 + wave) * 8), tag, RLX);
    STAMP(5);
    SCHED();

    const int h = hs ? hb : ha;
    const int T = 4 * j + w4;
    const unsigned NEGB = 0xF149F2CAu;
    const int nb = ty == 7 ? 15 : (0xF63F30Fu >> (4 * ty)) & 15;
    const bool local = ty < 7 && (hs == 0 || nb == 15);
    int fo[2], vo[2];
#pragma unroll
    for (int s = 0; s < 2; ++s) fo[s] = hs * 49152 + (2 * s + hh) * 512 + ((((t >> 2) ^ (2 * s + hh)) << 2) + (t & 3)) * 16;
    { const int pc8 = (4 * blk + 2 * (p & 1) + (p >> 1) + (h & 1)) & 7, cc = pc8 >> 1;
#pragma unroll
      for (int tt = 0; tt < 2; ++tt) { vo[tt] = hs * 49152 + 2048 + cc * 512 + ((((2 * tt + hh) ^ cc) << 2) + q) * 16 + (pc8 & 1) * 8; PIN(vo[tt]); } }
    u32x4 qf[2];
    qf[0] = *(const u32x4*)(smem + w4 * 4096 + fo[0]); qf[1] = *(const u32x4*)(smem + w4 * 4096 + fo[1]);
    f32x16 sdg, co;
    u32x4 pm[2];
    {
        const unsigned lo = (2u << t) - 1u;
        const unsigned pat = br ? __builtin_rotateleft32(0x55540001u, t) : ~0u;
        const int nd = (int)(~(pat & lo) >> (4 * hh)), no = (int)(~(pat & ~lo) >> (4 * hh)), lm = (int)(lo >> (4 * hh));
#pragma unroll
        for (int r = 0; r < 16; ++r) {
            const int bit = (r & 3) + 8 * (r >> 2);
            sdg[r] = __builtin_bit_cast(float, __builtin_amdgcn_sbfe(nd, bit, 1) & NEGB);
            co[r] = __builtin_bit_cast(float, __builtin_amdgcn_sbfe(no, bit, 1) & NEGB);
            if (r & 1) pm[r >> 3][(r >> 1) & 3] = __builtin_amdgcn_perm(__builtin_amdgcn_sbfe(lm, bit, 1), __builtin_amdgcn_sbfe(lm, bit - 1, 1), 0x05040100u);
        }
        PIN(sdg); PIN(co); PIN(pm[0]); PIN(pm[1]);
    }
    auto kfrag = [&](int slot, int s) { return *(const bf16x8*)(smem + slot * 4096 + fo[s]); };
    auto vfrag = [&](int slot, int s) { const char* vb = smem + slot * 4096 + s * 256; return tr_frag(vb + vo[0], vb + vo[1]); };
    auto qk = [&](int slot, const f32x16& c) {
        f32x16 r = MFMA(kfrag(slot, 0), __builtin_bit_cast(bf16x8, qf[0]), c);
        return MFMA(kfrag(slot, 1), __builtin_bit_cast(bf16x8, qf[1]), r);
    };
    auto pv = [&](int slot, bf16x8 p0, bf16x8 p1, f32x16& o) { o = MFMA(vfrag(slot, 0), p0, o); o = MFMA(vfrag(slot, 1), p1, o); };
    f32x16 cm = {};
    auto pre = [&]() {
        sdg = qk(8 + w4, sdg);
        float m = fmx(sdg[0], sdg[1]);
#pragma unroll
        for (int r = 2; r < 16; r += 2) m = fmx(fmx(m, sdg[r]), sdg[r + 1]);
        m = -fmx(m, xhalf(m));
#pragma unroll
        for (int r = 0; r < 16; ++r) cm[r] = m;
        sdg += cm; co += cm;
        PIN(cm); PIN(sdg); PIN(co);
    };
    const int sl = (ty == 2 || ty == 5) ? 3 : 0;
    const int qpl = 98304 + (ty == 7 ? wave * 2048 : w4 * 512);
    const u32x4 rxx = srd(ws + WS_X, X_BYTES);
    const bool ok = j > hs;
    int vv = ok ? lane * 16 : OOB;
    int ra = ok ? rec + (w4 - 4 - 4 * hs + ha * 64) * 6144 : 0, rb = ok ? rec + (w4 - 4 - 4 * hs + hb * 64) * 6144 : 0, lb = lbase + (4 - 4 * hs + w4) * 4096;
    int rh = rec + (h * 64 + w4) * 6144, lh = lbase + hs * 49152 + w4 * 4096 + 32768;
    asm volatile("" : "+v"(vv), "+s"(ra), "+s"(rb), "+s"(lb), "+s"(rh), "+s"(lh));
#pragma unroll 1
    for (int ph = 0;; ++ph) {
        if (ph == (local ? 0 : 1)) {
            if (ph) {
#pragma unroll
                for (int s = 0; s < 2; ++s) if (ty == 7 || 2 * s + hh == sl) qf[s] = *(const u32x4*)(smem + qpl + (fo[s] & (ty == 7 ? 2047 : 511)));
            }
            pre();
        }
        if (ph) break;
        WAITL0();
        if (wave == 4) {
            const int bk = lane >> 4;
            const int pg = ty == 7 ? (0x5421 >> (4 * q)) & 15 : (q == 0 ? ty : (q == 1 ? nb : 15));
            const bool act = bk < (br ? 2 : 3) && j - bk >= 0 && pg != 15 && !(bk == 0 && pg == ty);
            poll(ws + WS_F1 + (((br * 16 + j - bk) * 8 + pg) * 16 + p) * 8, act, tag);
        }
        STAMP(6);
        BAR();
#pragma unroll 1
        for (int g = 0; g < 2; ++g) {
            if (!local && g == hs) {
                if (ty == 7) {
                    dma16_dev(rxx, lbase + qpl, lane * 16, rh + 4096);
                    dma16_dev(rxx, lbase + qpl + 1024, lane * 16, rh + 5120);
                    dma64_dev(rxx, lh, lane * 16, rh);
                } else if (lane < 32) {
                    dma16_dev(rxx, lbase + qpl, lane * 16, rh + 4096 + sl * 512);
                    dma16_dev(rxx, lh + sl * 512, lane * 16, rh + sl * 512);
                    dma16_dev(rxx, lh + 2048 + sl * 512, lane * 16, rh + 2048 + sl * 512);
                }
            }
            if (g == 0 && (br == 0 || wave == 3)) {
                dma64_dev(rxx, lb, vv, ra);
                dma64_dev(rxx, lb + 49152, vv, rb);
                WAITV(4);
            } else {
                WAITV(0);
            }
            STAMP(7);
            BAR();
            if (!hs) break;
        }
        STAMP(8);
        if (wave < 4) __builtin_amdgcn_s_setprio(1);
    }
    const int so = br ? 7 + w4 : w4;
    STAMP(10); CST(19);
    const bf16x8 q0 = __builtin_bit_cast(bf16x8, qf[0]), q1 = __builtin_bit_cast(bf16x8, qf[1]);
    auto ex = [&](f32x16& s) {
#pragma unroll
        for (int r = 0; r < 16; ++r) s[r] = __builtin_amdgcn_exp2f(s[r]);
    };
    f32x16 o;
    {
        const f32x16 zero = {};
        bf16x8 k0 = kfrag(w4 + 1, 0), k1 = kfrag(w4 + 1, 1);
        f32x16 sa = qk(so, co), sb;
        bf16x8 v0 = vfrag(8 + w4, 0), v1 = vfrag(8 + w4, 1), v2 = vfrag(so, 0), v3 = vfrag(so, 1);
        SCHED();
        sb = MFMA(k0, q0, cm); sb = MFMA(k1, q1, sb);
        k0 = kfrag(w4 + 2, 0); k1 = kfrag(w4 + 2, 1);
#pragma unroll
        for (int r = 0; r < 16; ++r) sa[r] = fmx(sa[r], sdg[r]);
        ex(sa);
        const u32x4 e0 = __builtin_bit_cast(u32x4, pack8(sa, 0)), e1 = __builtin_bit_cast(u32x4, pack8(sa, 1));
        o = MFMA(v0, __builtin_bit_cast(bf16x8, e0 & pm[0]), zero);
        o = MFMA(v2, __builtin_bit_cast(bf16x8, e0 & ~pm[0]), o);
        bf16x8 p0 = __builtin_bit_cast(bf16x8, e1 & pm[1]), p1 = __builtin_bit_cast(bf16x8, e1 & ~pm[1]);
        v0 = v1; v1 = v3;
        SCHED();
        CST(23);
        if (!hs) { WAITV(0); BAR(); }
        if (br == 0) {
#define STG(n, cur, nxt) do { \
                if (n < 7) { nxt = MFMA(k0, q0, cm); nxt = MFMA(k1, q1, nxt); } \
                o = MFMA(v0, p0, o); o = MFMA(v1, p1, o); \
                if (n < 6) { k0 = kfrag(w4 + n + 2, 0); k1 = kfrag(w4 + n + 2, 1); } \
                v0 = vfrag(w4 + n, 0); v1 = vfrag(w4 + n, 1); \
                ex(cur); p0 = pack8(cur, 0); p1 = pack8(cur, 1); \
                if (n < 7) { SG(0x8, 1); SG(0x400, 4); SG(0x8, 1); if (n < 6) SG(0x100, 2); SG(0x400, 4); } \
                SG(0x8, 1); SG(0x100, 2); SG(0x400, 4); SG(0x8, 1); SG(0x100, 2); SG(0x400, n < 7 ? 4 : 12); SG(0x2, 8); SCHED(); } while (0)
            STG(1, sb, sa); STG(2, sa, sb); STG(3, sb, sa); STG(4, sa, sb); STG(5, sb, sa); STG(6, sa, sb); STG(7, sb, sa);
        }
        o = MFMA(v0, p0, o); o = MFMA(v1, p1, o);
    }
    float l = xhalf(o[12]);
    l = hh ? o[12] : l;
    float big = fmx(__builtin_fabsf(o[0]), __builtin_fabsf(o[1]));
#pragma unroll
    for (int r = 2; r < 16; r += 2) big = fmx(fmx(big, __builtin_fabsf(o[r])), __builtin_fabsf(o[r + 1]));
    if (__builtin_expect(!__all(big < 1.0e36f), 0)) {
        const int nmid = br ? 0 : (T >= 7 ? 7 : T);
        f32x16 sol = qk(so, co);
        float m = 0.f;
#pragma unroll
        for (int r = 0; r < 16; ++r) { sol[r] = T < (br ? 1 : 8) ? sdg[r] : fmx(sol[r], sdg[r]); m = fmx(m, sol[r]); }
#pragma unroll 1
        for (int i = 1; i <= nmid; ++i) {
            const f32x16 s = qk(8 + w4 - i, cm);
#pragma unroll
            for (int r = 0; r < 16; ++r) m = fmx(m, s[r]);
        }
        m = fmx(m, xhalf(m));
        sol -= m; cm -= m;
        ex(sol);
        const u32x4 e0 = __builtin_bit_cast(u32x4, pack8(sol, 0)), e1 = __builtin_bit_cast(u32x4, pack8(sol, 1));
        o = (f32x16){};
        pv(8 + w4, __builtin_bit_cast(bf16x8, e0 & pm[0]), __builtin_bit_cast(bf16x8, e1 & pm[1]), o);
        pv(so, __builtin_bit_cast(bf16x8, e0 & ~pm[0]), __builtin_bit_cast(bf16x8, e1 & ~pm[1]), o);
#pragma unroll 1
        for (int i = 1; i <= nmid; ++i) {
            f32x16 s = qk(8 + w4 - i, cm);
            ex(s); pv(8 + w4 - i, pack8(s, 0), pack8(s, 1), o);
        }
        l = xhalf(o[12]);
        l = hh ? o[12] : l;
    }
    STAMP(11); CST(22);
    const __amdgpu_buffer_rsrc_t ros = rsrc(ws + WS_O, O_BYTES);
    const int orec = ((br * 64 + T) * 2 + hs) * 16384;
    const char* f2 = ws + WS_F2 + ((br * 64 + T) * 2 + hs) * 128;
    {
        const float inv = __builtin_amdgcn_rcpf(l);
        o[12] = hh ? 0.f : o[12];
        o = o * inv;
        __builtin_amdgcn_raw_buffer_store_b128(__builtin_bit_cast(u32x4, pack8(o, 0)), ros, lane * 16, orec + ty * 2048, 16);
        __builtin_amdgcn_raw_buffer_store_b128(__builtin_bit_cast(u32x4, pack8(o, 1)), ros, lane * 16 + 1024, orec + ty * 2048, 16);
    }
    WAITV(0);
    if (lane == 0) __hip_atomic_store((gu64*)(f2 + ty * 8), tag, RLX);
    __builtin_amdgcn_s_setprio(0);
    STAMP(13);
    SCHED();
    if (ty == 7) {
        if (b == 56) {
#pragma unroll 1
            for (int i = 0; i < 4; ++i) poll(ws + WS_F2 + (i * 64 + (threadIdx.x >> 3)) * 128 + u * 8, true, tag);
            BAR();
            if (threadIdx.x == 0) __hip_atomic_store((gu64*)ws, tag, RLX);
        }
        STAMP_FLUSH();
        return;
    }

    bf16x8 wf[16][2];
    {
        const unsigned tab = hs ? 0xBFD98621u : 0x4ECA7530u;
        const int wb0 = WO_OFF + (8 * hh + q) * 128 + ((blk * 2 + (p & 1)) << 4) + (p >> 1) * 8 + ((q >> 1) << 6), wb1 = wb0 ^ 64;
#pragma unroll
        for (int n = 0; n < 8; ++n) {
            const int row = ((tab >> (4 * n)) & 15) * (28 * 128);
            const char* a0 = smem + wb0 + row;
            const char* a1 = smem + wb1 + row;
#pragma unroll
            for (int s = 0; s < 2; ++s) { wf[2 * n + s][0] = tr_frag(a0 + s * 2048, a0 + s * 2048 + 512); wf[2 * n + s][1] = tr_frag(a1 + s * 2048, a1 + s * 2048 + 512); }
        }
    }
    STAMP(12);
    poll(lane < 8 ? f2 + lane * 8 : f2 + ((lane & 3) - w4) * 256 + ty * 8, lane < (hs ? 8 : 12), tag);
    STAMP(15);
    f32x16 acc[2];
    char* part = smem + w4 * 8192 + lane * 16;
    {
        u32x4 of[16];
#pragma unroll
        for (int ks = 0; ks < 16; ++ks) of[ks] = __builtin_amdgcn_raw_buffer_load_b128(ros, lane * 16 + (ks & 3) * 1024, orec + (ks >> 2) * 4096, 16);
        SCHED();
        STAMP(9);
        if (hs) {
            BAR();
#pragma unroll
            for (int i = 0; i < 8; ++i) { const f32x4 v = *(const f32x4*)(part + i * 1024); acc[i >> 2][4 * (i & 3)] = v[0]; acc[i >> 2][4 * (i & 3) + 1] = v[1]; acc[i >> 2][4 * (i & 3) + 2] = v[2]; acc[i >> 2][4 * (i & 3) + 3] = v[3]; }
        } else {
            acc[0] = (f32x16){}; acc[1] = (f32x16){};
        }
        STAMP(14);
#pragma unroll
        for (int ks = 0; ks < 16; ++ks)
#pragma unroll
            for (int i = 0; i < 2; ++i) acc[i] = MFMA(wf[ks][i], __builtin_bit_cast(bf16x8, of[ks]), acc[i]);
    }
    SCHED();
    STAMP(16);
    if (!hs) {
#pragma unroll
        for (int i = 0; i < 8; ++i) *(f32x4*)(part + i * 1024) = (f32x4){acc[i >> 2][4 * (i & 3)], acc[i >> 2][4 * (i & 3) + 1], acc[i >> 2][4 * (i & 3) + 2], acc[i >> 2][4 * (i & 3) + 3]};
        WAITL0(); BAR();
    } else {
        char* tb = smem + 32768 + w4 * 4096;
#pragma unroll
        for (int i = 0; i < 2; ++i)
#pragma unroll
            for (int s = 0; s < 2; ++s) *(bf16x8*)(tb + t * 128 + (((4 * i + 2 * s + hh) ^ (t & 7)) << 4)) = pack8(acc[i], s);
        WAITL0();
        const __amdgpu_buffer_rsrc_t rout = rsrc(out, 2048 * 1792);
#pragma unroll
        for (int i = 0; i < 4; ++i) {
            const u32x4 v = *(const u32x4*)(tb + (i * 8 + r3) * 128 + ((u ^ r3) << 4));
            __builtin_amdgcn_raw_buffer_store_b128(v, rout, r3 * 1792 + u * 16, (T * 32 + i * 8) * 1792 + br * 896 + ty * 128, 16);
        }
    }
    STAMP(17);
    STAMP_FLUSH();
}

extern "C" void kernel_launch(void* const* d_in, const int* in_sizes, int n_in, void* d_out, int out_size, void* d_ws, size_t ws_size, hipStream_t stream) {
    fused<<<256, 512, 0, stream>>>((const char*)d_in[0], (const char*)d_in[1], (const char*)d_in[2], (const char*)d_in[3], (const char*)d_in[4], (const char*)d_in[5], (char*)d_out, (char*)d_ws);
}
```

```cpp
#define DIAG 1
#include <hip/hip_runtime.h>
#include <hip/hip_bf16.h>

typedef __bf16 bf16x8 __attribute__((ext_vector_type(8)));
typedef short s16x4 __attribute__((ext_vector_type(4)));
typedef float f32x16 __attribute__((ext_vector_type(16)));
typedef float f32x8 __attribute__((ext_vector_type(8)));
typedef float f32x4 __attribute__((ext_vector_type(4)));
typedef unsigned u32x4 __attribute__((ext_vector_type(4)));
typedef unsigned u32x2 __attribute__((ext_vector_type(2)));
typedef unsigned long long u64;
typedef __attribute__((address_space(1))) u64 gu64;
#define LAS __attribute__((address_space(3)))
#define DI __device__ __forceinline__
#define MFMA(a, b, c) __builtin_amdgcn_mfma_f32_32x32x16_bf16((a), (b), (c), 0, 0, 0)
#define RLX __ATOMIC_RELAXED, __HIP_MEMORY_SCOPE_AGENT

constexpr int XB = 2048 * 896, WB = 448 * 2688, OB = 448 * 896;
constexpr int STAGE = 40960, RING = 40960, WO_OFF = 105472, LDS_BYTES = 163840;
constexpr int WS_F1 = 4096, WS_F2 = 65536, WS_X = 1 << 20, X_BYTES = 2 * 16 * 64 * 6144, WS_O = 16 << 20, O_BYTES = 2 * 64 * 16 * 2048;
constexpr int OOB = 0x40000000;
constexpr int CODE_LINES = 126;
constexpr int SPIN_MAX = 1 << 18;

__device__ u64 g_diag[256 * 8 * 32];
#define STAMP(i) do { u64 t_; asm volatile("s_memrealtime %0\n\ts_waitcnt lgkmcnt(0)" : "=s"(t_) :: "memory"); stamps[i] = t_; } while (0)
#define CST(i) do { u64 t_; asm volatile("s_memtime %0\n\ts_waitcnt lgkmcnt(0)" : "=s"(t_) :: "memory"); stamps[i] = t_; } while (0)
#define STAMP_DECL u64 stamps[24]; _Pragma("unroll") for (int i_ = 0; i_ < 24; ++i_) stamps[i_] = 0
#define STAMP_FLUSH() do { if ((threadIdx.x & 63) == 0) { _Pragma("unroll") for (int i_ = 0; i_ < 24; ++i_) g_diag[(blockIdx.x * 8 + (threadIdx.x >> 6)) * 32 + i_] = stamps[i_]; } } while (0)

DI u32x4 srd(const void* p, unsigned bytes) { const u64 a = (u64)p; return (u32x4){(unsigned)a, (unsigned)(a >> 32) & 0xffffu, bytes, 0x00020000u}; }
DI __amdgpu_buffer_rsrc_t rsrc(const void* p, int bytes) { return __builtin_amdgcn_make_buffer_rsrc((void*)p, 0, bytes, 0x00020000); }
DI void dma16(u32x4 r, int lds, int voff, int soff) {
    asm volatile("s_mov_b32 m0, %0\n\ts_nop 0\n\tbuffer_load_dwordx4 %1, %2, %3 offen lds" :: "s"(lds), "v"(voff), "s"(r), "s"(soff) : "memory");
}
DI void dma16_dev(u32x4 r, int lds, int voff, int soff) {
    asm volatile("s_mov_b32 m0, %0\n\ts_nop 0\n\tbuffer_load_dwordx4 %1, %2, %3 offen sc1 lds" :: "s"(lds), "v"(voff), "s"(r), "s"(soff) : "memory");
}
DI void dma64_dev(u32x4 r, int lds, int voff, int soff) {
    asm volatile("s_mov_b32 m0, %0\n\ts_nop 0\n\tbuffer_load_dwordx4 %1, %2, %3 offen sc1 lds\n\tbuffer_load_dwordx4 %1, %2, %3 offen offset:1024 sc1 lds\n\t"
                 "buffer_load_dwordx4 %1, %2, %3 offen offset:2048 sc1 lds\n\tbuffer_load_dwordx4 %1, %2, %3 offen offset:3072 sc1 lds" :: "s"(lds), "v"(voff), "s"(r), "s"(soff) : "memory");
}
DI bf16x8 tr_frag(const char* a0, const char* a1) {
    s16x4 lo = __builtin_amdgcn_ds_read_tr16_b64_v4i16((LAS s16x4*)a0);
    s16x4 hi = __builtin_amdgcn_ds_read_tr16_b64_v4i16((LAS s16x4*)a1);
    return __builtin_bit_cast(bf16x8, __builtin_shufflevector(lo, hi, 0, 1, 2, 3, 4, 5, 6, 7));
}
DI bf16x8 pack8(const f32x16& v, int s) {
    f32x8 t = {v[8 * s], v[8 * s + 1], v[8 * s + 2], v[8 * s + 3], v[8 * s + 4], v[8 * s + 5], v[8 * s + 6], v[8 * s + 7]};
    return __builtin_convertvector(t, bf16x8);
}
DI float xhalf(float v) {
    const unsigned u = __builtin_bit_cast(unsigned, v);
    const auto r = __builtin_amdgcn_permlane32_swap(u, u, false, false);
    return __builtin_bit_cast(float, (threadIdx.x & 32) ? r[0] : r[1]);
}
#define WAITV(n) asm volatile("s_waitcnt vmcnt(" #n ")" ::: "memory")
#define WAITL0() asm volatile("s_waitcnt lgkmcnt(0)" ::: "memory")
#define BAR() do { asm volatile("" ::: "memory"); __builtin_amdgcn_s_barrier(); asm volatile("" ::: "memory"); } while (0)
#define SCHED() __builtin_amdgcn_sched_barrier(0)

#define SG(m, n) __builtin_amdgcn_sched_group_barrier(m, n, 0)
DI float fmx(float a, float b) { return __builtin_elementwise_maximum(a, b); }
typedef int i32x4 __attribute__((ext_vector_type(4)));
#define PIN(x) asm volatile("" : "+v"(x))
DI void poll(const char* p, bool active, u64 tag) {
#pragma unroll 1
    for (int n = 0; n < SPIN_MAX; ++n) {
        u64 v = tag;
        if (active) v = __hip_atomic_load((gu64*)p, RLX);
        if (__all(v == tag)) break;
        __builtin_amdgcn_s_sleep(1);
    }
    asm volatile("" ::: "memory");
}

extern "C" __global__ void __launch_bounds__(512, 2)
fused(const char* xa, const char* wa, const char* woa, const char* xb, const char* wb, const char* wob, char* out, char* ws) {
    __shared__ __attribute__((aligned(16))) char smem[LDS_BYTES];
    STAMP_DECL;
    STAMP(0);
    const int b = blockIdx.x, xcd = b & 7, idx = b >> 3;
    const int br = xcd & 1, jj = idx >> 3, ty = idx & 7, j = (xcd >> 1) * 4 + jj;
    const int wave = __builtin_amdgcn_readfirstlane(threadIdx.x >> 6), lane = threadIdx.x & 63;
    const int r3 = lane >> 3, u = lane & 7;
    const int lbase = (int)(unsigned)(size_t)(LAS char*)smem;
    u64 epoch;
    asm volatile("global_load_dwordx2 %0, %1, %2 sc1" : "=v"(epoch) : "v"(0), "s"(ws) : "memory");
    const int w4 = wave & 3, hs = wave >> 2;
    const int hh = lane >> 5, t = lane & 31, blk = (lane >> 4) & 1, q = (lane >> 2) & 3, p = lane & 3;
    const int ha = (0x4ECA7530u >> (4 * ty)) & 15, hb = (0xBFD98621u >> (4 * ty)) & 15;
    const int rec = ((br * 16) * 64 + 4 * j) * 6144;
    const __amdgpu_buffer_rsrc_t rxs = rsrc(ws + WS_X, X_BYTES);

    if (ty < 7) {
        const u32x4 rx = srd(br ? xb : xa, XB), rw = srd(br ? wb : wa, WB);
        const int vx0 = r3 * 896 + ((u ^ (r3 >> 1)) << 4), vx1 = vx0 ^ 64;
        const int vw = r3 * 2688 + ((u ^ (((r3 >> 1) & 1) << 2)) << 4);
        int rot = ty + 2 * jj; if (rot >= 7) rot -= 7;
        const int sx = j * (128 * 896), sw = ty * 128;
        {
            const int st = lbase + RING + 2 * STAGE;
            dma16(rx, st + wave * 2048, vx0, sx + wave * 14336 + rot * 128);
            dma16(rx, st + wave * 2048 + 1024, vx1, sx + wave * 14336 + 7168 + rot * 128);
#pragma unroll
            for (int cb = 0; cb < 3; ++cb) dma16(rw, st + 16384 + cb * 8192 + wave * 1024, vw, sw + (rot * 64 + wave * 8) * 2688 + cb * 896);
        }
        const int wr = w4 >> 1, wc = w4 & 1;
        const int sp0 = (int)(0x04152630415263ull >> (4 * (2 * ty + wc))) & 15, sp1 = (int)(0x04152630415263ull >> (4 * (2 * ty + (wc ^ 1)))) & 15;
        char* tab = smem + w4 * 7168 + lane * 16;
        if (__builtin_expect(wave >= 4, 1)) {
            int ch = rot, sg = 2;
#pragma unroll 1
            for (int c = 1; c < 7; ++c) {
                ++ch; if (ch == 7) ch = 0;
                ++sg; if (sg == 3) sg = 0;
                const int st = lbase + RING + sg * STAGE;
#pragma unroll
                for (int i = 0; i < 4; ++i) dma16(rx, st + w4 * 4096 + i * 1024, (i & 1) ? vx1 : vx0, sx + (w4 * 4 + i) * 7168 + ch * 128);
#pragma unroll
                for (int cb = 0; cb < 3; ++cb)
#pragma unroll
                    for (int i = 0; i < 2; ++i) dma16(rw, st + 16384 + cb * 8192 + w4 * 2048 + i * 1024, vw, sw + (ch * 64 + w4 * 16 + i * 8) * 2688 + cb * 896);
                WAITV(10); BAR();
            }
            int g[3][2][2], l[3][2], g2[3][2], l2[3];
#pragma unroll
            for (int i = 0; i < 3; ++i) {
                const int sec = (3 * wc + i) >> 1, c0 = 8 * ty + 4 * ((wc + i) & 1), sp = (i & 1) ? sp1 : sp0;
                const int go = rec + wr * 12288 + (sec == 0 ? 4096 : (sec - 1) * 2048);
                const int lo = wr * 8192 + (sec == 0 ? 0 : 30720 + sec * 2048);
#pragma unroll
                for (int s = 0; s < 3; ++s) {
                    const int c = c0 + (s < 2 ? 2 * s + hh : sp), m = (c * 37) >> 8, r = c - 7 * m;
                    const int hd = 2 * m + (s < 2 ? (r > 3) : 1), slot = s < 2 ? (r > 3 ? r - 3 : r) : 0;
                    const int pos = slot * 512 + ((((t >> 2) ^ slot) << 2) + (t & 3)) * 16;
                    const bool on = s < 2 || hh == (sp & 1), own = hd == ha || hd == hb;
                    const int la = ((own && on) ? (hd == hb ? 49152 : 0) : 65536) + lo + pos;
                    if (s < 2) l[i][s] = la; else l2[i] = la;
#pragma unroll
                    for (int tt = 0; tt < 2; ++tt) {
                        const bool pub = sec != 0 && (br == 0 || (wr == 1 && tt == 1));
                        const int ga = (on && (pub || !own)) ? go + tt * 6144 + hd * (64 * 6144) + pos : OOB;
                        if (s < 2) g[i][tt][s] = ga; else g2[i][tt] = ga;
                    }
                }
                *(i32x4*)(tab + i * 2048) = (i32x4){g[i][0][0], g[i][0][1], g[i][1][0], g[i][1][1]};
                *(i32x4*)(tab + i * 2048 + 1024) = (i32x4){l[i][0], l[i][1], g2[i][0], g2[i][1]};
            }
            *(i32x4*)(tab + 6144) = (i32x4){l2[0], l2[1], l2[2], 0};
            asm volatile("s_waitcnt vmcnt(0) lgkmcnt(0)" ::: "memory"); BAR();
            BAR();
            const u32x4 rwo = srd(br ? wob : woa, OB);
            const int vwo = r3 * 896 + ((u ^ (((r3 >> 1) & 1) << 2)) << 4);
#pragma unroll 1
            for (int n = 12 + w4; n < 57; n += 4) dma16(rwo, lbase + WO_OFF + n * 1024, vwo, n * 7168 + ty * 128);
            BAR();
#pragma unroll 1
            for (int n = w4; n < 12; n += 4) dma16(rwo, lbase + WO_OFF + n * 1024, vwo, n * 7168 + ty * 128);
        } else {
            int xo[4], wo_[3];
#pragma unroll
            for (int ks = 0; ks < 4; ++ks) xo[ks] = (wr * 64 + t) * 128 + (((2 * ks + hh) ^ ((t >> 1) & 7)) << 4);
#pragma unroll
            for (int i = 0; i < 3; ++i) { const int ct = 3 * wc + i, cb = ct >> 1, half = ct & 1;
                wo_[i] = 16384 + cb * 8192 + (8 * hh + q) * 128 + ((((half ^ (q >> 1)) << 2) + blk * 2 + (p & 1)) << 4) + (p >> 1) * 8; }
            f32x16 acc[2][3] = {};
#pragma unroll
            for (int i = 0; i < 3; ++i) { PIN(acc[0][i]); PIN(acc[1][i]); }
            bf16x8 xf[2][2], wf[2][3];
            WAITV(0); BAR();
            STAMP(1); CST(20);
            int sg = 2;
            const char* st = smem + RING + 2 * STAGE;
            auto rd = [&](int f, int ks) {
#pragma unroll
                for (int i = 2; i >= 0; --i) {
                    wf[f][i] = tr_frag(st + wo_[i] + ks * 2048, st + wo_[i] + ks * 2048 + 512);
                    if (i == 2) { xf[f][0] = *(const bf16x8*)(st + xo[ks]); xf[f][1] = *(const bf16x8*)(st + xo[ks] + 4096); }
                }
            };
            auto mm = [&](int f) {
#pragma unroll
                for (int i = 2; i >= 0; --i)
#pragma unroll
                    for (int tt = 0; tt < 2; ++tt) acc[tt][i] = MFMA(wf[f][i], xf[f][tt], acc[tt][i]);
            };
#define MIX() do { SG(0x8, 1); SG(0x100, 2); SG(0x8, 1); SG(0x100, 2); SG(0x8, 1); SG(0x100, 2); SG(0x8, 1); SG(0x100, 2); SG(0x8, 2); SCHED(); } while (0)
            rd(0, 0);
            SCHED();
#pragma unroll 1
            for (int c = 0; c < 6; ++c) {
                rd(1, 1); mm(0); MIX();
                rd(0, 2); mm(1); MIX();
                rd(1, 3); mm(0); MIX();
                WAITL0(); BAR();
                ++sg; if (sg == 3) sg = 0;
                st = smem + RING + sg * STAGE;
                SCHED();
                rd(0, 0); mm(1); MIX();
            }
            i32x4 tb[7];
            rd(1, 1); mm(0); MIX();
            rd(0, 2); mm(1); MIX();
            rd(1, 3);
#pragma unroll
            for (int i = 0; i < 7; ++i) tb[i] = *(const i32x4*)(tab + i * 1024);
            mm(0);
            SG(0x8, 1); SG(0x100, 3); SG(0x8, 1); SG(0x100, 3); SG(0x8, 1); SG(0x100, 3); SG(0x8, 1); SG(0x100, 3); SG(0x8, 1); SG(0x100, 3); SG(0x8, 1); SCHED();
            WAITL0(); BAR();
            SCHED();
            mm(1);
            SCHED();
            STAMP(2); CST(21);
            const float qs = 0.18898223650461361f * 1.4426950408889634f;
#pragma unroll
            for (int i = 2; i >= 0; --i) {
                const int sp = (i & 1) ? sp1 : sp0;
                const unsigned padv = (wc && i) ? 0x00003F80u : 0u;
                if (i < 2 && wc == 0) { acc[0][i] *= qs; acc[1][i] *= qs; }
                u32x4 d[2][2];
#pragma unroll
                for (int tt = 0; tt < 2; ++tt)
#pragma unroll
                    for (int s = 0; s < 2; ++s) d[tt][s] = __builtin_bit_cast(u32x4, pack8(acc[tt][i], s));
                auto fix = [&](int s) {
                    const bool sh = hh == (sp & 1);
#pragma unroll
                    for (int tt = 0; tt < 2; ++tt) {
                        const u32x4 d2 = {padv, 0u, d[tt][s].z, d[tt][s].w};
                        __builtin_amdgcn_raw_buffer_store_b128(d2, rxs, tb[2 * i + 1][2 + tt], 0, 16);
                        *(u32x4*)(smem + tb[6][i] + tt * 4096) = d2;
                        d[tt][s].z = sh ? padv : d[tt][s].z; d[tt][s].w = sh ? 0u : d[tt][s].w;
                    }
                };
                if (sp < 2) fix(0); else if (sp < 4) fix(1);
#pragma unroll
                for (int tt = 0; tt < 2; ++tt)
#pragma unroll
                    for (int s = 0; s < 2; ++s) __builtin_amdgcn_raw_buffer_store_b128(d[tt][s], rxs, tb[2 * i][2 * tt + s], 0, 16);
#pragma unroll
                for (int tt = 0; tt < 2; ++tt)
#pragma unroll
                    for (int s = 0; s < 2; ++s) *(u32x4*)(smem + tb[2 * i + 1][s] + tt * 4096) = d[tt][s];
            }
            WAITL0();
            STAMP(3);
            BAR();
        }
    } else {
        u64 pc; unsigned pco;
        asm volatile("s_getpc_b64 %0\n.Lhere%=:\n\ts_mov_b32 %1, .Lhere%=-fused" : "=s"(pc), "=s"(pco));
        unsigned d0 = 0, d1 = 0, d2 = 0;
        if ((int)threadIdx.x < CODE_LINES) asm volatile("global_load_dword %0, %1, %2" : "=v"(d0) : "v"(threadIdx.x * 128), "s"(pc - pco) : "memory");
        const char* wo = br ? wob : woa;
        asm volatile("global_load_dword %0, %1, %2" : "=v"(d1) : "v"((jj * 784 + threadIdx.x) * 128), "s"(wo) : "memory");
        if (threadIdx.x < 272) asm volatile("global_load_dword %0, %1, %2" : "=v"(d2) : "v"((jj * 784 + 512 + threadIdx.x) * 128), "s"(wo) : "memory");
        asm volatile("s_waitcnt vmcnt(0)" : "+v"(d0), "+v"(d1), "+v"(d2) :: "memory");
    }
    STAMP(4);
    asm volatile("s_waitcnt vmcnt(0)" : "+v"(epoch) :: "memory");
    const u64 tag = epoch * 0x9E3779B97F4A7C15ull + 0x7F4A7C159E3779B9ull;
    if (wave < 4 && ty < 7 && lane == 0) __hip_atomic_store((gu64*)(ws + WS_F1 + (((br * 16 + j) * 8 + ty) * 16 + wave) * 8), tag, RLX);
    STAMP(5);
    SCHED();

    const int h = hs ? hb : ha;
    const int T = 4 * j + w4;
    const unsigned NEGB = 0xF149F2CAu;
    const int nb = ty == 7 ? 15 : (0xF63F30Fu >> (4 * ty)) & 15;
    const bool local = ty < 7 && (hs == 0 || nb == 15);
    int fo[2], vo[2];
#pragma unroll
    for (int s = 0; s < 2; ++s) fo[s] = hs * 49152 + (2 * s + hh) * 512 + ((((t >> 2) ^ (2 * s + hh)) << 2) + (t & 3)) * 16;
    { const int pc8 = (4 * blk + 2 * (p & 1) + (p >> 1) + (h & 1)) & 7, cc = pc8 >> 1;
#pragma unroll
      for (int tt = 0; tt < 2; ++tt) { vo[tt] = hs * 49152 + 2048 + cc * 512 + ((((2 * tt + hh) ^ cc) << 2) + q) * 16 + (pc8 & 1) * 8; PIN(vo[tt]); } }
    u32x4 qf[2];
    qf[0] = *(const u32x4*)(smem + w4 * 4096 + fo[0]); qf[1] = *(const u32x4*)(smem + w4 * 4096 + fo[1]);
    f32x16 sdg, co;
    u32x4 pm[2];
    {
        const unsigned lo = (2u << t) - 1u;
        const unsigned pat = br ? __builtin_rotateleft32(0x55540001u, t) : ~0u;
        const int nd = (int)(~(pat & lo) >> (4 * hh)), no = (int)(~(pat & ~lo) >> (4 * hh)), lm = (int)(lo >> (4 * hh));
#pragma unroll
        for (int r = 0; r < 16; ++r) {
            const int bit = (r & 3) + 8 * (r >> 2);
            sdg[r] = __builtin_bit_cast(float, __builtin_amdgcn_sbfe(nd, bit, 1) & NEGB);
            co[r] = __builtin_bit_cast(float, __builtin_amdgcn_sbfe(no, bit, 1) & NEGB);
            if (r & 1) pm[r >> 3][(r >> 1) & 3] = __builtin_amdgcn_perm(__builtin_amdgcn_sbfe(lm, bit, 1), __builtin_amdgcn_sbfe(lm, bit - 1, 1), 0x05040100u);
        }
        PIN(sdg); PIN(co); PIN(pm[0]); PIN(pm[1]);
    }
    auto kfrag = [&](int slot, int s) { return *(const bf16x8*)(smem + slot * 4096 + fo[s]); };
    auto vfrag = [&](int slot, int s) { const char* vb = smem + slot * 4096 + s * 256; return tr_frag(vb + vo[0], vb + vo[1]); };
    auto qk = [&](int slot, const f32x16& c) {
        f32x16 r = MFMA(kfrag(slot, 0), __builtin_bit_cast(bf16x8, qf[0]), c);
        return MFMA(kfrag(slot, 1), __builtin_bit_cast(bf16x8, qf[1]), r);
    };
    auto pv = [&](int slot, bf16x8 p0, bf16x8 p1, f32x16& o) { o = MFMA(vfrag(slot, 0), p0, o); o = MFMA(vfrag(slot, 1), p1, o); };
    f32x16 cm = {};
    auto pre = [&]() {
        sdg = qk(8 + w4, sdg);
        float m = fmx(sdg[0], sdg[1]);
#pragma unroll
        for (int r = 2; r < 16; r += 2) m = fmx(fmx(m, sdg[r]), sdg[r + 1]);
        m = -fmx(m, xhalf(m));
#pragma unroll
        for (int r = 0; r < 16; ++r) cm[r] = m;
        sdg += cm; co += cm;
        PIN(cm); PIN(sdg); PIN(co);
    };
    const int sl = (ty == 2 || ty == 5) ? 3 : 0;
    const int qpl = 98304 + (ty == 7 ? wave * 2048 : w4 * 512);
    const u32x4 rxx = srd(ws + WS_X, X_BYTES);
    const bool ok = j > hs;
    int vv = ok ? lane * 16 : OOB;
    int ra = ok ? rec + (w4 - 4 - 4 * hs + ha * 64) * 6144 : 0, rb = ok ? rec + (w4 - 4 - 4 * hs + hb * 64) * 6144 : 0, lb = lbase + (4 - 4 * hs + w4) * 4096;
    int rh = rec + (h * 64 + w4) * 6144, lh = lbase + hs * 49152 + w4 * 4096 + 32768;
    asm volatile("" : "+v"(vv), "+s"(ra), "+s"(rb), "+s"(lb), "+s"(rh), "+s"(lh));
#pragma unroll 1
    for (int ph = 0;; ++ph) {
        if (ph == (local ? 0 : 1)) {
            if (ph) {
#pragma unroll
                for (int s = 0; s < 2; ++s) if (ty == 7 || 2 * s + hh == sl) qf[s] = *(const u32x4*)(smem + qpl + (fo[s] & (ty == 7 ? 2047 : 511)));
            }
            pre();
        }
        if (ph) break;
        WAITL0();
        if (wave == 4) {
            const int bk = lane >> 4;
            const int pg = ty == 7 ? (0x5421 >> (4 * q)) & 15 : (q == 0 ? ty : (q == 1 ? nb : 15));
            const bool act = bk < (br ? 2 : 3) && j - bk >= 0 && pg != 15 && !(bk == 0 && pg == ty);
            poll(ws + WS_F1 + (((br * 16 + j - bk) * 8 + pg) * 16 + p) * 8, act, tag);
        }
        STAMP(6);
        BAR();
#pragma unroll 1
        for (int g = 0; g < 2; ++g) {
            if (!local && g == hs) {
                if (ty == 7) {
                    dma16_dev(rxx, lbase + qpl, lane * 16, rh + 4096);
                    dma16_dev(rxx, lbase + qpl + 1024, lane * 16, rh + 5120);
                    dma64_dev(rxx, lh, lane * 16, rh);
                } else if (lane < 32) {
                    dma16_dev(rxx, lbase + qpl, lane * 16, rh + 4096 + sl * 512);
                    dma16_dev(rxx, lh + sl * 512, lane * 16, rh + sl * 512);
                    dma16_dev(rxx, lh + 2048 + sl * 512, lane * 16, rh + 2048 + sl * 512);
                }
            }
            if (g == 0 && (br == 0 || wave == 3)) {
                dma64_dev(rxx, lb, vv, ra);
                dma64_dev(rxx, lb + 49152, vv, rb);
                WAITV(4);
            } else {
                WAITV(0);
            }
            STAMP(7);
            BAR();
            if (!hs) break;
        }
        STAMP(8);
        if (wave < 4) __builtin_amdgcn_s_setprio(1);
    }
    const int so = br ? 7 + w4 : w4;
    STAMP(10); CST(19);
    const bf16x8 q0 = __builtin_bit_cast(bf16x8, qf[0]), q1 = __builtin_bit_cast(bf16x8, qf[1]);
    auto ex = [&](f32x16& s) {
#pragma unroll
        for (int r = 0; r < 16; ++r) s[r] = __builtin_amdgcn_exp2f(s[r]);
    };
    f32x16 o;
    {
        const f32x16 zero = {};
        bf16x8 k0 = kfrag(w4 + 1, 0), k1 = kfrag(w4 + 1, 1);
        f32x16 sa = qk(so, co), sb;
        bf16x8 v0 = vfrag(8 + w4, 0), v1 = vfrag(8 + w4, 1), v2 = vfrag(so, 0), v3 = vfrag(so, 1);
        SCHED();
        sb = MFMA(k0, q0, cm); sb = MFMA(k1, q1, sb);
        k0 = kfrag(w4 + 2, 0); k1 = kfrag(w4 + 2, 1);
#pragma unroll
        for (int r = 0; r < 16; ++r) sa[r] = fmx(sa[r], sdg[r]);
        ex(sa);
        const u32x4 e0 = __builtin_bit_cast(u32x4, pack8(sa, 0)), e1 = __builtin_bit_cast(u32x4, pack8(sa, 1));
        o = MFMA(v0, __builtin_bit_cast(bf16x8, e0 & pm[0]), zero);
        o = MFMA(v2, __builtin_bit_cast(bf16x8, e0 & ~pm[0]), o);
        bf16x8 p0 = __builtin_bit_cast(bf16x8, e1 & pm[1]), p1 = __builtin_bit_cast(bf16x8, e1 & ~pm[1]);
        v0 = v1; v1 = v3;
        SCHED();
        CST(23);
        if (!hs) { WAITV(0); BAR(); }
        if (br == 0) {
#define STG(n, cur, nxt) do { \
                if (n < 7) { nxt = MFMA(k0, q0, cm); nxt = MFMA(k1, q1, nxt); } \
                o = MFMA(v0, p0, o); o = MFMA(v1, p1, o); \
                if (n < 6) { k0 = kfrag(w4 + n + 2, 0); k1 = kfrag(w4 + n + 2, 1); } \
                v0 = vfrag(w4 + n, 0); v1 = vfrag(w4 + n, 1); \
                ex(cur); p0 = pack8(cur, 0); p1 = pack8(cur, 1); \
                if (n < 7) { SG(0x8, 1); SG(0x400, 4); SG(0x8, 1); if (n < 6) SG(0x100, 2); SG(0x400, 4); } \
                SG(0x8, 1); SG(0x100, 2); SG(0x400, 4); SG(0x8, 1); SG(0x100, 2); SG(0x400, n < 7 ? 4 : 12); SG(0x2, 8); SCHED(); } while (0)
            STG(1, sb, sa); STG(2, sa, sb); STG(3, sb, sa); STG(4, sa, sb); STG(5, sb, sa); STG(6, sa, sb); STG(7, sb, sa);
        }
        o = MFMA(v0, p0, o); o = MFMA(v1, p1, o);
    }
    float l = xhalf(o[12]);
    l = hh ? o[12] : l;
    float big = fmx(__builtin_fabsf(o[0]), __builtin_fabsf(o[1]));
#pragma unroll
    for (int r = 2; r < 16; r += 2) big = fmx(fmx(big, __builtin_fabsf(o[r])), __builtin_fabsf(o[r + 1]));
    if (__builtin_expect(!__all(big < 1.0e36f), 0)) {
        const int nmid = br ? 0 : (T >= 7 ? 7 : T);
        f32x16 sol = qk(so, co);
        float m = 0.f;
#pragma unroll
        for (int r = 0; r < 16; ++r) { sol[r] = T < (br ? 1 : 8) ? sdg[r] : fmx(sol[r], sdg[r]); m = fmx(m, sol[r]); }
#pragma unroll 1
        for (int i = 1; i <= nmid; ++i) {
            const f32x16 s = qk(8 + w4 - i, cm);
#pragma unroll
            for (int r = 0; r < 16; ++r) m = fmx(m, s[r]);
        }
        m = fmx(m, xhalf(m));
        sol -= m; cm -= m;
        ex(sol);
        const u32x4 e0 = __builtin_bit_cast(u32x4, pack8(sol, 0)), e1 = __builtin_bit_cast(u32x4, pack8(sol, 1));
        o = (f32x16){};
        pv(8 + w4, __builtin_bit_cast(bf16x8, e0 & pm[0]), __builtin_bit_cast(bf16x8, e1 & pm[1]), o);
        pv(so, __builtin_bit_cast(bf16x8, e0 & ~pm[0]), __builtin_bit_cast(bf16x8, e1 & ~pm[1]), o);
#pragma unroll 1
        for (int i = 1; i <= nmid; ++i) {
            f32x16 s = qk(8 + w4 - i, cm);
            ex(s); pv(8 + w4 - i, pack8(s, 0), pack8(s, 1), o);
        }
        l = xhalf(o[12]);
        l = hh ? o[12] : l;
    }
    STAMP(11); CST(22);
    const __amdgpu_buffer_rsrc_t ros = rsrc(ws + WS_O, O_BYTES);
    const int orec = ((br * 64 + T) * 2 + hs) * 16384;
    const char* f2 = ws + WS_F2 + ((br * 64 + T) * 2 + hs) * 128;
    {
        const float inv = __builtin_amdgcn_rcpf(l);
        o[12] = hh ? 0.f : o[12];
        o = o * inv;
        __builtin_amdgcn_raw_buffer_store_b128(__builtin_bit_cast(u32x4, pack8(o, 0)), ros, lane * 16, orec + ty * 2048, 16);
        __builtin_amdgcn_raw_buffer_store_b128(__builtin_bit_cast(u32x4, pack8(o, 1)), ros, lane * 16 + 1024, orec + ty * 2048, 16);
    }
    WAITV(0);
    if (lane == 0) __hip_atomic_store((gu64*)(f2 + ty * 8), tag, RLX);
    __builtin_amdgcn_s_setprio(0);
    STAMP(13);
    SCHED();
    if (ty == 7) {
        if (b == 56) {
#pragma unroll 1
            for (int i = 0; i < 4; ++i) poll(ws + WS_F2 + (i * 64 + (threadIdx.x >> 3)) * 128 + u * 8, true, tag);
            BAR();
            if (threadIdx.x == 0) __hip_atomic_store((gu64*)ws, tag, RLX);
        }
        STAMP_FLUSH();
        return;
    }

    bf16x8 wf[16][2];
    {
        const unsigned tab = hs ? 0xBFD98621u : 0x4ECA7530u;
        const int wb0 = WO_OFF + (8 * hh + q) * 128 + ((blk * 2 + (p & 1)) << 4) + (p >> 1) * 8 + ((q >> 1) << 6), wb1 = wb0 ^ 64;
#pragma unroll
        for (int n = 0; n < 8; ++n) {
            const int row = ((tab >> (4 * n)) & 15) * (28 * 128);
            const char* a0 = smem + wb0 + row;
            const char* a1 = smem + wb1 + row;
#pragma unroll
            for (int s = 0; s < 2; ++s) { wf[2 * n + s][0] = tr_frag(a0 + s * 2048, a0 + s * 2048 + 512); wf[2 * n + s][1] = tr_frag(a1 + s * 2048, a1 + s * 2048 + 512); }
        }
    }
    STAMP(12);
    poll(lane < 8 ? f2 + lane * 8 : f2 + ((lane & 3) - w4) * 256 + ty * 8, lane < (hs ? 8 : 12), tag);
    STAMP(15);
    f32x16 acc[2];
    char* part = smem + w4 * 8192 + lane * 16;
    {
        u32x4 of[16];
#pragma unroll
        for (int ks = 0; ks < 16; ++ks) of[ks] = __builtin_amdgcn_raw_buffer_load_b128(ros, lane * 16 + (ks & 3) * 1024, orec + (ks >> 2) * 4096, 16);
        SCHED();
        STAMP(9);
        if (hs) {
            BAR();
#pragma unroll
            for (int i = 0; i < 8; ++i) { const f32x4 v = *(const f32x4*)(part + i * 1024); acc[i >> 2][4 * (i & 3)] = v[0]; acc[i >> 2][4 * (i & 3) + 1] = v[1]; acc[i >> 2][4 * (i & 3) + 2] = v[2]; acc[i >> 2][4 * (i & 3) + 3] = v[3]; }
        } else {
            acc[0] = (f32x16){}; acc[1] = (f32x16){};
        }
        STAMP(14);
#pragma unroll
        for (int ks = 0; ks < 16; ++ks)
#pragma unroll
            for (int i = 0; i < 2; ++i) acc[i] = MFMA(wf[ks][i], __builtin_bit_cast(bf16x8, of[ks]), acc[i]);
    }
    SCHED();
    STAMP(16);
    if (!hs) {
#pragma unroll
        for (int i = 0; i < 8; ++i) *(f32x4*)(part + i * 1024) = (f32x4){acc[i >> 2][4 * (i & 3)], acc[i >> 2][4 * (i & 3) + 1], acc[i >> 2][4 * (i & 3) + 2], acc[i >> 2][4 * (i & 3) + 3]};
        WAITL0(); BAR();
    } else {
        char* tb = smem + 32768 + w4 * 4096;
#pragma unroll
        for (int i = 0; i < 2; ++i)
#pragma unroll
            for (int s = 0; s < 2; ++s) *(bf16x8*)(tb + t * 128 + (((4 * i + 2 * s + hh) ^ (t & 7)) << 4)) = pack8(acc[i], s);
        WAITL0();
        const __amdgpu_buffer_rsrc_t rout = rsrc(out, 2048 * 1792);
#pragma unroll
        for (int i = 0; i < 4; ++i) {
            const u32x4 v = *(const u32x4*)(tb + (i * 8 + r3) * 128 + ((u ^ r3) << 4));
            __builtin_amdgcn_raw_buffer_store_b128(v, rout, r3 * 1792 + u * 16, (T * 32 + i * 8) * 1792 + br * 896 + ty * 128, 16);
        }
    }
    STAMP(17);
    WAITV(0);
    STAMP(18);
    STAMP_FLUSH();
}

__global__ void k_probe(int ia, int maxa) {
    __shared__ unsigned long long sa[1024];
    const int b = threadIdx.x;
    unsigned long long x0 = g_diag[(b * 2) * 32 + ia], x1 = g_diag[(b * 2 + 1) * 32 + ia];
    if (!maxa) { if (x0 == 0) x0 = ~0ull; if (x1 == 0) x1 = ~0ull; }
    sa[b] = maxa ? (x0 > x1 ? x0 : x1) : (x0 < x1 ? x0 : x1); __syncthreads();
    for (int o = 512; o > 0; o >>= 1) { if (b < o) { unsigned long long x = sa[b], y = sa[b + o]; sa[b] = maxa ? (x > y ? x : y) : (x < y ? x : y); } __syncthreads(); }
    if (b == 0) {
        const unsigned long long A = sa[0];
        unsigned long long t; int n = 0; do { asm volatile("s_memrealtime %0\n\ts_waitcnt lgkmcnt(0)" : "=s"(t) :: "memory"); } while (t - A < 6000 && ++n < 100000);
    }
}
extern "C" void kernel_launch(void* const* d_in, const int* in_sizes, int n_in, void* d_out, int out_size, void* d_ws, size_t ws_size, hipStream_t stream) {
    fused<<<256, 512, 0, stream>>>((const char*)d_in[0], (const char*)d_in[1], (const char*)d_in[2], (const char*)d_in[3], (const char*)d_in[4], (const char*)d_in[5], (char*)d_out, (char*)d_ws);
    k_probe<<<1, 1024, 0, stream>>>(0,0);
}
```

```cpp
#include <hip/hip_runtime.h>
#include <hip/hip_bf16.h>

typedef __bf16 bf16x8 __attribute__((ext_vector_type(8)));
typedef short s16x4 __attribute__((ext_vector_type(4)));
typedef float f32x16 __attribute__((ext_vector_type(16)));
typedef float f32x8 __attribute__((ext_vector_type(8)));
typedef float f32x4 __attribute__((ext_vector_type(4)));
typedef unsigned u32x4 __attribute__((ext_vector_type(4)));
typedef unsigned u32x2 __attribute__((ext_vector_type(2)));
typedef unsigned long long u64;
typedef __attribute__((address_space(1))) u64 gu64;
#define LAS __attribute__((address_space(3)))
#define DI __device__ __forceinline__
#define MFMA(a, b, c) __builtin_amdgcn_mfma_f32_32x32x16_bf16((a), (b), (c), 0, 0, 0)
#define RLX __ATOMIC_RELAXED, __HIP_MEMORY_SCOPE_AGENT

constexpr int XB = 2048 * 896, WB = 448 * 2688, OB = 448 * 896;
constexpr int STAGE = 40960, RING = 40960, WO_OFF = 105472, LDS_BYTES = 163840;
constexpr int WS_F1 = 4096, WS_F2 = 65536, WS_X = 1 << 20, X_BYTES = 2 * 16 * 64 * 6144, WS_O = 16 << 20, O_BYTES = 2 * 64 * 16 * 2048;
constexpr int OOB = 0x40000000;
constexpr int CODE_LINES = 126;
constexpr int SPIN_MAX = 1 << 18;

#define STAMP(i)
#define CST(i)
#define STAMP_DECL
#define STAMP_FLUSH()

DI u32x4 srd(const void* p, unsigned bytes) { const u64 a = (u64)p; return (u32x4){(unsigned)a, (unsigned)(a >> 32) & 0xffffu, bytes, 0x00020000u}; }
DI __amdgpu_buffer_rsrc_t rsrc(const void* p, int bytes) { return __builtin_amdgcn_make_buffer_rsrc((void*)p, 0, bytes, 0x00020000); }
DI void dma16(u32x4 r, int lds, int voff, int soff) {
    asm volatile("s_mov_b32 m0, %0\n\ts_nop 0\n\tbuffer_load_dwordx4 %1, %2, %3 offen lds" :: "s"(lds), "v"(voff), "s"(r), "s"(soff) : "memory");
}
DI void dma16_dev(u32x4 r, int lds, int voff, int soff) {
    asm volatile("s_mov_b32 m0, %0\n\ts_nop 0\n\tbuffer_load_dwordx4 %1, %2, %3 offen sc1 lds" :: "s"(lds), "v"(voff), "s"(r), "s"(soff) : "memory");
}
DI void dma64_dev(u32x4 r, int lds, int voff, int soff) {
    asm volatile("s_mov_b32 m0, %0\n\ts_nop 0\n\tbuffer_load_dwordx4 %1, %2, %3 offen sc1 lds\n\tbuffer_load_dwordx4 %1, %2, %3 offen offset:1024 sc1 lds\n\t"
                 "buffer_load_dwordx4 %1, %2, %3 offen offset:2048 sc1 lds\n\tbuffer_load_dwordx4 %1, %2, %3 offen offset:3072 sc1 lds" :: "s"(lds), "v"(voff), "s"(r), "s"(soff) : "memory");
}
DI bf16x8 tr_frag(const char* a0, const char* a1) {
    s16x4 lo = __builtin_amdgcn_ds_read_tr16_b64_v4i16((LAS s16x4*)a0);
    s16x4 hi = __builtin_amdgcn_ds_read_tr16_b64_v4i16((LAS s16x4*)a1);
    return __builtin_bit_cast(bf16x8, __builtin_shufflevector(lo, hi, 0, 1, 2, 3, 4, 5, 6, 7));
}
DI bf16x8 pack8(const f32x16& v, int s) {
    f32x8 t = {v[8 * s], v[8 * s + 1], v[8 * s + 2], v[8 * s + 3], v[8 * s + 4], v[8 * s + 5], v[8 * s + 6], v[8 * s + 7]};
    return __builtin_convertvector(t, bf16x8);
}
DI float xhalf(float v) {
    const unsigned u = __builtin_bit_cast(unsigned, v);
    const auto r = __builtin_amdgcn_permlane32_swap(u, u, false, false);
    return __builtin_bit_cast(float, (threadIdx.x & 32) ? r[0] : r[1]);
}
#define WAITV(n) asm volatile("s_waitcnt vmcnt(" #n ")" ::: "memory")
#define WAITL0() asm volatile("s_waitcnt lgkmcnt(0)" ::: "memory")
#define BAR() do { asm volatile("" ::: "memory"); __builtin_amdgcn_s_barrier(); asm volatile("" ::: "memory"); } while (0)
#define SCHED() __builtin_amdgcn_sched_barrier(0)

#define SG(m, n) __builtin_amdgcn_sched_group_barrier(m, n, 0)
DI float fmx(float a, float b) { return __builtin_elementwise_maximum(a, b); }
typedef int i32x4 __attribute__((ext_vector_type(4)));
#define PIN(x) asm volatile("" : "+v"(x))
DI void poll(const char* p, bool active, u64 tag) {
#pragma unroll 1
    for (int n = 0; n < SPIN_MAX; ++n) {
        u64 v = tag;
        if (active) v = __hip_atomic_load((gu64*)p, RLX);
        if (__all(v == tag)) break;
        __builtin_amdgcn_s_sleep(1);
    }
    asm volatile("" ::: "memory");
}

extern "C" __global__ void __launch_bounds__(512, 2)
fused(const char* xa, const char* wa, const char* woa, const char* xb, const char* wb, const char* wob, char* out, char* ws) {
    __shared__ __attribute__((aligned(16))) char smem[LDS_BYTES];
    STAMP_DECL;
    STAMP(0);
    const int b = blockIdx.x, xcd = b & 7, idx = b >> 3;
    const int br = xcd & 1, jj = idx >> 3, ty = idx & 7, j = (xcd >> 1) * 4 + jj;
    const int wave = __builtin_amdgcn_readfirstlane(threadIdx.x >> 6), lane = threadIdx.x & 63;
    const int r3 = lane >> 3, u = lane & 7;
    const int lbase = (int)(unsigned)(size_t)(LAS char*)smem;
    u64 epoch;
    asm volatile("global_load_dwordx2 %0, %1, %2 sc1" : "=v"(epoch) : "v"(0), "s"(ws) : "memory");
    const int w4 = wave & 3, hs = wave >> 2;
    const int hh = lane >> 5, t = lane & 31, blk = (lane >> 4) & 1, q = (lane >> 2) & 3, p = lane & 3;
    const int ha = (0x4ECA7530u >> (4 * ty)) & 15, hb = (0xBFD98621u >> (4 * ty)) & 15;
    const int rec = ((br * 16) * 64 + 4 * j) * 6144;
    const __amdgpu_buffer_rsrc_t rxs = rsrc(ws + WS_X, X_BYTES);

    if (ty < 7) {
        const u32x4 rx = srd(br ? xb : xa, XB), rw = srd(br ? wb : wa, WB);
        const int vx0 = r3 * 896 + ((u ^ (r3 >> 1)) << 4), vx1 = vx0 ^ 64;
        const int vw = r3 * 2688 + ((u ^ (((r3 >> 1) & 1) << 2)) << 4);
        int rot = ty + 2 * jj; if (rot >= 7) rot -= 7;
        const int sx = j * (128 * 896), sw = ty * 128;
        {
            const int st = lbase + RING + 2 * STAGE;
            dma16(rx, st + wave * 2048, vx0, sx + wave * 14336 + rot * 128);
            dma16(rx, st + wave * 2048 + 1024, vx1, sx + wave * 14336 + 7168 + rot * 128);
#pragma unroll
            for (int cb = 0; cb < 3; ++cb) dma16(rw, st + 16384 + cb * 8192 + wave * 1024, vw, sw + (rot * 64 + wave * 8) * 2688 + cb * 896);
        }
        const int wr = w4 >> 1, wc = w4 & 1;
        const int sp0 = (int)(0x04152630415263ull >> (4 * (2 * ty + wc))) & 15, sp1 = (int)(0x04152630415263ull >> (4 * (2 * ty + (wc ^ 1)))) & 15;
        char* tab = smem + w4 * 7168 + lane * 16;
        if (__builtin_expect(wave >= 4, 1)) {
            int ch = rot, sg = 2;
#pragma unroll 1
            for (int c = 1; c < 7; ++c) {
                ++ch; if (ch == 7) ch = 0;
                ++sg; if (sg == 3) sg = 0;
                const int st = lbase + RING + sg * STAGE;
#pragma unroll
                for (int i = 0; i < 4; ++i) dma16(rx, st + w4 * 4096 + i * 1024, (i & 1) ? vx1 : vx0, sx + (w4 * 4 + i) * 7168 + ch * 128);
#pragma unroll
                for (int cb = 0; cb < 3; ++cb)
#pragma unroll
                    for (int i = 0; i < 2; ++i) dma16(rw, st + 16384 + cb * 8192 + w4 * 2048 + i * 1024, vw, sw + (ch * 64 + w4 * 16 + i * 8) * 2688 + cb * 896);
                WAITV(10); BAR();
            }
            int g[3][2][2], l[3][2], g2[3][2], l2[3];
#pragma unroll
            for (int i = 0; i < 3; ++i) {
                const int sec = (3 * wc + i) >> 1, c0 = 8 * ty + 4 * ((wc + i) & 1), sp = (i & 1) ? sp1 : sp0;
                const int go = rec + wr * 12288 + (sec == 0 ? 4096 : (sec - 1) * 2048);
                const int lo = wr * 8192 + (sec == 0 ? 0 : 30720 + sec * 2048);
#pragma unroll
                for (int s = 0; s < 3; ++s) {
                    const int c = c0 + (s < 2 ? 2 * s + hh : sp), m = (c * 37) >> 8, r = c - 7 * m;
                    const int hd = 2 * m + (s < 2 ? (r > 3) : 1), slot = s < 2 ? (r > 3 ? r - 3 : r) : 0;
                    const int pos = slot * 512 + ((((t >> 2) ^ slot) << 2) + (t & 3)) * 16;
                    const bool on = s < 2 || hh == (sp & 1), own = hd == ha || hd == hb;
                    const int la = ((own && on) ? (hd == hb ? 49152 : 0) : 65536) + lo + pos;
                    if (s < 2) l[i][s] = la; else l2[i] = la;
#pragma unroll
                    for (int tt = 0; tt < 2; ++tt) {
                        const bool pub = sec != 0 && (br == 0 || (wr == 1 && tt == 1));
                        const int ga = (on && (pub || !own)) ? go + tt * 6144 + hd * (64 * 6144) + pos : OOB;
                        if (s < 2) g[i][tt][s] = ga; else g2[i][tt] = ga;
                    }
                }
                *(i32x4*)(tab + i * 2048) = (i32x4){g[i][0][0], g[i][0][1], g[i][1][0], g[i][1][1]};
                *(i32x4*)(tab + i * 2048 + 1024) = (i32x4){l[i][0], l[i][1], g2[i][0], g2[i][1]};
            }
            *(i32x4*)(tab + 6144) = (i32x4){l2[0], l2[1], l2[2], 0};
            asm volatile("s_waitcnt vmcnt(0) lgkmcnt(0)" ::: "memory"); BAR();
            BAR();
            const u32x4 rwo = srd(br ? wob : woa, OB);
            const int vwo = r3 * 896 + ((u ^ (((r3 >> 1) & 1) << 2)) << 4);
#pragma unroll 1
            for (int n = 12 + w4; n < 57; n += 4) dma16(rwo, lbase + WO_OFF + n * 1024, vwo, n * 7168 + ty * 128);
            BAR();
#pragma unroll 1
            for (int n = w4; n < 12; n += 4) dma16(rwo, lbase + WO_OFF + n * 1024, vwo, n * 7168 + ty * 128);
        } else {
            int xo[4], wo_[3];
#pragma unroll
            for (int ks = 0; ks < 4; ++ks) xo[ks] = (wr * 64 + t) * 128 + (((2 * ks + hh) ^ ((t >> 1) & 7)) << 4);
#pragma unroll
            for (int i = 0; i < 3; ++i) { const int ct = 3 * wc + i, cb = ct >> 1, half = ct & 1;
                wo_[i] = 16384 + cb * 8192 + (8 * hh + q) * 128 + ((((half ^ (q >> 1)) << 2) + blk * 2 + (p & 1)) << 4) + (p >> 1) * 8; }
            f32x16 acc[2][3] = {};
#pragma unroll
            for (int i = 0; i < 3; ++i) { PIN(acc[0][i]); PIN(acc[1][i]); }
            bf16x8 xf[2][2], wf[2][3];
            WAITV(0); BAR();
            STAMP(1); CST(20);
            int sg = 2;
            const char* st = smem + RING + 2 * STAGE;
            auto rd = [&](int f, int ks) {
#pragma unroll
                for (int i = 2; i >= 0; --i) {
                    wf[f][i] = tr_frag(st + wo_[i] + ks * 2048, st + wo_[i] + ks * 2048 + 512);
                    if (i == 2) { xf[f][0] = *(const bf16x8*)(st + xo[ks]); xf[f][1] = *(const bf16x8*)(st + xo[ks] + 4096); }
                }
            };
            auto mm = [&](int f) {
#pragma unroll
                for (int i = 2; i >= 0; --i)
#pragma unroll
                    for (int tt = 0; tt < 2; ++tt) acc[tt][i] = MFMA(wf[f][i], xf[f][tt], acc[tt][i]);
            };
#define MIX() do { SG(0x8, 1); SG(0x100, 2); SG(0x8, 1); SG(0x100, 2); SG(0x8, 1); SG(0x100, 2); SG(0x8, 1); SG(0x100, 2); SG(0x8, 2); SCHED(); } while (0)
            rd(0, 0);
            SCHED();
#pragma unroll 1
            for (int c = 0; c < 6; ++c) {
                rd(1, 1); mm(0); MIX();
                rd(0, 2); mm(1); MIX();
                rd(1, 3); mm(0); MIX();
                WAITL0(); BAR();
                ++sg; if (sg == 3) sg = 0;
                st = smem + RING + sg * STAGE;
                SCHED();
                rd(0, 0); mm(1); MIX();
            }
            i32x4 tb[7];
            rd(1, 1); mm(0); MIX();
            rd(0, 2); mm(1); MIX();
            rd(1, 3);
#pragma unroll
            for (int i = 0; i < 7; ++i) tb[i] = *(const i32x4*)(tab + i * 1024);
            mm(0);
            SG(0x8, 1); SG(0x100, 3); SG(0x8, 1); SG(0x100, 3); SG(0x8, 1); SG(0x100, 3); SG(0x8, 1); SG(0x100, 3); SG(0x8, 1); SG(0x100, 3); SG(0x8, 1); SCHED();
            WAITL0(); BAR();
            SCHED();
            mm(1);
            SCHED();
            STAMP(2); CST(21);
            const float qs = 0.18898223650461361f * 1.4426950408889634f;
#pragma unroll
            for (int i = 2; i >= 0; --i) {
                const int sp = (i & 1) ? sp1 : sp0;
                const unsigned padv = (wc && i) ? 0x00003F80u : 0u;
                if (i < 2 && wc == 0) { acc[0][i] *= qs; acc[1][i] *= qs; }
                u32x4 d[2][2];
#pragma unroll
                for (int tt = 0; tt < 2; ++tt)
#pragma unroll
                    for (int s = 0; s < 2; ++s) d[tt][s] = __builtin_bit_cast(u32x4, pack8(acc[tt][i], s));
                auto fix = [&](int s) {
                    const bool sh = hh == (sp & 1);
#pragma unroll
                    for (int tt = 0; tt < 2; ++tt) {
                        const u32x4 d2 = {padv, 0u, d[tt][s].z, d[tt][s].w};
                        __builtin_amdgcn_raw_buffer_store_b128(d2, rxs, tb[2 * i + 1][2 + tt], 0, 16);
                        *(u32x4*)(smem + tb[6][i] + tt * 4096) = d2;
                        d[tt][s].z = sh ? padv : d[tt][s].z; d[tt][s].w = sh ? 0u : d[tt][s].w;
                    }
                };
                if (sp < 2) fix(0); else if (sp < 4) fix(1);
#pragma unroll
                for (int tt = 0; tt < 2; ++tt)
#pragma unroll
                    for (int s = 0; s < 2; ++s) __builtin_amdgcn_raw_buffer_store_b128(d[tt][s], rxs, tb[2 * i][2 * tt + s], 0, 16);
#pragma unroll
                for (int tt = 0; tt < 2; ++tt)
#pragma unroll
                    for (int s = 0; s < 2; ++s) *(u32x4*)(smem + tb[2 * i + 1][s] + tt * 4096) = d[tt][s];
            }
            WAITL0();
            STAMP(3);
            BAR();
        }
    } else {
        u64 pc; unsigned pco;
        asm volatile("s_getpc_b64 %0\n.Lhere%=:\n\ts_mov_b32 %1, .Lhere%=-fused" : "=s"(pc), "=s"(pco));
        unsigned d0 = 0, d1 = 0, d2 = 0;
        if ((int)threadIdx.x < CODE_LINES) asm volatile("global_load_dword %0, %1, %2" : "=v"(d0) : "v"(threadIdx.x * 128), "s"(pc - pco) : "memory");
        const char* wo = br ? wob : woa;
        asm volatile("global_load_dword %0, %1, %2" : "=v"(d1) : "v"((jj * 784 + threadIdx.x) * 128), "s"(wo) : "memory");
        if (threadIdx.x < 272) asm volatile("global_load_dword %0, %1, %2" : "=v"(d2) : "v"((jj * 784 + 512 + threadIdx.x) * 128), "s"(wo) : "memory");
        asm volatile("s_waitcnt vmcnt(0)" : "+v"(d0), "+v"(d1), "+v"(d2) :: "memory");
    }
    STAMP(4);
    asm volatile("s_waitcnt vmcnt(0)" : "+v"(epoch) :: "memory");
    const u64 tag = epoch * 0x9E3779B97F4A7C15ull + 0x7F4A7C159E3779B9ull;
    if (wave < 4 && ty < 7 && lane == 0) __hip_atomic_store((gu64*)(ws + WS_F1 + (((br * 16 + j) * 8 + ty) * 16 + wave) * 8), tag, RLX);
    STAMP(5);
    SCHED();

    const int h = hs ? hb : ha;
    const int T = 4 * j + w4;
    const unsigned NEGB = 0xF149F2CAu;
    const int nb = ty == 7 ? 15 : (0xF63F30Fu >> (4 * ty)) & 15;
    const bool local = ty < 7 && (hs == 0 || nb == 15);
    int fo[2], vo[2];
#pragma unroll
    for (int s = 0; s < 2; ++s) fo[s] = hs * 49152 + (2 * s + hh) * 512 + ((((t >> 2) ^ (2 * s + hh)) << 2) + (t & 3)) * 16;
    { const int pc8 = (4 * blk + 2 * (p & 1) + (p >> 1) + (h & 1)) & 7, cc = pc8 >> 1;
#pragma unroll
      for (int tt = 0; tt < 2; ++tt) { vo[tt] = hs * 49152 + 2048 + cc * 512 + ((((2 * tt + hh) ^ cc) << 2) + q) * 16 + (pc8 & 1) * 8; PIN(vo[tt]); } }
    u32x4 qf[2];
    qf[0] = *(const u32x4*)(smem + w4 * 4096 + fo[0]); qf[1] = *(const u32x4*)(smem + w4 * 4096 + fo[1]);
    f32x16 sdg, co;
    u32x4 pm[2];
    {
        const unsigned lo = (2u << t) - 1u;
        const unsigned pat = br ? __builtin_rotateleft32(0x55540001u, t) : ~0u;
        const int nd = (int)(~(pat & lo) >> (4 * hh)), no = (int)(~(pat & ~lo) >> (4 * hh)), lm = (int)(lo >> (4 * hh));
#pragma unroll
        for (int r = 0; r < 16; ++r) {
            const int bit = (r & 3) + 8 * (r >> 2);
            sdg[r] = __builtin_bit_cast(float, __builtin_amdgcn_sbfe(nd, bit, 1) & NEGB);
            co[r] = __builtin_bit_cast(float, __builtin_amdgcn_sbfe(no, bit, 1) & NEGB);
            if (r & 1) pm[r >> 3][(r >> 1) & 3] = __builtin_amdgcn_perm(__builtin_amdgcn_sbfe(lm, bit, 1), __builtin_amdgcn_sbfe(lm, bit - 1, 1), 0x05040100u);
        }
        PIN(sdg); PIN(co); PIN(pm[0]); PIN(pm[1]);
    }
    auto kfrag = [&](int slot, int s) { return *(const bf16x8*)(smem + slot * 4096 + fo[s]); };
    auto vfrag = [&](int slot, int s) { const char* vb = smem + slot * 4096 + s * 256; return tr_frag(vb + vo[0], vb + vo[1]); };
    auto qk = [&](int slot, const f32x16& c) {
        f32x16 r = MFMA(kfrag(slot, 0), __builtin_bit_cast(bf16x8, qf[0]), c);
        return MFMA(kfrag(slot, 1), __builtin_bit_cast(bf16x8, qf[1]), r);
    };
    auto pv = [&](int slot, bf16x8 p0, bf16x8 p1, f32x16& o) { o = MFMA(vfrag(slot, 0), p0, o); o = MFMA(vfrag(slot, 1), p1, o); };
    f32x16 cm = {};
    auto pre = [&]() {
        sdg = qk(8 + w4, sdg);
        float m = fmx(sdg[0], sdg[1]);
#pragma unroll
        for (int r = 2; r < 16; r += 2) m = fmx(fmx(m, sdg[r]), sdg[r + 1]);
        m = -fmx(m, xhalf(m));
#pragma unroll
        for (int r = 0; r < 16; ++r) cm[r] = m;
        sdg += cm; co += cm;
        PIN(cm); PIN(sdg); PIN(co);
    };
    const int sl = (ty == 2 || ty == 5) ? 3 : 0;
    const int qpl = 98304 + (ty == 7 ? wave * 2048 : w4 * 512);
    const u32x4 rxx = srd(ws + WS_X, X_BYTES);
    const bool ok = j > hs;
    int vv = ok ? lane * 16 : OOB;
    int ra = ok ? rec + (w4 - 4 - 4 * hs + ha * 64) * 6144 : 0, rb = ok ? rec + (w4 - 4 - 4 * hs + hb * 64) * 6144 : 0, lb = lbase + (4 - 4 * hs + w4) * 4096;
    int rh = rec + (h * 64 + w4) * 6144, lh = lbase + hs * 49152 + w4 * 4096 + 32768;
    asm volatile("" : "+v"(vv), "+s"(ra), "+s"(rb), "+s"(lb), "+s"(rh), "+s"(lh));
#pragma unroll 1
    for (int ph = 0;; ++ph) {
        if (ph == (local ? 0 : 1)) {
            if (ph) {
#pragma unroll
                for (int s = 0; s < 2; ++s) if (ty == 7 || 2 * s + hh == sl) qf[s] = *(const u32x4*)(smem + qpl + (fo[s] & (ty == 7 ? 2047 : 511)));
            }
            pre();
        }
        if (ph) break;
        WAITL0();
        if (wave == 4) {
            const int bk = lane >> 4;
            const int pg = ty == 7 ? (0x5421 >> (4 * q)) & 15 : (q == 0 ? ty : (q == 1 ? nb : 15));
            const bool act = bk < (br ? 2 : 3) && j - bk >= 0 && pg != 15 && !(bk == 0 && pg == ty);
            poll(ws + WS_F1 + (((br * 16 + j - bk) * 8 + pg) * 16 + p) * 8, act, tag);
        }
        STAMP(6);
        BAR();
#pragma unroll 1
        for (int g = 0; g < 2; ++g) {
            if (!local && g == hs) {
                if (ty == 7) {
                    dma16_dev(rxx, lbase + qpl, lane * 16, rh + 4096);
                    dma16_dev(rxx, lbase + qpl + 1024, lane * 16, rh + 5120);
                    dma64_dev(rxx, lh, lane * 16, rh);
                } else if (lane < 32) {
                    dma16_dev(rxx, lbase + qpl, lane * 16, rh + 4096 + sl * 512);
                    dma16_dev(rxx, lh + sl * 512, lane * 16, rh + sl * 512);
                    dma16_dev(rxx, lh + 2048 + sl * 512, lane * 16, rh + 2048 + sl * 512);
                }
            }
            if (g == 0 && (br == 0 || wave == 3)) {
                dma64_dev(rxx, lb, vv, ra);
                dma64_dev(rxx, lb + 49152, vv, rb);
                WAITV(4);
            } else {
                WAITV(0);
            }
            STAMP(7);
            BAR();
            if (!hs) break;
        }
        STAMP(8);
        if (wave < 4) __builtin_amdgcn_s_setprio(1);
    }
    const int so = br ? 7 + w4 : w4;
    STAMP(10); CST(19);
    const bf16x8 q0 = __builtin_bit_cast(bf16x8, qf[0]), q1 = __builtin_bit_cast(bf16x8, qf[1]);
    auto ex = [&](f32x16& s) {
#pragma unroll
        for (int r = 0; r < 16; ++r) s[r] = __builtin_amdgcn_exp2f(s[r]);
    };
    f32x16 o;
    {
        const f32x16 zero = {};
        bf16x8 k0 = kfrag(w4 + 1, 0), k1 = kfrag(w4 + 1, 1);
        f32x16 sa = qk(so, co), sb;
        bf16x8 v0 = vfrag(8 + w4, 0), v1 = vfrag(8 + w4, 1), v2 = vfrag(so, 0), v3 = vfrag(so, 1);
        SCHED();
        sb = MFMA(k0, q0, cm); sb = MFMA(k1, q1, sb);
        k0 = kfrag(w4 + 2, 0); k1 = kfrag(w4 + 2, 1);
#pragma unroll
        for (int r = 0; r < 16; ++r) sa[r] = fmx(sa[r], sdg[r]);
        ex(sa);
        const u32x4 e0 = __builtin_bit_cast(u32x4, pack8(sa, 0)), e1 = __builtin_bit_cast(u32x4, pack8(sa, 1));
        o = MFMA(v0, __builtin_bit_cast(bf16x8, e0 & pm[0]), zero);
        o = MFMA(v2, __builtin_bit_cast(bf16x8, e0 & ~pm[0]), o);
        bf16x8 p0 = __builtin_bit_cast(bf16x8, e1 & pm[1]), p1 = __builtin_bit_cast(bf16x8, e1 & ~pm[1]);
        v0 = v1; v1 = v3;
        SCHED();
        CST(23);
        if (!hs) { WAITV(0); BAR(); }
        if (br == 0) {
#define STG(n, cur, nxt) do { \
                if (n < 7) { nxt = MFMA(k0, q0, cm); nxt = MFMA(k1, q1, nxt); } \
                o = MFMA(v0, p0, o); o = MFMA(v1, p1, o); \
                if (n < 6) { k0 = kfrag(w4 + n + 2, 0); k1 = kfrag(w4 + n + 2, 1); } \
                v0 = vfrag(w4 + n, 0); v1 = vfrag(w4 + n, 1); \
                ex(cur); p0 = pack8(cur, 0); p1 = pack8(cur, 1); \
                if (n < 7) { SG(0x8, 1); SG(0x400, 4); SG(0x8, 1); if (n < 6) SG(0x100, 2); SG(0x400, 4); } \
                SG(0x8, 1); SG(0x100, 2); SG(0x400, 4); SG(0x8, 1); SG(0x100, 2); SG(0x400, n < 7 ? 4 : 12); SG(0x2, 8); SCHED(); } while (0)
            STG(1, sb, sa); STG(2, sa, sb); STG(3, sb, sa); STG(4, sa, sb); STG(5, sb, sa); STG(6, sa, sb); STG(7, sb, sa);
        }
        o = MFMA(v0, p0, o); o = MFMA(v1, p1, o);
    }
    float l = xhalf(o[12]);
    l = hh ? o[12] : l;
    float big = fmx(__builtin_fabsf(o[0]), __builtin_fabsf(o[1]));
#pragma unroll
    for (int r = 2; r < 16; r += 2) big = fmx(fmx(big, __builtin_fabsf(o[r])), __builtin_fabsf(o[r + 1]));
    if (__builtin_expect(!__all(big < 1.0e36f), 0)) {
        const int nmid = br ? 0 : (T >= 7 ? 7 : T);
        f32x16 sol = qk(so, co);
        float m = 0.f;
#pragma unroll
        for (int r = 0; r < 16; ++r) { sol[r] = T < (br ? 1 : 8) ? sdg[r] : fmx(sol[r], sdg[r]); m = fmx(m, sol[r]); }
#pragma unroll 1
        for (int i = 1; i <= nmid; ++i) {
            const f32x16 s = qk(8 + w4 - i, cm);
#pragma unroll
            for (int r = 0; r < 16; ++r) m = fmx(m, s[r]);
        }
        m = fmx(m, xhalf(m));
        sol -= m; cm -= m;
        ex(sol);
        const u32x4 e0 = __builtin_bit_cast(u32x4, pack8(sol, 0)), e1 = __builtin_bit_cast(u32x4, pack8(sol, 1));
        o = (f32x16){};
        pv(8 + w4, __builtin_bit_cast(bf16x8, e0 & pm[0]), __builtin_bit_cast(bf16x8, e1 & pm[1]), o);
        pv(so, __builtin_bit_cast(bf16x8, e0 & ~pm[0]), __builtin_bit_cast(bf16x8, e1 & ~pm[1]), o);
#pragma unroll 1
        for (int i = 1; i <= nmid; ++i) {
            f32x16 s = qk(8 + w4 - i, cm);
            ex(s); pv(8 + w4 - i, pack8(s, 0), pack8(s, 1), o);
        }
        l = xhalf(o[12]);
        l = hh ? o[12] : l;
    }
    STAMP(11); CST(22);
    const __amdgpu_buffer_rsrc_t ros = rsrc(ws + WS_O, O_BYTES);
    const int orec = ((br * 64 + T) * 2 + hs) * 16384;
    const char* f2 = ws + WS_F2 + ((br * 64 + T) * 2 + hs) * 128;
    {
        const float inv = __builtin_amdgcn_rcpf(l);
        o[12] = hh ? 0.f : o[12];
        o = o * inv;
        __builtin_amdgcn_raw_buffer_store_b128(__builtin_bit_cast(u32x4, pack8(o, 0)), ros, lane * 16, orec + ty * 2048, 16);
        __builtin_amdgcn_raw_buffer_store_b128(__builtin_bit_cast(u32x4, pack8(o, 1)), ros, lane * 16 + 1024, orec + ty * 2048, 16);
    }
    WAITV(0);
    if (lane == 0) __hip_atomic_store((gu64*)(f2 + ty * 8), tag, RLX);
    __builtin_amdgcn_s_setprio(0);
    STAMP(13);
    SCHED();
    if (ty == 7) {
        if (b == 56) {
#pragma unroll 1
            for (int i = 0; i < 4; ++i) poll(ws + WS_F2 + (i * 64 + (threadIdx.x >> 3)) * 128 + u * 8, true, tag);
            BAR();
            if (threadIdx.x == 0) __hip_atomic_store((gu64*)ws, tag, RLX);
        }
        STAMP_FLUSH();
        return;
    }

    bf16x8 wf[16][2];
    {
        const unsigned tab = hs ? 0xBFD98621u : 0x4ECA7530u;
        const int wb0 = WO_OFF + (8 * hh + q) * 128 + ((blk * 2 + (p & 1)) << 4) + (p >> 1) * 8 + ((q >> 1) << 6), wb1 = wb0 ^ 64;
#pragma unroll
        for (int n = 0; n < 8; ++n) {
            const int row = ((tab >> (4 * n)) & 15) * (28 * 128);
            const char* a0 = smem + wb0 + row;
            const char* a1 = smem + wb1 + row;
#pragma unroll
            for (int s = 0; s < 2; ++s) { wf[2 * n + s][0] = tr_frag(a0 + s * 2048, a0 + s * 2048 + 512); wf[2 * n + s][1] = tr_frag(a1 + s * 2048, a1 + s * 2048 + 512); }
        }
    }
    STAMP(12);
    poll(lane < 8 ? f2 + lane * 8 : f2 + ((lane & 3) - w4) * 256 + ty * 8, lane < (hs ? 8 : 12), tag);
    STAMP(15);
    f32x16 acc[2];
    char* part = smem + w4 * 8192 + lane * 16;
    {
        u32x4 of[16];
#pragma unroll
        for (int ks = 0; ks < 16; ++ks) of[ks] = __builtin_amdgcn_raw_buffer_load_b128(ros, lane * 16 + (ks & 3) * 1024, orec + (ks >> 2) * 4096, 16);
        SCHED();
        STAMP(9);
        if (hs) {
            BAR();
#pragma unroll
            for (int i = 0; i < 8; ++i) { const f32x4 v = *(const f32x4*)(part + i * 1024); acc[i >> 2][4 * (i & 3)] = v[0]; acc[i >> 2][4 * (i & 3) + 1] = v[1]; acc[i >> 2][4 * (i & 3) + 2] = v[2]; acc[i >> 2][4 * (i & 3) + 3] = v[3]; }
        } else {
            acc[0] = (f32x16){}; acc[1] = (f32x16){};
        }
        STAMP(14);
#pragma unroll
        for (int ks = 0; ks < 16; ++ks)
#pragma unroll
            for (int i = 0; i < 2; ++i) acc[i] = MFMA(wf[ks][i], __builtin_bit_cast(bf16x8, of[ks]), acc[i]);
    }
    SCHED();
    STAMP(16);
    if (!hs) {
#pragma unroll
        for (int i = 0; i < 8; ++i) *(f32x4*)(part + i * 1024) = (f32x4){acc[i >> 2][4 * (i & 3)], acc[i >> 2][4 * (i & 3) + 1], acc[i >> 2][4 * (i & 3) + 2], acc[i >> 2][4 * (i & 3) + 3]};
        WAITL0(); BAR();
    } else {
        char* tb = smem + 32768 + w4 * 4096;
#pragma unroll
        for (int i = 0; i < 2; ++i)
#pragma unroll
            for (int s = 0; s < 2; ++s) *(bf16x8*)(tb + t * 128 + (((4 * i + 2 * s + hh) ^ (t & 7)) << 4)) = pack8(acc[i], s);
        WAITL0();
        const __amdgpu_buffer_rsrc_t rout = rsrc(out, 2048 * 1792);
#pragma unroll
        for (int i = 0; i < 4; ++i) {
            const u32x4 v = *(const u32x4*)(tb + (i * 8 + r3) * 128 + ((u ^ r3) << 4));
            __builtin_amdgcn_raw_buffer_store_b128(v, rout, r3 * 1792 + u * 16, (T * 32 + i * 8) * 1792 + br * 896 + ty * 128, 16);
        }
    }
    STAMP(17);
    STAMP_FLUSH();
}

extern "C" void kernel_launch(void* const* d_in, const int* in_sizes, int n_in, void* d_out, int out_size, void* d_ws, size_t ws_size, hipStream_t stream) {
    fused<<<256, 512, 0, stream>>>((const char*)d_in[0], (const char*)d_in[1], (const char*)d_in[2], (const char*)d_in[3], (const char*)d_in[4], (const char*)d_in[5], (char*)d_out, (char*)d_ws + (128u << 20));
}
```

```cpp
#include <hip/hip_runtime.h>
#include <hip/hip_bf16.h>

typedef __bf16 bf16x8 __attribute__((ext_vector_type(8)));
typedef short s16x4 __attribute__((ext_vector_type(4)));
typedef float f32x16 __attribute__((ext_vector_type(16)));
typedef float f32x8 __attribute__((ext_vector_type(8)));
typedef float f32x4 __attribute__((ext_vector_type(4)));
typedef unsigned u32x4 __attribute__((ext_vector_type(4)));
typedef unsigned u32x2 __attribute__((ext_vector_type(2)));
typedef unsigned long long u64;
typedef __attribute__((address_space(1))) u64 gu64;
#define LAS __attribute__((address_space(3)))
#define DI __device__ __forceinline__
#define MFMA(a, b, c) __builtin_amdgcn_mfma_f32_32x32x16_bf16((a), (b), (c), 0, 0, 0)
#define RLX __ATOMIC_RELAXED, __HIP_MEMORY_SCOPE_AGENT

constexpr int XB = 2048 * 896, WB = 448 * 2688, OB = 448 * 896;
constexpr int STAGE = 40960, RING = 40960, LA = 0, LW = 57344, LR = 114688, LDS_BYTES = 163840;
constexpr int WS_F1 = 4096, WS_F2 = 65536, WS_X = 1 << 20, XT = 168 * 512, X_BYTES = 2 * 64 * XT, WS_O = 16 << 20, O_BYTES = 2 * 64 * 16 * 2048;
constexpr int OOB = 0x40000000;
constexpr int CODE_LINES = 196;
#define ROT 2
#define PF0 2
constexpr int SPIN_MAX = 1 << 18;

#define STAMP(i)
#define CST(i)
#define STAMP_DECL
#define STAMP_FLUSH()

DI u32x4 srd(const void* p, unsigned bytes) { const u64 a = (u64)p; return (u32x4){(unsigned)a, (unsigned)(a >> 32) & 0xffffu, bytes, 0x00020000u}; }
DI __amdgpu_buffer_rsrc_t rsrc(const void* p, int bytes) { return __builtin_amdgcn_make_buffer_rsrc((void*)p, 0, bytes, 0x00020000); }
DI void dma16(u32x4 r, int lds, int voff, int soff) {
    asm volatile("s_mov_b32 m0, %0\n\ts_nop 0\n\tbuffer_load_dwordx4 %1, %2, %3 offen lds" :: "s"(lds), "v"(voff), "s"(r), "s"(soff) : "memory");
}
DI void dma16_dev(u32x4 r, int lds, int voff, int soff) {
    asm volatile("s_mov_b32 m0, %0\n\ts_nop 0\n\tbuffer_load_dwordx4 %1, %2, %3 offen sc1 lds" :: "s"(lds), "v"(voff), "s"(r), "s"(soff) : "memory");
}
DI void dma64_dev(u32x4 r, int lds, int voff, int soff) {
    asm volatile("s_mov_b32 m0, %0\n\ts_nop 0\n\tbuffer_load_dwordx4 %1, %2, %3 offen sc1 lds\n\tbuffer_load_dwordx4 %1, %2, %3 offen offset:1024 sc1 lds\n\t"
                 "buffer_load_dwordx4 %1, %2, %3 offen offset:2048 sc1 lds\n\tbuffer_load_dwordx4 %1, %2, %3 offen offset:3072 sc1 lds" :: "s"(lds), "v"(voff), "s"(r), "s"(soff) : "memory");
}
DI void dma32_dev(u32x4 r, int lds, int voff, int soff) {
    asm volatile("s_mov_b32 m0, %0\n\ts_nop 0\n\tbuffer_load_dwordx4 %1, %2, %3 offen sc1 lds\n\tbuffer_load_dwordx4 %1, %2, %3 offen offset:1024 sc1 lds"
                 :: "s"(lds), "v"(voff), "s"(r), "s"(soff) : "memory");
}
DI void store_dev(u32x4 v, __amdgpu_buffer_rsrc_t r, int voff, int soff) {
    __builtin_amdgcn_raw_buffer_store_b128(v, r, voff, soff, 16);
    asm volatile("s_nop 1" :: "v"(v) : "memory");
}
DI bf16x8 tr_frag(const char* a0, const char* a1) {
    s16x4 lo = __builtin_amdgcn_ds_read_tr16_b64_v4i16((LAS s16x4*)a0);
    s16x4 hi = __builtin_amdgcn_ds_read_tr16_b64_v4i16((LAS s16x4*)a1);
    return __builtin_bit_cast(bf16x8, __builtin_shufflevector(lo, hi, 0, 1, 2, 3, 4, 5, 6, 7));
}
DI bf16x8 pack8(const f32x16& v, int s) {
    f32x8 t = {v[8 * s], v[8 * s + 1], v[8 * s + 2], v[8 * s + 3], v[8 * s + 4], v[8 * s + 5], v[8 * s + 6], v[8 * s + 7]};
    return __builtin_convertvector(t, bf16x8);
}
DI float xhalf(float v) {
    const unsigned u = __builtin_bit_cast(unsigned, v);
    const auto r = __builtin_amdgcn_permlane32_swap(u, u, false, false);
    return __builtin_bit_cast(float, (threadIdx.x & 32) ? r[0] : r[1]);
}
#define WAITV(n) asm volatile("s_waitcnt vmcnt(" #n ")" ::: "memory")
#define WAITL0() asm volatile("s_waitcnt lgkmcnt(0)" ::: "memory")
#define BAR() do { asm volatile("" ::: "memory"); __builtin_amdgcn_s_barrier(); asm volatile("" ::: "memory"); } while (0)
#define SCHED() __builtin_amdgcn_sched_barrier(0)

#define SG(m, n) __builtin_amdgcn_sched_group_barrier(m, n, 0)
DI float fmx(float a, float b) { return __builtin_elementwise_maximum(a, b); }
typedef int i32x4 __attribute__((ext_vector_type(4)));
#define PIN(x) asm volatile("" : "+v"(x))
DI void raise(char* p, u64 tag) { (void)__hip_atomic_exchange((gu64*)p, tag, RLX); }
DI void poll(const char* p, bool active, u64 tag) {
    const u64 mask = __builtin_amdgcn_ballot_w64(active);
    if (mask == 0) return;
    u64 v, zero, sv; unsigned cnt = SPIN_MAX;
    asm volatile("s_mov_b64 %2, exec\n\ts_mov_b64 exec, %6\n\tv_mov_b64 %1, 0\n"
                 ".Lpl%=:\n\tglobal_atomic_add_x2 %0, %4, %1, off sc0\n\ts_waitcnt vmcnt(0)\n\tv_cmp_ne_u64 vcc, %5, %0\n\ts_cbranch_vccz .Lpd%=\n\t"
                 "s_sub_u32 %3, %3, 1\n\ts_cbranch_scc0 .Lpl%=\n"
                 ".Lpd%=:\n\ts_mov_b64 exec, %2"
                 : "=&v"(v), "=&v"(zero), "=&s"(sv), "+s"(cnt) : "v"(p), "s"(tag), "s"(mask) : "vcc", "scc", "memory");
}

extern "C" __global__ void __launch_bounds__(512, 2)
fused(const char* xa, const char* wa, const char* woa, const char* xb, const char* wb, const char* wob, char* out, char* ws) {
    __shared__ __attribute__((aligned(16))) char smem[LDS_BYTES];
    STAMP_DECL;
    STAMP(0);
    const int b = blockIdx.x, xcd = b & 7, idx = b >> 3;
#define PART 1
    const int br = xcd & 1, jj = idx >> 3, ty = idx & 7, j = (xcd >> 1) * 4 + jj;
    const int wave = __builtin_amdgcn_readfirstlane(threadIdx.x >> 6), lane = threadIdx.x & 63;
    const int r3 = lane >> 3, u = lane & 7;
    const int lbase = (int)(unsigned)(size_t)(LAS char*)smem;
    u64 tag = *(const __attribute__((address_space(4))) u64*)ws;
    auto make_tag = [&]() { asm volatile("" : "+s"(tag)); tag = tag * 0x9E3779B97F4A7C15ull + 0x7F4A7C159E3779B9ull; asm volatile("" : "+s"(tag)); };
    const int w4 = wave & 3;
    const int hh = lane >> 5, t = lane & 31, blk = (lane >> 4) & 1, q = (lane >> 2) & 3, p = lane & 3;
    const int ha = (0x4ECA7530u >> (4 * ty)) & 15, hb = (0xBFD98621u >> (4 * ty)) & 15;
    const int ca = (7 * ha) >> 1, cb_ = (7 * hb) >> 1;
    const int cs = min(ca, cb_);
    const __amdgpu_buffer_rsrc_t rxs = rsrc(ws + WS_X, X_BYTES);

    if (ty < 7) {
        const u32x4 rx = srd(br ? xb : xa, XB), rw = srd(br ? wb : wa, WB);
        const int vx0 = r3 * 896 + ((u ^ (r3 >> 1)) << 4), vx1 = vx0 ^ 64;
        const int vw = r3 * 2688 + ((u ^ (((r3 >> 1) & 1) << 2)) << 4);
        const int rot = ROT == 2 ? ((ty + j) & 1) * 4 : 0;
        const int sx = j * (128 * 896), sw = ty * 128;
        dma16(rx, lbase + RING + 2 * STAGE + wave * 2048, vx0, sx + wave * 14336 + rot * 128);
        dma16(rx, lbase + RING + 2 * STAGE + wave * 2048 + 1024, vx1, sx + wave * 14336 + 7168 + rot * 128);
#pragma unroll
        for (int cb = 0; cb < 3; ++cb) dma16(rw, lbase + RING + 2 * STAGE + 16384 + cb * 8192 + wave * 1024, vw, sw + (rot * 64 + wave * 8) * 2688 + cb * 896);
        if (__builtin_expect(wave >= 4, 1)) {
            int ch = rot, sg = 2;
#pragma unroll 1
            for (int c = 1; c < 7; ++c) {
                ++ch; if (ch == 7) ch = 0;
                ++sg; if (sg == 3) sg = 0;
                const int st = lbase + RING + sg * STAGE;
#pragma unroll
                for (int i = 0; i < 4; ++i) dma16(rx, st + w4 * 4096 + i * 1024, (i & 1) ? vx1 : vx0, sx + (w4 * 4 + i) * 7168 + ch * 128);
#pragma unroll
                for (int cb = 0; cb < 3; ++cb)
#pragma unroll
                    for (int i = 0; i < 2; ++i) dma16(rw, st + 16384 + cb * 8192 + w4 * 2048 + i * 1024, vw, sw + (ch * 64 + w4 * 16 + i * 8) * 2688 + cb * 896);
                WAITV(10); BAR();
            }
            make_tag();
            const int vwo = r3 * 896 + ((u ^ (((r3 >> 1) & 1) << 2)) << 4);
            const u32x4 rwa = srd(br ? woa : wob, OB);
#pragma unroll 1
            for (int n = w4; n < 40; n += 4) dma16(rwa, lbase + LA + n * 1024, vwo, n * 7168 + ty * 128);
            WAITV(10); BAR();
#pragma unroll 1
            for (int n = 40 + w4; n < 56; n += 4) dma16(rwa, lbase + LA + n * 1024, vwo, n * 7168 + ty * 128);
            BAR();
        } else {
            const int wr = w4 >> 1, wc = w4 & 1;
            int xo[4], wo_[3];
#pragma unroll
            for (int ks = 0; ks < 4; ++ks) xo[ks] = (wr * 64 + t) * 128 + (((2 * ks + hh) ^ ((t >> 1) & 7)) << 4);
#pragma unroll
            for (int i = 0; i < 3; ++i) { const int ct = 3 * wc + i, cb = ct >> 1, half = ct & 1;
                wo_[i] = 16384 + cb * 8192 + (8 * hh + q) * 128 + ((((half ^ (q >> 1)) << 2) + blk * 2 + (p & 1)) << 4) + (p >> 1) * 8; }
            int es[2], el[2][2];
#pragma unroll
            for (int s = 0; s < 2; ++s) {
                const int sw16 = ((((t >> 2) ^ (2 * s + hh)) << 2) + (t & 3)) * 16;
                es[s] = (2 * s + hh) * 512 + sw16; PIN(es[s]);
#pragma unroll
                for (int e = 0; e < 2; ++e) {
                    const int ps = 8 * ty + 4 * ((wc + e) & 1) + 2 * s + hh - (br ? cs : ca);
                    el[e][s] = (unsigned)ps < (br ? 8u : 4u) ? ps * 512 + sw16 : -1; PIN(el[e][s]);
                }
            }
            const unsigned nd = ((br ? 0x1189188u : 0xDDBDDBEu) >> (4 * ty)) & 15u;
            unsigned pb[2];
            pb[0] = (br ? nd * 0x111u : nd | 0xFF0u) >> (6 * wc);
            pb[1] = (wr ? nd | 0xFF0u : (br ? nd * 0x111u : nd | 0xFF0u)) >> (6 * wc);
            int g0 = (br * 64 + 4 * j + 2 * wr) * XT + ty * 4096 + wc * (56 * 512 + 2048);
            asm volatile("" : "+s"(pb[0]), "+s"(pb[1]), "+s"(g0));
            f32x16 acc[2][3] = {};
#pragma unroll
            for (int i = 0; i < 3; ++i) { PIN(acc[0][i]); PIN(acc[1][i]); }
            bf16x8 xf[2][2], wf[2][3];
            char* f1 = ws + WS_F1 + (((br * 16 + j) * 8 + ty) * 16 + wave) * 8;
            asm volatile("" : "+s"(f1));
            make_tag();
            WAITV(0); BAR();
            STAMP(1); CST(20);
            int sg = 2;
            const char* st = smem + RING + 2 * STAGE;
            auto rd = [&](int f, int ks) {
#pragma unroll
                for (int i = 2; i >= 0; --i) {
                    wf[f][i] = tr_frag(st + wo_[i] + ks * 2048, st + wo_[i] + ks * 2048 + 512);
                    if (i == 2) { xf[f][0] = *(const bf16x8*)(st + xo[ks]); xf[f][1] = *(const bf16x8*)(st + xo[ks] + 4096); }
                }
            };
            auto mm = [&](int f) {
#pragma unroll
                for (int i = 2; i >= 0; --i)
#pragma unroll
                    for (int tt = 0; tt < 2; ++tt) acc[tt][i] = MFMA(wf[f][i], xf[f][tt], acc[tt][i]);
            };
#define MIX() do { SG(0x8, 1); SG(0x100, 2); SG(0x8, 1); SG(0x100, 2); SG(0x8, 1); SG(0x100, 2); SG(0x8, 1); SG(0x100, 2); SG(0x8, 2); SCHED(); } while (0)
            rd(0, 0);
            SCHED();
#pragma unroll 1
            for (int c = 0; c < 7; ++c) {
                rd(1, 1); mm(0); MIX();
                rd(0, 2); mm(1); MIX();
                rd(1, 3); mm(0); MIX();
                WAITL0(); BAR();
                ++sg; if (sg == 3) sg = 0;
                st = smem + RING + sg * STAGE;
                SCHED();
                rd(0, 0); mm(1); MIX();
            }
            STAMP(2); CST(21);
            const float qs = 0.18898223650461361f * 1.4426950408889634f;
            u32x4 d[3][2][2];
#pragma unroll
            for (int i = 2; i >= 0; --i) {
                const int go = g0 + (i == 0 ? 0 : (i == 2 ? 56 * 512 : (wc ? 56 * 512 - 2048 : 2048)));
                if (i < 2 && wc == 0) { acc[0][i] *= qs; acc[1][i] *= qs; }
#pragma unroll
                for (int tt = 0; tt < 2; ++tt)
#pragma unroll
                    for (int s = 0; s < 2; ++s) {
                        d[i][tt][s] = __builtin_bit_cast(u32x4, pack8(acc[tt][i], s));
                        if ((pb[tt] >> (2 * i + s)) & 1) __builtin_amdgcn_raw_buffer_store_b128(d[i][tt][s], rxs, es[s], go + tt * XT, 16);
                    }
            }
            STAMP(3);
#pragma unroll
            for (int i = 0; i < 3; ++i) {
                if (i == 2 && br) { WAITV(0); if (lane == 0) raise(f1, tag); }
                const int sec = (3 * wc + i) >> 1;
                const int ts = (br && sec) ? 8192 : 4096;
                const int lo = (br ? LW + (sec == 0 ? 40960 : 4096 + sec * 4096) : LR + (sec == 0 ? 0 : 30720 + sec * 2048)) + 2 * wr * ts;
#pragma unroll
                for (int s = 0; s < 2; ++s)
                    if (el[i & 1][s] >= 0) {
                        *(u32x4*)(smem + el[i & 1][s] + lo) = d[i][0][s];
                        *(u32x4*)(smem + el[i & 1][s] + lo + ts) = d[i][1][s];
                    }
            }
            if (!br) { WAITV(0); if (lane == 0) raise(f1, tag); }
        }
    } else {
        u64 pc; unsigned pco;
        asm volatile("s_getpc_b64 %0\n.Lhere%=:\n\ts_mov_b32 %1, .Lhere%=-fused" : "=s"(pc), "=s"(pco));
        unsigned d0 = 0;
        if ((int)threadIdx.x < CODE_LINES) asm volatile("global_load_dword %0, %1, %2" : "+v"(d0) : "v"(threadIdx.x * 128), "s"(pc - pco) : "memory");
        const int tl = (jj * 8 + wave) * 58 + (lane < 58 ? lane : 57);
        const char* tp = tl < 512 ? (br ? xb : xa) + ((xcd >> 1) * 512 + tl) * 896 : (br ? wb : wa) + (tl - 512) * 128;
        const int ts = tl < 512 ? 128 : 172032;
        if (PART == 1 && PF0 < 7) {
            asm volatile("global_load_dword %0, %1, off" : "+v"(d0) : "v"(tp + 2 * ts) : "memory");
            asm volatile("global_load_dword %0, %1, off" : "+v"(d0) : "v"(tp + 6 * ts) : "memory");
            asm volatile("global_load_dword %0, %1, off" : "+v"(d0) : "v"(tp + 3 * ts) : "memory");
        }
        const int wl = (jj * 784 + (threadIdx.x < 392 ? threadIdx.x : 0)) * 128;
        asm volatile("global_load_dword %0, %1, %2" : "+v"(d0) : "v"(wl), "s"(woa) : "memory");
        asm volatile("global_load_dword %0, %1, %2" : "+v"(d0) : "v"(wl + 392 * 128), "s"(woa) : "memory");
        if (br == 0 || PART != 1) {
            asm volatile("global_load_dword %0, %1, %2" : "+v"(d0) : "v"(wl), "s"(wob) : "memory");
            asm volatile("global_load_dword %0, %1, %2" : "+v"(d0) : "v"(wl + 392 * 128), "s"(wob) : "memory");
        }
        asm volatile("s_waitcnt vmcnt(0)" : "+v"(d0) :: "memory");
        make_tag();
    }
    STAMP(4);
    SCHED();

    const bool aux = wave >= 4;
    const int T = 4 * j + w4;
    const unsigned NEGB = 0xF149F2CAu;
    f32x16 cd, co;
    u32x4 pm[2];
    auto masks = [&](bool dil) {
        const unsigned lo = (2u << t) - 1u;
        const unsigned pat = dil ? __builtin_rotateleft32(0x55540001u, t) : ~0u;
        const int nd = (int)(~(pat & lo) >> (4 * hh)), no = T < (dil ? 1 : 8) ? -1 : (int)(~(pat & ~lo) >> (4 * hh)), lm = (int)(lo >> (4 * hh));
#pragma unroll
        for (int r = 0; r < 16; ++r) {
            const int bit = (r & 3) + 8 * (r >> 2);
            cd[r] = __builtin_bit_cast(float, __builtin_amdgcn_sbfe(nd, bit, 1) & NEGB);
            co[r] = __builtin_bit_cast(float, __builtin_amdgcn_sbfe(no, bit, 1) & NEGB);
            if (r & 1) pm[r >> 3][(r >> 1) & 3] = __builtin_amdgcn_perm(__builtin_amdgcn_sbfe(lm, bit, 1), __builtin_amdgcn_sbfe(lm, bit - 1, 1), 0x05040100u);
        }
        PIN(cd); PIN(co); PIN(pm[0]); PIN(pm[1]);
    };
    auto koff = [&](int c0, int s) { return (2 * s + hh) * 512 + ((((t >> 2) ^ ((c0 + 2 * s + hh) & 3)) << 2) + (t & 3)) * 16; };
    auto voff = [&](int h, int c0, int tt) {
        const int pc8 = (4 * blk + 2 * (p & 1) + (p >> 1) + (h & 1)) & 7, cc = pc8 >> 1;
        return cc * 512 + ((((2 * tt + hh) ^ ((c0 + cc) & 3)) << 2) + q) * 16 + (pc8 & 1) * 8;
    };
    auto qfix = [&](int h, u32x4& q0, u32x4& q1) {
        const bool z0 = (h & 1) && !hh, z1 = !(h & 1) && hh;
        q0.x = z0 ? 0u : q0.x; q0.y = z0 ? 0u : q0.y; q1.z = z1 ? 0u : q1.z; q1.w = z1 ? 0u : q1.w;
    };
    auto ex = [&](f32x16& s) {
#pragma unroll
        for (int r = 0; r < 16; ++r) s[r] = __builtin_amdgcn_exp2f(s[r]);
    };
    auto rowmax = [&](const f32x16& s) {
        float m = fmx(s[0], s[1]);
#pragma unroll
        for (int r = 2; r < 16; r += 2) m = fmx(fmx(m, s[r]), s[r + 1]);
        return fmx(m, xhalf(m));
    };
    const unsigned one2 = (((lane >> 4) ^ (lane >> 2)) & 1) ? 0u : 0x3F803F80u;
    const bf16x8 sel = __builtin_bit_cast(bf16x8, (u32x4){one2, one2, one2, one2});
#define ROWSUM(pp, l) l = __builtin_amdgcn_mfma_f32_16x16x32_bf16(sel, pp, l, 0, 0, 0)
    const __amdgpu_buffer_rsrc_t ros = rsrc(ws + WS_O, O_BYTES);
    auto put_o = [&](f32x16 o, float l, int rec) {
        o = o * __builtin_amdgcn_rcpf(l);
        u32x4 o1 = __builtin_bit_cast(u32x4, pack8(o, 1));
        o1.z = hh ? 0u : o1.z; o1.w = hh ? 0u : o1.w;
        store_dev(__builtin_bit_cast(u32x4, pack8(o, 0)), ros, lane * 16, rec);
        store_dev(o1, ros, lane * 16 + 1024, rec);
    };
    const int nb = ty == 7 ? 15 : (0xF63F30Fu >> (4 * ty)) & 15;
    const u32x4 rxx = srd(ws + WS_X, X_BYTES);
    const __amdgpu_buffer_rsrc_t rout = rsrc(out, 2048 * 1792);
    const int wb0 = (8 * hh + q) * 128 + ((blk * 2 + (p & 1)) << 4) + (p >> 1) * 8 + ((q >> 1) << 6), wb1 = wb0 ^ 64;

    const bool local = br == 0 && ty < 7;
    const int h = br ? hb : ha, c0 = br ? cb_ : ca;
    const int rh = T * XT + c0 * 512, lh = lbase + LR + w4 * 4096;
    auto wait_a = [&]() {
        const int bk = lane >> 3, pg = ty == 7 ? 1 + 3 * br + (q & 1) : ((q & 1) ? (br ? nb : 15) : ty);
        const bool act = bk < 3 && j - bk >= 0 && pg != 15 && !(bk == 0 && local) && (p >> 1) == (w4 >> 1);
        poll(ws + WS_F1 + (((j - bk) * 8 + pg) * 16 + p) * 8, act, tag);
    };
    auto fetch = [&](int back) {
        const int v = j >= back ? lane * 16 : OOB, r = j >= back ? rh - back * 4 * XT : 0, l = lh + (2 - back) * 16384;
        dma32_dev(rxx, l, v, r + 56 * 512); dma32_dev(rxx, l + 2048, v, r + 112 * 512);
    };
    auto ones = [&](int back, bool two) {
        const int bb = back + (two ? hh : 0);
        if (two || !hh) *(u32x2*)(smem + LR + w4 * 4096 + (2 - bb) * 16384 + 2048 + ((h & 1) ? 0 : 1544) + t * 16) = (u32x2){j >= bb ? 0x3F80u : 0u, 0u};
    };

    if (aux) {
        if (!local) {
            masks(true);
            const int hd[2] = {ty == 7 ? h : ha, ty == 7 ? h : hb}, cf[2] = {ty == 7 ? c0 : ca, ty == 7 ? c0 : cb_};
            const int bk = lane >> 4;
            const int pg = q > 1 ? 15 : (ty == 7 ? 1 + 3 * br + q : (q ? nb : ty));
            const bool act = bk < (w4 == 0 ? 2 : 1) && j - bk >= 0 && pg != 15 && !(bk == 0 && pg == ty);
            const int sl = (ty == 2 || ty == 5) ? 3 : 0;
            int fo[2][2], vo[2][2], po[2];
#pragma unroll
            for (int e = 0; e < 2; ++e) {
                po[e] = ty == 7 ? 0 : (cf[e] - cs) * 512;
#pragma unroll
                for (int s = 0; s < 2; ++s) { fo[e][s] = koff(cf[e], s); vo[e][s] = voff(hd[e], cf[e], s) + 4096 + LW + w4 * 8192 + po[e]; PIN(fo[e][s]); PIN(vo[e][s]); }
            }
            const char* f1 = ws + WS_F1 + (((16 + j - bk) * 8 + pg) * 16 + p) * 8;
            char* f2 = ws + WS_F2 + (64 + T) * 128 + (lane ? hd[1] : hd[0]) * 8;
            PIN(f1); PIN(f2);
            STAMP(5);
            __builtin_amdgcn_s_setprio(2);
            poll(f1, act, tag);
            STAMP(7);
            const int rt = (64 + T) * XT, lt = lbase + LW + w4 * 8192;
            if (ty == 7) {
                dma32_dev(rxx, lt + 8192, lane * 16, rt + (56 + c0) * 512); dma32_dev(rxx, lt + 12288, lane * 16, rt + (112 + c0) * 512);
                dma32_dev(rxx, lt, T ? lane * 16 : OOB, T ? rt - XT + (56 + c0) * 512 : 0); dma32_dev(rxx, lt + 4096, T ? lane * 16 : OOB, T ? rt - XT + (112 + c0) * 512 : 0);
            } else {
                if (w4 == 0) { dma64_dev(rxx, lt, T ? lane * 16 : OOB, T ? rt - XT + (56 + cs) * 512 : 0); dma64_dev(rxx, lt + 4096, T ? lane * 16 : OOB, T ? rt - XT + (112 + cs) * 512 : 0); }
                if (nb != 15 && lane < 32) {
                    const int cc = (cb_ + sl) * 512, l0 = lt + po[1] + sl * 512;
                    dma16_dev(rxx, l0 + 8192, lane * 16, rt + 56 * 512 + cc);
                    dma16_dev(rxx, l0 + 12288, lane * 16, rt + 112 * 512 + cc);
                    if (w4) { dma16_dev(rxx, l0, lane * 16, rt - XT + 56 * 512 + cc); dma16_dev(rxx, l0 + 4096, lane * 16, rt - XT + 112 * 512 + cc); }
                }
            }
            u32x4 qf[2][2], qp[2];
#pragma unroll
            for (int s = 0; s < 2; ++s) qp[s] = __builtin_amdgcn_raw_buffer_load_b128(rxs, (ty == 7 || (nb != 15 && 2 * s + hh == sl)) ? fo[1][s] : OOB, rt + cf[1] * 512, 16);
            WAITV(0);
            STAMP(6);
            BAR();
            STAMP(8);
#pragma unroll
            for (int e = 0; e < 2; ++e)
#pragma unroll
                for (int s = 0; s < 2; ++s) {
                    qf[e][s] = *(const u32x4*)(smem + LW + 40960 + w4 * 4096 + po[e] + fo[e][s]);
                    if (ty == 7 || (e && nb != 15 && 2 * s + hh == sl)) qf[e][s] = qp[s];
                }
            f32x16 sd[2], so[2], o[2]; f32x4 l[2];
            bf16x8 kf[2][4], vf[2][4];
#pragma unroll
            for (int e = 0; e < 2; ++e) {
                const char* kb = smem + LW + w4 * 8192 + po[e];
                kf[e][0] = *(const bf16x8*)(kb + 8192 + fo[e][0]); kf[e][1] = *(const bf16x8*)(kb + fo[e][0]);
                kf[e][2] = *(const bf16x8*)(kb + 8192 + fo[e][1]); kf[e][3] = *(const bf16x8*)(kb + fo[e][1]);
                qfix(hd[e], qf[e][0], qf[e][1]);
            }
            SCHED();
#pragma unroll
            for (int e = 0; e < 2; ++e) {
                const bf16x8 q0 = __builtin_bit_cast(bf16x8, qf[e][0]), q1 = __builtin_bit_cast(bf16x8, qf[e][1]);
                sd[e] = MFMA(kf[e][0], q0, cd); so[e] = MFMA(kf[e][1], q0, co);
                sd[e] = MFMA(kf[e][2], q1, sd[e]); so[e] = MFMA(kf[e][3], q1, so[e]);
                const char* v0 = smem + vo[e][0]; const char* v1 = smem + vo[e][1];
                vf[e][0] = tr_frag(v0 + 8192, v1 + 8192); vf[e][1] = tr_frag(v0, v1); vf[e][2] = tr_frag(v0 + 8448, v1 + 8448); vf[e][3] = tr_frag(v0 + 256, v1 + 256);
                SCHED();
            }
#pragma unroll
            for (int e = 0; e < 2; ++e) {
#pragma unroll
                for (int r = 0; r < 16; ++r) sd[e][r] = fmx(sd[e][r], so[e][r]);
                sd[e] -= rowmax(sd[e]);
                ex(sd[e]);
                const u32x4 e0 = __builtin_bit_cast(u32x4, pack8(sd[e], 0)), e1 = __builtin_bit_cast(u32x4, pack8(sd[e], 1));
                l[e] = (f32x4){}; o[e] = (f32x16){};
                ROWSUM(__builtin_bit_cast(bf16x8, e0), l[e]); ROWSUM(__builtin_bit_cast(bf16x8, e1), l[e]);
                o[e] = MFMA(vf[e][0], __builtin_bit_cast(bf16x8, e0 & pm[0]), o[e]);
                o[e] = MFMA(vf[e][1], __builtin_bit_cast(bf16x8, e0 & ~pm[0]), o[e]);
                o[e] = MFMA(vf[e][2], __builtin_bit_cast(bf16x8, e1 & pm[1]), o[e]);
                o[e] = MFMA(vf[e][3], __builtin_bit_cast(bf16x8, e1 & ~pm[1]), o[e]);
            }
            put_o(o[0], l[0][0], ((64 + T) * 16 + hd[0]) * 2048);
            put_o(o[1], l[1][0], ((64 + T) * 16 + hd[1]) * 2048);
            STAMP(11);
            WAITV(0);
            if (lane < 2) raise(f2, tag);
            STAMP(13);
            __builtin_amdgcn_s_setprio(0);
            if (ty == 7 && br == 0 && j == 0 && wave == 4) {
                poll(ws + WS_F2 + (4 * j + lane) * 128 + h * 8, lane < 4, tag);
                __builtin_amdgcn_s_sleep(16);
#pragma unroll 1
                for (int n = 0; n < SPIN_MAX; ++n) {
                    u64 v[16];
                    const u64 zero = 0;
#pragma unroll
                    for (int i = 0; i < 16; ++i) asm volatile("global_atomic_add_x2 %0, %1, %2, %3 offset:%4 sc0" : "=&v"(v[i]) : "v"(lane * 8 + (i >> 3) * 4096), "v"(zero), "s"(ws + WS_F2), "i"((i & 7) * 512) : "memory");
                    asm volatile("s_waitcnt vmcnt(0)" : "+v"(v[0]), "+v"(v[1]), "+v"(v[2]), "+v"(v[3]), "+v"(v[4]), "+v"(v[5]), "+v"(v[6]), "+v"(v[7]),
                                 "+v"(v[8]), "+v"(v[9]), "+v"(v[10]), "+v"(v[11]), "+v"(v[12]), "+v"(v[13]), "+v"(v[14]), "+v"(v[15]) :: "memory");
                    bool ok = true;
#pragma unroll
                    for (int i = 0; i < 16; ++i) ok &= v[i] == tag;
                    if (__all(ok)) break;
                    __builtin_amdgcn_s_sleep(8);
                }
                if (lane == 0) __hip_atomic_store((gu64*)ws, tag, RLX);
            }
        } else {
            WAITV(0); BAR(); BAR();
            const char* a0 = smem + LA + wb0;
            const char* a1 = smem + LA + wb1;
            bf16x8 w[32][2];
            auto rdw = [&](int ks) { const int row = (ks >> 1) * (28 * 128) + (ks & 1) * 2048; w[ks][0] = tr_frag(a0 + row, a0 + row + 512); w[ks][1] = tr_frag(a1 + row, a1 + row + 512); };
            STAMP(6);
            wait_a();
            STAMP(7);
            fetch(1); fetch(2);
#pragma unroll
            for (int ks = 0; ks < 16; ++ks) rdw(ks);
            WAITV(4); ones(1, false); WAITL0();
            STAMP(8);
            BAR();
            WAITV(0); ones(2, false); WAITL0();
            {
                const int vwo = r3 * 896 + ((u ^ (((r3 >> 1) & 1) << 2)) << 4);
                const u32x4 rwa = srd(woa, OB);
#pragma unroll 1
                for (int n = w4; n < 56; n += 4) { dma16(rwa, lbase + LW + n * 1024, vwo, n * 7168 + ty * 128); if ((n >> 2) == 1) BAR(); }
            }
            STAMP(14);
            poll(ws + WS_F2 + (64 + T) * 128 + lane * 8, lane < 16, tag);
            STAMP(15);
            f32x16 acc[2] = {};
            u32x4 of[32];
            auto ld = [&](int ks) { of[ks] = __builtin_amdgcn_raw_buffer_load_b128(ros, lane * 16 + (ks & 3) * 1024, (64 + T) * 32768 + (ks >> 2) * 4096, 16); };
#pragma unroll
            for (int ks = 0; ks < 12; ++ks) { ld(ks); SCHED(); }
            STAMP(9);
#pragma unroll
            for (int ks = 0; ks < 32; ++ks) {
                acc[0] = MFMA(w[ks][0], __builtin_bit_cast(bf16x8, of[ks]), acc[0]);
                acc[1] = MFMA(w[ks][1], __builtin_bit_cast(bf16x8, of[ks]), acc[1]);
                if (ks + 12 < 32) ld(ks + 12);
                if (ks + 16 < 32) rdw(ks + 16);
                SCHED();
            }
            STAMP(16);
            BAR();
            char* tb = smem + LR + 32768 + w4 * 4096;
#pragma unroll
            for (int i = 0; i < 2; ++i)
#pragma unroll
                for (int s = 0; s < 2; ++s) *(bf16x8*)(tb + t * 128 + (((4 * i + 2 * s + hh) ^ (t & 7)) << 4)) = pack8(acc[i], s);
            WAITL0();
#pragma unroll
            for (int i = 0; i < 4; ++i) {
                const u32x4 v = *(const u32x4*)(tb + (i * 8 + r3) * 128 + ((u ^ r3) << 4));
                store_dev(v, rout, r3 * 1792 + u * 16, (T * 32 + i * 8) * 1792 + 896 + ty * 128);
            }
            STAMP(17);
        }
        STAMP_FLUSH();
        return;
    }

    LAS char* wd = (LAS char*)(smem + LW + 40960 + 7 * 512 + 8);
    if (!local && lane < 3) *(volatile LAS u64*)(wd + lane * 64 + w4 * 16) = 0;
    int fo[2] = {koff(c0, 0), koff(c0, 1)};
    const char* tl = smem + LR + w4 * 4096;
    u32x4 qf[2];
    if (local) {
        WAITL0(); BAR();
        qf[0] = *(const u32x4*)(tl + fo[0]); qf[1] = *(const u32x4*)(tl + fo[1]);
        ones(0, false);
        WAITL0(); BAR();
    }
    STAMP(5);
    SCHED();
    masks(false);
    int vo[2] = {voff(h, c0, 0) + 2048, voff(h, c0, 1) + 2048};
    PIN(vo[0]); PIN(vo[1]);
    auto kfrag = [&](int n, int s) { return *(const bf16x8*)(tl + (8 - n) * 4096 + fo[s]); };
    auto vfrag = [&](int n, int s) { const char* vb = tl + (8 - n) * 4096 + s * 256; return tr_frag(vb + vo[0], vb + vo[1]); };
    auto qk = [&](int n, const f32x16& c) {
        f32x16 r = MFMA(kfrag(n, 0), __builtin_bit_cast(bf16x8, qf[0]), c);
        return MFMA(kfrag(n, 1), __builtin_bit_cast(bf16x8, qf[1]), r);
    };
    auto pv = [&](int n, bf16x8 p0, bf16x8 p1, f32x16& o) { o = MFMA(vfrag(n, 0), p0, o); o = MFMA(vfrag(n, 1), p1, o); };
    auto meet = [&](int n) {
        *(volatile LAS u64*)(wd + n * 64 + w4 * 16) = tag | 1;
#pragma unroll 1
        for (int i = 0; i < SPIN_MAX; ++i) if (__all(*(volatile LAS u64*)(wd + n * 64 + p * 16) == (tag | 1))) break;
        asm volatile("" ::: "memory");
    };
    if (!local) {
        WAITL0(); BAR();
        STAMP(6);
        wait_a();
        STAMP(7);
        asm volatile("buffer_load_dwordx4 %0, %2, %4, %5 offen sc1\n\tbuffer_load_dwordx4 %1, %3, %4, %5 offen sc1"
                     : "=&v"(qf[0]), "=&v"(qf[1]) : "v"(fo[0]), "v"(fo[1]), "s"(rxx), "s"(rh) : "memory");
        fetch(0); fetch(1); fetch(2);
        asm volatile("s_waitcnt vmcnt(8)" : "+v"(qf[0]), "+v"(qf[1]) :: "memory");
    }
    __builtin_amdgcn_s_setprio(1);
    f32x16 cm;
    {
        qfix(h, qf[0], qf[1]);
        cd = qk(0, cd);
        const float m = -rowmax(cd);
#pragma unroll
        for (int r = 0; r < 16; ++r) cm[r] = m;
        cd += cm; co += cm;
        PIN(cm); PIN(cd); PIN(co);
    }
    STAMP(8);
    if (local) BAR(); else { WAITV(4); ones(0, true); WAITL0(); meet(0); }
    STAMP(10); CST(19);
    const bf16x8 q0 = __builtin_bit_cast(bf16x8, qf[0]), q1 = __builtin_bit_cast(bf16x8, qf[1]);
    f32x16 o = {};
    {
        bf16x8 k0, k1, v0, v1, p0, p1;
        f32x16 sa = qk(1, cm), sb;
        k0 = kfrag(2, 0); k1 = kfrag(2, 1);
        SCHED();
#define STG(n, nn, cur, nxt, cin) do { \
            nxt = MFMA(k0, q0, cin); nxt = MFMA(k1, q1, nxt); \
            if (n > 1) { o = MFMA(v0, p0, o); o = MFMA(v1, p1, o); } \
            if (nn) { k0 = kfrag(nn, 0); k1 = kfrag(nn, 1); } \
            v0 = vfrag(n, 0); v1 = vfrag(n, 1); \
            ex(cur); p0 = pack8(cur, 0); p1 = pack8(cur, 1); \
            SG(0x8, 1); SG(0x400, 4); SG(0x8, 1); if (nn) SG(0x100, 2); SG(0x400, 4); SG(0x2, 2); \
            if (n > 1) SG(0x8, 1); SG(0x100, 2); SG(0x400, 4); SG(0x2, 2); if (n > 1) SG(0x8, 1); SG(0x100, 2); SG(0x400, 4); SG(0x2, 4); SCHED(); } while (0)
        STG(1, 3, sa, sb, cm);
        STG(2, 4, sb, sa, cm);
        PIN(p0); PIN(p1);
        if (local) BAR(); else { WAITV(0); ones(2, false); WAITL0(); meet(1); }
        STG(3, 8, sa, sb, cm);
        STG(4, 5, sb, sa, co);
        {
            sb = MFMA(k0, q0, cm); sb = MFMA(k1, q1, sb);
            o = MFMA(v0, p0, o); o = MFMA(v1, p1, o);
            k0 = kfrag(6, 0); k1 = kfrag(6, 1);
            const bf16x8 va = vfrag(0, 0), vb = vfrag(8, 0);
            v0 = vfrag(0, 1); v1 = vfrag(8, 1);
#pragma unroll
            for (int r = 0; r < 16; ++r) sa[r] = fmx(sa[r], cd[r]);
            ex(sa);
            const u32x4 e0 = __builtin_bit_cast(u32x4, pack8(sa, 0)), e1 = __builtin_bit_cast(u32x4, pack8(sa, 1));
            o = MFMA(va, __builtin_bit_cast(bf16x8, e0 & pm[0]), o);
            o = MFMA(vb, __builtin_bit_cast(bf16x8, e0 & ~pm[0]), o);
            p0 = __builtin_bit_cast(bf16x8, e1 & pm[1]); p1 = __builtin_bit_cast(bf16x8, e1 & ~pm[1]);
            SG(0x8, 1); SG(0x2, 8); SG(0x8, 1); SG(0x100, 6); SG(0x400, 4); SG(0x8, 1); SG(0x100, 2); SG(0x400, 4); SG(0x8, 1); SG(0x100, 2); SG(0x2, 16);
            SG(0x8, 1); SG(0x2, 4); SG(0x400, 4); SG(0x8, 1); SG(0x400, 4); SG(0x2, 12); SCHED();
        }
        STG(5, 7, sb, sa, cm);
        STG(6, 0, sa, sb, cm);
        o = MFMA(v0, p0, o); o = MFMA(v1, p1, o);
        v0 = vfrag(7, 0); v1 = vfrag(7, 1);
        ex(sb); p0 = pack8(sb, 0); p1 = pack8(sb, 1);
        o = MFMA(v0, p0, o); o = MFMA(v1, p1, o);
        SG(0x8, 1); SG(0x100, 2); SG(0x400, 4); SG(0x8, 1); SG(0x100, 2); SG(0x400, 4); SG(0x2, 4); SG(0x8, 1); SG(0x400, 8); SG(0x2, 4); SG(0x8, 1); SCHED();
    }
    float big = fmx(__builtin_fabsf(o[0]), __builtin_fabsf(o[1]));
#pragma unroll
    for (int r = 2; r < 16; r += 2) big = fmx(fmx(big, __builtin_fabsf(o[r])), __builtin_fabsf(o[r + 1]));
    if (__builtin_expect(!__all(big < 1.0e36f), 0)) {
        const int nmid = T >= 7 ? 7 : T;
        f32x16 sol = qk(8, co);
#pragma unroll
        for (int r = 0; r < 16; ++r) sol[r] = fmx(sol[r], cd[r]);
        float m = rowmax(sol);
#pragma unroll 1
        for (int i = 1; i <= nmid; ++i) m = fmx(m, rowmax(qk(i, cm)));
        sol -= m; cm -= m;
        ex(sol);
        const u32x4 e0 = __builtin_bit_cast(u32x4, pack8(sol, 0)), e1 = __builtin_bit_cast(u32x4, pack8(sol, 1));
        o = (f32x16){};
        pv(0, __builtin_bit_cast(bf16x8, e0 & pm[0]), __builtin_bit_cast(bf16x8, e1 & pm[1]), o);
        pv(8, __builtin_bit_cast(bf16x8, e0 & ~pm[0]), __builtin_bit_cast(bf16x8, e1 & ~pm[1]), o);
#pragma unroll 1
        for (int i = 1; i <= nmid; ++i) {
            f32x16 s = qk(i, cm);
            ex(s); pv(i, pack8(s, 0), pack8(s, 1), o);
        }
    }
    STAMP(11); CST(22);
    {
        const float lx = xhalf(o[12]);
        put_o(o, hh ? o[12] : lx, (T * 16 + h) * 2048);
    }
    SCHED();
    const int kh = w4 >> 1, To = 4 * j + 2 * br + (w4 & 1);
    if (ty == 7) {
        WAITV(0);
        if (lane == 0) raise(ws + WS_F2 + T * 128 + h * 8, tag);
        STAMP(13);
        STAMP_FLUSH();
        return;
    }
    bf16x8 wf[16][2];
    {
        WAITV(0);
        if (lane == 0) raise(ws + WS_F2 + T * 128 + h * 8, tag);
        STAMP(13);
        if (local) BAR(); else meet(2);
        const char* a0 = smem + (local ? LW : LA) + kh * (224 * 128) + wb0;
        const char* a1 = smem + (local ? LW : LA) + kh * (224 * 128) + wb1;
#pragma unroll
        for (int ks = 0; ks < 16; ++ks) { const int row = (ks >> 1) * (28 * 128) + (ks & 1) * 2048; wf[ks][0] = tr_frag(a0 + row, a0 + row + 512); wf[ks][1] = tr_frag(a1 + row, a1 + row + 512); }
    }
    STAMP(12);
    poll(ws + WS_F2 + To * 128 + (kh * 8 + lane) * 8, lane < 8, tag);
    STAMP(15);
    f32x16 acc[2] = {};
    {
        u32x4 of[16];
        auto ld = [&](int ks) { of[ks] = __builtin_amdgcn_raw_buffer_load_b128(ros, lane * 16 + (ks & 3) * 1024, (To * 16 + kh * 8) * 2048 + (ks >> 2) * 4096, 16); };
#pragma unroll
        for (int ks = 0; ks < 10; ++ks) { ld(ks); SCHED(); }
        SCHED();
        STAMP(9);
#pragma unroll
        for (int ks = 0; ks < 16; ++ks) {
            acc[0] = MFMA(wf[ks][0], __builtin_bit_cast(bf16x8, of[ks]), acc[0]);
            if (ks + 10 < 16) ld(ks + 10);
            acc[1] = MFMA(wf[ks][1], __builtin_bit_cast(bf16x8, of[ks]), acc[1]);
            SCHED();
        }
    }
    STAMP(16);
    {
        const f32x16 snd = kh ? acc[0] : acc[1];
        f32x16 keep = kh ? acc[1] : acc[0];
        char* mine = smem + LR + w4 * 8192 + 1024;
        const char* other = smem + LR + (w4 ^ 2) * 8192 + 1024;
#pragma unroll
        for (int i = 0; i < 4; ++i) *(f32x4*)(mine + lane * 16 + i * 1024) = (f32x4){snd[4 * i], snd[4 * i + 1], snd[4 * i + 2], snd[4 * i + 3]};
        WAITL0();
        *(volatile LAS u64*)(LAS char*)(mine + 4096) = tag | 1;
#pragma unroll 1
        for (int n = 0; n < SPIN_MAX; ++n) if (*(volatile LAS u64*)(LAS char*)(other + 4096) == (tag | 1)) break;
        asm volatile("" ::: "memory");
#pragma unroll
        for (int i = 0; i < 4; ++i) { const f32x4 v = *(const f32x4*)(other + lane * 16 + i * 1024); keep[4 * i] += v[0]; keep[4 * i + 1] += v[1]; keep[4 * i + 2] += v[2]; keep[4 * i + 3] += v[3]; }
        char* tb = mine + 5120;
#pragma unroll
        for (int s = 0; s < 2; ++s) *(bf16x8*)(tb + t * 64 + (((2 * s + hh) ^ ((t >> 1) & 3)) << 4)) = pack8(keep, s);
        WAITL0();
#pragma unroll
        for (int i = 0; i < 2; ++i) {
            const int row = i * 16 + (lane >> 2);
            const u32x4 v = *(const u32x4*)(tb + row * 64 + ((p ^ ((row >> 1) & 3)) << 4));
            store_dev(v, rout, (lane >> 2) * 1792 + p * 16, (To * 32 + i * 16) * 1792 + ty * 128 + kh * 64);
        }
    }
    STAMP(17);
    STAMP_FLUSH();
}

extern "C" void kernel_launch(void* const* d_in, const int* in_sizes, int n_in, void* d_out, int out_size, void* d_ws, size_t ws_size, hipStream_t stream) {
    fused<<<256, 512, 0, stream>>>((const char*)d_in[0], (const char*)d_in[1], (const char*)d_in[2], (const char*)d_in[3], (const char*)d_in[4], (const char*)d_in[5], (char*)d_out, (char*)d_ws);
}
```

```cpp
#include <hip/hip_runtime.h>
#include <hip/hip_bf16.h>

typedef __bf16 bf16x8 __attribute__((ext_vector_type(8)));
typedef short s16x4 __attribute__((ext_vector_type(4)));
typedef float f32x16 __attribute__((ext_vector_type(16)));
typedef float f32x8 __attribute__((ext_vector_type(8)));
typedef float f32x4 __attribute__((ext_vector_type(4)));
typedef unsigned u32x4 __attribute__((ext_vector_type(4)));
typedef unsigned u32x2 __attribute__((ext_vector_type(2)));
typedef unsigned long long u64;
typedef __attribute__((address_space(1))) u64 gu64;
#define LAS __attribute__((address_space(3)))
#define DI __device__ __forceinline__
#define MFMA(a, b, c) __builtin_amdgcn_mfma_f32_32x32x16_bf16((a), (b), (c), 0, 0, 0)
#define MFMA16(a, b, c) __builtin_amdgcn_mfma_f32_16x16x32_bf16((a), (b), (c), 0, 0, 0)
#define RLX __ATOMIC_RELAXED, __HIP_MEMORY_SCOPE_AGENT

constexpr int XB = 2048 * 896, WB = 448 * 2688, OB = 448 * 896;
constexpr int STAGE = 32768, RING = 65536, LA = 0, LW = 57344, LR = 114688, LDS_BYTES = 163840;
constexpr int WS_F1 = 4096, WS_F2 = 65536, WS_X = 1 << 20, XT = 168 * 512, X_BYTES = 2 * 64 * XT, WS_O = 16 << 20, O_BYTES = 2 * 64 * 16 * 2048;
constexpr int OOB = 0x40000000;
constexpr int CODE_LINES = 214;
#define ROT 2
#define PF0 2
#define RAISE_A 11
#define RAISE_B 2
#define BV1 5
#define KH_DELAY 8
#define OPRE 10
#define OROT 0
constexpr int SPIN_MAX = 1 << 18;

#define STAMP(i)
#define CST(i)
#define STAMP_DECL
#define STAMP_FLUSH()

DI u32x4 srd(const void* p, unsigned bytes) { const u64 a = (u64)p; return (u32x4){(unsigned)a, (unsigned)(a >> 32) & 0xffffu, bytes, 0x00020000u}; }
DI __amdgpu_buffer_rsrc_t rsrc(const void* p, int bytes) { return __builtin_amdgcn_make_buffer_rsrc((void*)p, 0, bytes, 0x00020000); }
DI void dma16(u32x4 r, int lds, int voff, int soff) {
    asm volatile("s_mov_b32 m0, %0\n\ts_nop 0\n\tbuffer_load_dwordx4 %1, %2, %3 offen lds" :: "s"(lds), "v"(voff), "s"(r), "s"(soff) : "memory");
}
DI void dma16_dev(u32x4 r, int lds, int voff, int soff) {
    asm volatile("s_mov_b32 m0, %0\n\ts_nop 0\n\tbuffer_load_dwordx4 %1, %2, %3 offen sc1 lds" :: "s"(lds), "v"(voff), "s"(r), "s"(soff) : "memory");
}
DI void dma64_dev(u32x4 r, int lds, int voff, int soff) {
    asm volatile("s_mov_b32 m0, %0\n\ts_nop 0\n\tbuffer_load_dwordx4 %1, %2, %3 offen sc1 lds\n\tbuffer_load_dwordx4 %1, %2, %3 offen offset:1024 sc1 lds\n\t"
                 "buffer_load_dwordx4 %1, %2, %3 offen offset:2048 sc1 lds\n\tbuffer_load_dwordx4 %1, %2, %3 offen offset:3072 sc1 lds" :: "s"(lds), "v"(voff), "s"(r), "s"(soff) : "memory");
}
DI void dma32_dev(u32x4 r, int lds, int voff, int soff) {
    asm volatile("s_mov_b32 m0, %0\n\ts_nop 0\n\tbuffer_load_dwordx4 %1, %2, %3 offen sc1 lds\n\tbuffer_load_dwordx4 %1, %2, %3 offen offset:1024 sc1 lds"
                 :: "s"(lds), "v"(voff), "s"(r), "s"(soff) : "memory");
}
DI void store_dev(u32x4 v, __amdgpu_buffer_rsrc_t r, int voff, int soff) {
    __builtin_amdgcn_raw_buffer_store_b128(v, r, voff, soff, 16);
    asm volatile("s_nop 1" :: "v"(v) : "memory");
}
DI bf16x8 tr_frag(const char* a0, const char* a1) {
    s16x4 lo = __builtin_amdgcn_ds_read_tr16_b64_v4i16((LAS s16x4*)a0);
    s16x4 hi = __builtin_amdgcn_ds_read_tr16_b64_v4i16((LAS s16x4*)a1);
    return __builtin_bit_cast(bf16x8, __builtin_shufflevector(lo, hi, 0, 1, 2, 3, 4, 5, 6, 7));
}
DI bf16x8 pack8(const f32x16& v, int s) {
    f32x8 t = {v[8 * s], v[8 * s + 1], v[8 * s + 2], v[8 * s + 3], v[8 * s + 4], v[8 * s + 5], v[8 * s + 6], v[8 * s + 7]};
    return __builtin_convertvector(t, bf16x8);
}
DI float xhalf(float v) {
    const unsigned u = __builtin_bit_cast(unsigned, v);
    const auto r = __builtin_amdgcn_permlane32_swap(u, u, false, false);
    return __builtin_bit_cast(float, (threadIdx.x & 32) ? r[0] : r[1]);
}
#define WAITV(n) asm volatile("s_waitcnt vmcnt(" #n ")" ::: "memory")
#define WAITL0() asm volatile("s_waitcnt lgkmcnt(0)" ::: "memory")
#define BAR() do { asm volatile("" ::: "memory"); __builtin_amdgcn_s_barrier(); asm volatile("" ::: "memory"); } while (0)
#define SCHED() __builtin_amdgcn_sched_barrier(0)

#define SG(m, n) __builtin_amdgcn_sched_group_barrier(m, n, 0)
DI float fmx(float a, float b) { return __builtin_elementwise_maximum(a, b); }
typedef int i32x4 __attribute__((ext_vector_type(4)));
#define PIN(x) asm volatile("" : "+v"(x))
DI void raise(char* p, u64 tag) { (void)__hip_atomic_exchange((gu64*)p, tag, RLX); }
template <bool TWO_BARRIERS>
DI u64 poll(const char* p, u64 all, u64 need, u64 tag) {
    u64 v, zero, sv, left; unsigned cnt = SPIN_MAX;
#define POLL_LOOP ".Lpw%=:\n\ts_waitcnt vmcnt(0)\n\tv_cmp_ne_u64 %4, %6, %0\n\ts_and_b64 vcc, %4, %8\n\ts_cbranch_scc0 .Lpd%=\n\ts_sub_u32 %3, %3, 1\n\ts_cbranch_scc1 .Lpd%=\n\t" \
                  "global_atomic_add_x2 %0, %5, %1, off sc0\n\ts_branch .Lpw%=\n.Lpd%=:\n\ts_mov_b64 exec, %2"
#define POLL_OPS : "=&v"(v), "=&v"(zero), "=&s"(sv), "+s"(cnt), "=&s"(left) : "v"(p), "s"(tag), "s"(all), "s"(need) : "vcc", "scc", "memory"
    if (TWO_BARRIERS)
        asm volatile("s_mov_b64 %2, exec\n\ts_mov_b64 exec, %7\n\tv_mov_b64 %1, 0\n\tglobal_atomic_add_x2 %0, %5, %1, off sc0\n\ts_barrier\n\ts_barrier\n" POLL_LOOP POLL_OPS);
    else
        asm volatile("s_mov_b64 %2, exec\n\ts_mov_b64 exec, %7\n\tv_mov_b64 %1, 0\n\tglobal_atomic_add_x2 %0, %5, %1, off sc0\n" POLL_LOOP POLL_OPS);
    return left;
}

extern "C" __global__ void __launch_bounds__(512, 2)
fused(const char* xa, const char* wa, const char* woa, const char* xb, const char* wb, const char* wob, char* out, char* ws) {
    __shared__ __attribute__((aligned(16))) char smem[LDS_BYTES];
    STAMP_DECL;
    STAMP(0);
    const int b = blockIdx.x, xcd = b & 7, idx = b >> 3;
    const int br = xcd & 1, jj = idx >> 3, ty = idx & 7, j = (xcd >> 1) * 4 + jj;
    const int wave = __builtin_amdgcn_readfirstlane(threadIdx.x >> 6), lane = threadIdx.x & 63;
    const int r3 = lane >> 3, u = lane & 7;
    const int lbase = (int)(unsigned)(size_t)(LAS char*)smem;
    u64 tag = *(const __attribute__((address_space(4))) u64*)ws;
    auto make_tag = [&]() { asm volatile("" : "+s"(tag)); tag = tag * 0x9E3779B97F4A7C15ull + 0x7F4A7C159E3779B9ull; asm volatile("" : "+s"(tag)); };
    const int w4 = wave & 3;
    const bool aux = wave >= 4;
    const int hh = lane >> 5, t = lane & 31, blk = (lane >> 4) & 1, q = (lane >> 2) & 3, p = lane & 3;
    const int ha = (0x4ECA7530u >> (4 * ty)) & 15, hb = (0xBFD98621u >> (4 * ty)) & 15;
    const int ca = (7 * ha) >> 1, cb_ = (7 * hb) >> 1;
    const int cs = min(ca, cb_);
    const int T = 4 * j + w4;
    const bool local = br == 0 && ty < 7;
    const int h = br ? hb : ha, c0 = br ? cb_ : ca;
    const __amdgpu_buffer_rsrc_t rxs = rsrc(ws + WS_X, X_BYTES);
    const int wb0 = (8 * hh + q) * 128 + ((blk * 2 + (p & 1)) << 4) + (p >> 1) * 8 + ((q >> 1) << 6), wb1 = wb0 ^ 64;

    const u32x4 rx = srd(br ? xb : xa, XB), rw = srd(br ? wb : wa, WB);
    const int vx0 = r3 * 896 + ((u ^ (r3 >> 1)) << 4), vx1 = vx0 ^ 64;
    const int vw = r3 * 2688 + ((u ^ (((r3 >> 1) & 1) << 2)) << 4);
    const int rot = ROT == 2 ? ((ty + j) & 1) * 4 : 0;
    const int sx = j * (128 * 896), sw = ty * 128;
    if (ty < 7) {
        dma16(rx, lbase + RING + wave * 2048, vx0, sx + wave * 14336 + rot * 128);
        dma16(rx, lbase + RING + wave * 2048 + 1024, vx1, sx + wave * 14336 + 7168 + rot * 128);
#pragma unroll
        for (int cb = 0; cb < 2; ++cb) dma16(rw, lbase + RING + 16384 + cb * 8192 + wave * 1024, vw, sw + (rot * 64 + wave * 8) * 2688 + cb * 896);
    } else {
        u64 pc; unsigned pco;
        asm volatile("s_getpc_b64 %0\n.Lhere%=:\n\ts_mov_b32 %1, .Lhere%=-fused" : "=s"(pc), "=s"(pco));
        unsigned d0 = 0;
        if ((int)threadIdx.x < CODE_LINES) asm volatile("global_load_dword %0, %1, %2" : "+v"(d0) : "v"(threadIdx.x * 128), "s"(pc - pco) : "memory");
        {
            const int ln = jj * 392 + (threadIdx.x < 392 ? threadIdx.x : 0), row = (ln * 9363) >> 16;
            const int vl = row * 2688 + 1792 + (ln - row * 7) * 128;
            asm volatile("global_load_dword %0, %1, %2" : "+v"(d0) : "v"(vl), "s"(br ? wb : wa) : "memory");
            asm volatile("global_load_dword %0, %1, %2" : "+v"(d0) : "v"(vl + 224 * 2688), "s"(br ? wb : wa) : "memory");
        }
        const int wl = (jj * 784 + (threadIdx.x < 392 ? threadIdx.x : 0)) * 128;
        asm volatile("global_load_dword %0, %1, %2" : "+v"(d0) : "v"(wl), "s"(br ? wob : woa) : "memory");
        asm volatile("global_load_dword %0, %1, %2" : "+v"(d0) : "v"(wl + 392 * 128), "s"(br ? wob : woa) : "memory");
        asm volatile("s_waitcnt vmcnt(0)" : "+v"(d0) :: "memory");
    }

    const unsigned NEGB = 0xF149F2CAu;
    f32x16 cd, co;
    u32x4 pm[2];
    int nd, no, lm;
    auto mask_bits = [&]() {
        const unsigned lo = (2u << t) - 1u;
        nd = (int)(~lo >> (4 * hh)); lm = (int)(lo >> (4 * hh)); no = T < 8 ? -1 : lm;
    };
    auto mask_reg = [&](int r) {
        const int bit = (r & 3) + 8 * (r >> 2);
        float a = __builtin_bit_cast(float, __builtin_amdgcn_sbfe(nd, bit, 1) & NEGB), c = __builtin_bit_cast(float, __builtin_amdgcn_sbfe(no, bit, 1) & NEGB);
        PIN(a); PIN(c); cd[r] = a; co[r] = c;
        if (r & 1) { unsigned m = __builtin_amdgcn_perm(__builtin_amdgcn_sbfe(lm, bit, 1), __builtin_amdgcn_sbfe(lm, bit - 1, 1), 0x05040100u); PIN(m); pm[r >> 3][(r >> 1) & 3] = m; }
    };
    auto koff = [&](int cf, int s) { return (2 * s + hh) * 512 + ((((t >> 2) ^ ((cf + 2 * s + hh) & 3)) << 2) + (t & 3)) * 16; };
    auto voff = [&](int hd, int cf, int tt) {
        const int pc8 = (4 * blk + 2 * (p & 1) + (p >> 1) + (hd & 1)) & 7, cc = pc8 >> 1;
        return cc * 512 + ((((2 * tt + hh) ^ ((cf + cc) & 3)) << 2) + q) * 16 + (pc8 & 1) * 8;
    };
    auto qfix = [&](int hd, u32x4& q0, u32x4& q1) {
        const bool z0 = (hd & 1) && !hh, z1 = !(hd & 1) && hh;
        q0.x = z0 ? 0u : q0.x; q0.y = z0 ? 0u : q0.y; q1.z = z1 ? 0u : q1.z; q1.w = z1 ? 0u : q1.w;
    };
    auto ex = [&](f32x16& s) {
#pragma unroll
        for (int r = 0; r < 16; ++r) s[r] = __builtin_amdgcn_exp2f(s[r]);
    };
    auto rowmax = [&](const f32x16& s) {
        float m = fmx(s[0], s[1]);
#pragma unroll
        for (int r = 2; r < 16; r += 2) m = fmx(fmx(m, s[r]), s[r + 1]);
        const unsigned mu = __builtin_bit_cast(unsigned, m);
        const auto r = __builtin_amdgcn_permlane32_swap(mu, mu, false, false);
        const unsigned r0 = r[0], r1 = r[1];
        return fmx(__builtin_bit_cast(float, r0), __builtin_bit_cast(float, r1));
    };
    const __amdgpu_buffer_rsrc_t ros = rsrc(ws + WS_O, O_BYTES);
    auto put_o = [&](f32x16 o, float l, int rec) {
        o = o * __builtin_amdgcn_rcpf(l);
        u32x4 o1 = __builtin_bit_cast(u32x4, pack8(o, 1));
        o1.z = hh ? 0u : o1.z; o1.w = hh ? 0u : o1.w;
        store_dev(__builtin_bit_cast(u32x4, pack8(o, 0)), ros, lane * 16, rec);
        store_dev(o1, ros, lane * 16 + 1024, rec);
    };
    auto ones = [&](int back, bool two) {
        const int bb = back + (two ? hh : 0);
        if (two || !hh) *(u32x2*)(smem + LR + w4 * 4096 + (2 - bb) * 16384 + 2048 + ((h & 1) ? 0 : 1544) + t * 16) = (u32x2){j >= bb ? 0x3F80u : 0u, 0u};
    };
    LAS char* wd = (LAS char*)(smem + LW + 40960 + 7 * 512 + 8);

    if (__builtin_expect(aux, 1)) {
        int ch = rot, sg = 0;
        auto chunk = [&]() {
            if (ty < 7) {
                ++ch; if (ch == 7) ch = 0;
                ++sg; if (sg == 3) sg = 0;
                const int st = lbase + RING + sg * STAGE;
#pragma unroll
                for (int i = 0; i < 4; ++i) dma16(rx, st + w4 * 4096 + i * 1024, (i & 1) ? vx1 : vx0, sx + (w4 * 4 + i) * 7168 + ch * 128);
#pragma unroll
                for (int cb = 0; cb < 2; ++cb)
#pragma unroll
                    for (int i = 0; i < 2; ++i) dma16(rw, st + 16384 + cb * 8192 + w4 * 2048 + i * 1024, vw, sw + (ch * 64 + w4 * 16 + i * 8) * 2688 + cb * 896);
            }
        };
        auto landed = [&]() { if (ty < 7) { WAITV(8); BAR(); } };
#pragma unroll 1
        for (int c = 1; c < 3; ++c) { chunk(); landed(); }
        chunk();
        make_tag();
        int tz = ty, lz = lane;
        asm volatile("" : "+s"(tz)); PIN(lz);
        const bool x7 = tz == 7;
        int nb = x7 ? 15 : (0xF63F30Fu >> (4 * tz)) & 15;
        int sl = (tz == 2 || tz == 5) ? 3 : 0;
        const char* f1;
        u64 mall, mb;
        {
            const int bk = (lz >> 3) & 3, pi = (lz >> 2) & 1;
            const int pg = pi * (x7 ? 1 : (br ? nb : 15) - tz) + (x7 ? 1 + 3 * br : tz);
            const u64 two = (x7 || (br && nb != 15)) ? 0xFFull : 0x0Full;
            mb = local ? 0 : ((x7 ? 0xFFull : two & 0xF0ull) | (j ? (two & 0x88ull) << 8 : 0)) << 32;
            mall = mb | two | (j >= 1 ? two << 8 : 0) | (j >= 2 ? two << 16 : 0);
            f1 = ws + WS_F1 + ((lz >> 5) * 16 + j - bk) * 256 + pg * 32 + (lz & 3) * 8;
            PIN(f1); asm volatile("" : "+s"(mall), "+s"(mb), "+s"(nb), "+s"(sl));
        }
        landed();
        chunk();
        asm volatile("" : "+s"(tz));
        int hd[2] = {x7 ? h : ha, x7 ? h : hb}, cf[2] = {x7 ? c0 : ca, x7 ? c0 : cb_};
        int po[2] = {x7 ? 0 : (cf[0] - cs) * 512, x7 ? 0 : (cf[1] - cs) * 512};
        const int rt = (64 + T) * XT;
        int ra0 = T * XT + (56 + c0) * 512, ra1 = j >= 1 ? ra0 - 4 * XT : OOB, ra2 = j >= 2 ? ra0 - 8 * XT : OOB, lh = lbase + LR + w4 * 4096;
        int rb0 = rt + (56 + (x7 ? c0 : cb_ + sl)) * 512, rb1 = T == 0 ? OOB : (!x7 && w4 == 0 ? rt + (56 + cs) * 512 : rb0) - XT;
        int lb0 = lbase + LW + w4 * 8192 + 8192 + (x7 ? 0 : po[1] + sl * 512), lb1 = (!x7 && w4 == 0) ? lbase + LW : lb0 - 8192;
        int rq = rt + cf[1] * 512, l16 = lz * 16;
        asm volatile("" : "+s"(ra0), "+s"(ra1), "+s"(ra2), "+s"(lh), "+s"(rb0), "+s"(rb1), "+s"(lb0), "+s"(lb1), "+s"(rq)); PIN(l16);
        asm volatile("" : "+s"(hd[0]), "+s"(hd[1]), "+s"(cf[0]), "+s"(cf[1]), "+s"(po[0]), "+s"(po[1]));
        landed();
        chunk();
        const int g4 = lz >> 4, c16 = lz & 15;
        int fo[2], ko[2], qo[2], vo[2][2], qs;
        f32x4 md, mo;
        const bool pt = x7 || (nb != 15 && g4 == sl);
#pragma unroll
        for (int e = 0; e < 2; ++e) {
            fo[e] = g4 * 512 + (((c16 >> 1) ^ ((cf[e] + g4) & 3)) << 6) + (c16 & 1) * 32;
            ko[e] = LW + w4 * 8192 + po[e] + fo[e]; qo[e] = LW + 40960 + w4 * 4096 + po[e] + fo[e]; PIN(ko[e]); PIN(qo[e]);
#pragma unroll
            for (int s = 0; s < 2; ++s) {
                const int pc8 = (2 * p + s + (hd[e] & 1)) & 7, cc = pc8 >> 1;
                vo[e][s] = LW + w4 * 8192 + 4096 + po[e] + cc * 512 + (((2 * g4 + (q >> 1)) ^ ((cf[e] + cc) & 3)) << 6) + (q & 1) * 32 + (pc8 & 1) * 8; PIN(vo[e][s]);
            }
        }
        qs = pt ? fo[1] : OOB; PIN(qs);
        landed();
        chunk();
        landed();
        if (ty < 7) {
#pragma unroll 1
            for (int c = 0; c < 7; ++c) {
                ++ch; if (ch == 7) ch = 0;
#pragma unroll
                for (int i = 0; i < 2; ++i) dma16(rw, lbase + LA + c * 8192 + w4 * 2048 + i * 1024, vw, sw + (ch * 64 + w4 * 16 + i * 8) * 2688 + 1792);
                if (c == 3) { WAITV(8); BAR(); }
            }
            BAR();
            WAITV(6); BAR();
        }
        if (wave == 4 && lane < 7) *(volatile LAS u64*)(wd + lane * 16) = 0;
        asm volatile("s_waitcnt vmcnt(0) lgkmcnt(0)" ::: "memory"); BAR();
        STAMP(4);
#pragma unroll
        for (int r = 0; r < 4; ++r) {
            const int d = c16 - 4 * g4 - r;
            md[r] = (unsigned)d < 8u ? 0.f : -1.0e30f; mo[r] = (d < -8 && T) ? 0.f : -1.0e30f;
        }
        PIN(md); PIN(mo);
        const u32x4 rxx = srd(ws + WS_X, X_BYTES);
        auto tell = [&](int n) { if (lane == 0) *(volatile LAS u64*)(wd + 64 + n * 16) = tag | 1; };
        auto hear = [&](int n) {
#pragma unroll 1
            for (int i = 0; i < SPIN_MAX * 4; ++i) { if (__all(*(volatile LAS u64*)(wd + 64 + n * 16) == (tag | 1))) break; __builtin_amdgcn_s_sleep(1); }
            asm volatile("" ::: "memory");
        };
        auto fetch_b = [&](int v) {
            const int go = v * (56 * 512), lo = v * 4096;
            if (x7) { dma32_dev(rxx, lb0 + lo, l16, rb0 + go); dma32_dev(rxx, lb1 + lo, l16, rb1 + go); }
            else {
                if (w4 == 0) dma64_dev(rxx, lb1 + lo, l16, rb1 + go);
                if (nb != 15 && lane < 32) { dma16_dev(rxx, lb0 + lo, l16, rb0 + go); if (w4) dma16_dev(rxx, lb1 + lo, l16, rb1 + go); }
            }
        };
        auto fetch_a = [&](int v) {
            const int go = v * (56 * 512), lo = v * 2048;
            dma32_dev(rxx, lh + 32768 + lo, l16, ra0 + go);
            dma32_dev(rxx, lh + 16384 + lo, l16, ra1 + go);
            dma32_dev(rxx, lh + lo, l16, ra2 + go);
        };
        STAMP(5);
        unsigned left = 1;
        if (!local) { if (wave == 4) { left = __builtin_amdgcn_readfirstlane((unsigned)poll<false>(f1, mall, mb, tag)); tell(0); if (!left) tell(1); } else hear(0); }
        STAMP(2);
        u32x4 qp[2];
        asm volatile("buffer_load_dwordx4 %0, %2, %3, %4 offen sc1\n\tbuffer_load_dwordx4 %1, %2, %3, %4 offen offset:16 sc1"
                     : "=&v"(qp[0]), "=&v"(qp[1]) : "v"(qs), "s"(rxx), "s"(local ? OOB : rq) : "memory");
        if (!local) fetch_b(0);
        if (wave == 4) { if (left) { poll<false>(f1, mall ^ mb, mall ^ mb, tag); tell(1); } } else hear(1);
        STAMP(6);
        dma32_dev(rxx, lh + 2048, l16, ra0 - 56 * 512);
        fetch_a(0);
        WAITV(8);
        PIN(qp[0]); PIN(qp[1]);
        STAMP(3);
        bf16x8 pe[2][2];
        float ls[2][2];
        if (!local) {
            f32x4 sd[2][2], so[2][2];
#pragma unroll
            for (int e = 0; e < 2; ++e)
#pragma unroll
                for (int y = 0; y < 2; ++y) {
                    const char* kb = smem + ko[e] + y * 16;
                    u32x4 qv = *(const u32x4*)(smem + qo[e] + y * 16);
                    const bool a = pt && (e || x7), z0 = (hd[e] & 1) && g4 == 0, z1 = !(hd[e] & 1) && g4 == 3;
                    qv = (u32x4){a ? qp[y].x : qv.x, a ? qp[y].y : qv.y, a ? qp[y].z : qv.z, a ? qp[y].w : qv.w};
                    qv = (u32x4){z0 ? 0u : qv.x, z0 ? 0u : qv.y, z1 ? 0u : qv.z, z1 ? 0u : qv.w};
                    sd[e][y] = MFMA16(*(const bf16x8*)(kb + 8192), __builtin_bit_cast(bf16x8, qv), md);
                    so[e][y] = MFMA16(*(const bf16x8*)kb, __builtin_bit_cast(bf16x8, qv), mo);
                }
            const bf16x8 one = __builtin_bit_cast(bf16x8, (u32x4){0x3F803F80u, 0x3F803F80u, 0x3F803F80u, 0x3F803F80u});
            float m[4];
#pragma unroll
            for (int i = 0; i < 4; ++i) {
                const f32x4 a = sd[i >> 1][i & 1], c = so[i >> 1][i & 1];
                m[i] = fmx(fmx(fmx(fmx(a[0], a[1]), a[2]), fmx(fmx(a[3], c[0]), c[1])), fmx(c[2], c[3]));
            }
#pragma unroll
            for (int i = 0; i < 4; ++i) {
                const unsigned mu = __builtin_bit_cast(unsigned, m[i]);
                const auto r = __builtin_amdgcn_permlane32_swap(mu, mu, false, false);
                const unsigned r0 = r[0], r1 = r[1];
                m[i] = fmx(__builtin_bit_cast(float, r0), __builtin_bit_cast(float, r1));
            }
#pragma unroll
            for (int i = 0; i < 4; ++i) {
                const unsigned mu = __builtin_bit_cast(unsigned, m[i]);
                const auto r = __builtin_amdgcn_permlane16_swap(mu, mu, false, false);
                const unsigned r0 = r[0], r1 = r[1];
                m[i] = fmx(__builtin_bit_cast(float, r0), __builtin_bit_cast(float, r1));
            }
            SCHED();
            f32x4 sm[4];
#pragma unroll
            for (int i = 0; i < 4; ++i) {
                const f32x4 a = sd[i >> 1][i & 1], c = so[i >> 1][i & 1];
                f32x8 x = {a[0], a[1], a[2], a[3], c[0], c[1], c[2], c[3]};
                x -= m[i];
#pragma unroll
                for (int r = 0; r < 8; ++r) x[r] = __builtin_amdgcn_exp2f(x[r]);
                pe[i >> 1][i & 1] = __builtin_convertvector(x, bf16x8);
                sm[i] = MFMA16(one, pe[i >> 1][i & 1], (f32x4){});
                SCHED();
            }
#pragma unroll
            for (int i = 0; i < 4; ++i) ls[i >> 1][i & 1] = __builtin_amdgcn_rcpf(sm[i][0]);
#pragma unroll
            for (int i = 0; i < 4; ++i) { PIN(pe[i >> 1][i & 1]); PIN(ls[i >> 1][i & 1]); }
        }
        STAMP(10);
        asm volatile("s_waitcnt vmcnt(0) lgkmcnt(0)" ::: "memory");
        STAMP(7);
        __builtin_amdgcn_s_setprio(2);
        if (wave == 4) { poll<true>(f1 + 8192, mall, mall, tag); tell(2); } else { BAR(); BAR(); hear(2); }
        STAMP(14);
        if (!local) fetch_b(1);
        fetch_a(1);
        if (ty < 7) {
            const int vwo = r3 * 896 + ((u ^ (((r3 >> 1) & 1) << 2)) << 4);
            const u32x4 rwo = srd(br ? wob : woa, OB);
#pragma unroll 1
            for (int n = w4; n < 24; n += 4) dma16(rwo, lbase + LA + n * 1024, vwo, n * 7168 + ty * 128);
            WAITV(6);
        } else WAITV(0);
        ones(0, true); ones(2, false);
        WAITL0();
        if (lane == 0) *(volatile LAS u64*)(wd + w4 * 16) = tag | 1;
        STAMP(9);
        if (!local) {
            u32x4 ov[2][2];
#pragma unroll
            for (int e = 0; e < 2; ++e)
#pragma unroll
                for (int y = 0; y < 2; ++y) {
                    const char* v0 = smem + vo[e][0] + y * 16; const char* v1 = smem + vo[e][1] + y * 16;
                    const f32x4 a = MFMA16(tr_frag(v0 + 8192, v0), pe[e][y], (f32x4){}), c = MFMA16(tr_frag(v1 + 8192, v1), pe[e][y], (f32x4){});
                    f32x8 x = {a[0], a[1], a[2], a[3], c[0], c[1], c[2], c[3]};
                    x *= ls[e][y];
                    ov[e][y] = __builtin_bit_cast(u32x4, __builtin_convertvector(x, bf16x8));
                    ov[e][y].z = g4 == 3 ? 0u : ov[e][y].z; ov[e][y].w = g4 == 3 ? 0u : ov[e][y].w;
                }
#pragma unroll
            for (int e = 0; e < 2; ++e)
#pragma unroll
                for (int y = 0; y < 2; ++y) store_dev(ov[e][y], ros, g4 * 512 + c16 * 16 + y * 256, ((64 + T) * 16 + hd[e]) * 2048);
        }
        STAMP(11);
        __builtin_amdgcn_s_setprio(0);
        if (ty < 7) {
            const int vwo = r3 * 896 + ((u ^ (((r3 >> 1) & 1) << 2)) << 4);
            const u32x4 rwo = srd(br ? wob : woa, OB);
#pragma unroll 1
            for (int n = 24 + w4; n < 57 - br; n += 4) dma16(rwo, lbase + LA + n * 1024, vwo, n * 7168 + ty * 128);
            if (br) { WAITV(8); if (lane < 2) raise(ws + WS_F2 + (64 + T) * 128 + (lane ? hb : ha) * 8, tag); }
            WAITV(0);
        } else {
            WAITV(0);
            if (lane == 0) raise(ws + WS_F2 + (64 + T) * 128 + h * 8, tag);
            STAMP(13);
            if (br == 0 && j == 0 && wave == 4) {
                poll<false>(ws + WS_F2 + (4 * j + lane) * 128 + h * 8, 15, 15, tag);
                __builtin_amdgcn_s_sleep(16);
#pragma unroll 1
                for (int n = 0; n < SPIN_MAX; ++n) {
                    u64 v[16];
                    const u64 zero = 0;
#pragma unroll
                    for (int i = 0; i < 16; ++i) asm volatile("global_atomic_add_x2 %0, %1, %2, %3 offset:%4 sc0" : "=&v"(v[i]) : "v"(lane * 8 + (i >> 3) * 4096), "v"(zero), "s"(ws + WS_F2), "i"((i & 7) * 512) : "memory");
                    asm volatile("s_waitcnt vmcnt(0)" : "+v"(v[0]), "+v"(v[1]), "+v"(v[2]), "+v"(v[3]), "+v"(v[4]), "+v"(v[5]), "+v"(v[6]), "+v"(v[7]),
                                 "+v"(v[8]), "+v"(v[9]), "+v"(v[10]), "+v"(v[11]), "+v"(v[12]), "+v"(v[13]), "+v"(v[14]), "+v"(v[15]) :: "memory");
                    bool ok = true;
#pragma unroll
                    for (int i = 0; i < 16; ++i) ok &= v[i] == tag;
                    if (__all(ok)) break;
                    __builtin_amdgcn_s_sleep(8);
                }
                if (lane == 0) __hip_atomic_store((gu64*)ws, tag, RLX);
            }
            STAMP(18);
            STAMP_FLUSH();
            return;
        }
    } else {
        make_tag();
        mask_bits();
        if (ty < 7) {
            int xo[2][4], wo_[2][2];
#pragma unroll
            for (int g = 0; g < 2; ++g) {
#pragma unroll
                for (int ks = 0; ks < 4; ++ks) { xo[g][ks] = RING + g * 65536 + (w4 * 32 + t) * 128 + (((2 * ks + hh) ^ ((t >> 1) & 7)) << 4); PIN(xo[g][ks]); }
                wo_[g][0] = RING + g * 65536 + wb0; wo_[g][1] = RING + g * 65536 + wb1; PIN(wo_[g][0]); PIN(wo_[g][1]);
            }
            int es[2], el[2][2];
#pragma unroll
            for (int s = 0; s < 2; ++s) {
                const int sw16 = ((((t >> 2) ^ (2 * s + hh)) << 2) + (t & 3)) * 16;
                es[s] = (2 * s + hh) * 512 + sw16; PIN(es[s]);
#pragma unroll
                for (int e = 0; e < 2; ++e) {
                    const int ps = 8 * ty + 4 * e + 2 * s + hh - cs;
                    el[e][s] = (unsigned)ps < 8u ? ps * 512 + sw16 : -1; PIN(el[e][s]);
                }
            }
            unsigned pq = br ? (0x1189188u >> (4 * ty)) & 15u : 15u, pk = w4 != 3 ? pq : 15u;
            int g0 = (br * 64 + T) * XT + ty * 4096;
            char* f1 = ws + WS_F1 + (br * 16 + j) * 256 + ty * 32 + w4 * 8;
            asm volatile("" : "+s"(pq), "+s"(pk), "+s"(g0), "+s"(f1));
            auto put = [&](const f32x16& a0, const f32x16& a1, int sec, unsigned pub, u32x4 (&d)[2][2]) {
#pragma unroll
                for (int e = 0; e < 2; ++e)
#pragma unroll
                    for (int s = 0; s < 2; ++s) {
                        d[e][s] = __builtin_bit_cast(u32x4, pack8(e ? a1 : a0, s));
                        if ((pub >> (2 * e + s)) & 1) store_dev(d[e][s], rxs, es[s] + e * 2048, g0 + sec * (56 * 512));
                    }
            };
            const int spare = LR + (4 + w4) * 4096 + 2048 + lane * 16;
            auto keep1 = [&](int lo, int e, int s, const u32x4& d) { *(u32x4*)(smem + (el[e][s] >= 0 ? el[e][s] + lo : spare)) = d; };
            auto keep = [&](int lo, const u32x4 (&d)[2][2]) {
#pragma unroll
                for (int e = 0; e < 2; ++e)
#pragma unroll
                    for (int s = 0; s < 2; ++s) keep1(lo, e, s, d[e][s]);
            };
            f32x16 acc[4];
            bf16x8 xf[28], wf[2][4];
            const f32x16 zero = {};
            WAITV(0); BAR();
            STAMP(1); CST(20);
            auto rd = [&](int n) {
                const int sg = (n >> 2) % 3, ks = n & 3;
                const char* st = smem + (sg & 1) * STAGE;
#pragma unroll
                for (int i = 3; i >= 0; --i) {
                    const char* a = st + wo_[sg >> 1][i & 1] + 16384 + (i >> 1) * 8192 + ks * 2048;
                    wf[n & 1][i] = tr_frag(a, a + 512);
                    if (i == 3) xf[n] = *(const bf16x8*)(st + xo[sg >> 1][ks]);
                }
            };
            rd(0);
            SCHED();
#pragma unroll
            for (int n = 0; n < 28; ++n) {
                if ((n & 3) == 3) { WAITL0(); BAR(); SCHED(); }
                if (n < 27) rd(n + 1);
#pragma unroll
                for (int i = 3; i >= 0; --i) acc[i] = MFMA(wf[n & 1][i], xf[n], n ? acc[i] : zero);
                if (n < 27) { SG(0x8, 1); SG(0x100, 3); SG(0x8, 1); SG(0x100, 3); SG(0x8, 1); SG(0x100, 3); SG(0x8, 1); }
                SCHED();
            }
            STAMP(2);
            const float qs = 0.18898223650461361f * 1.4426950408889634f;
            {
                u32x4 dk[2][2], dq[2][2];
                put(acc[2], acc[3], 1, pk, dk);
                put(acc[0] * qs, acc[1] * qs, 0, pq, dq);
                if (br) { keep(LW + 8192 + w4 * 8192, dk); keep(LW + 40960 + w4 * 4096, dq); }
            }
            STAMP(3);
            BAR();
            auto rv = [&](int n) {
#pragma unroll
                for (int i = 0; i < 2; ++i) { const char* a = smem + LA + n * 2048 + (i ? wb1 : wb0); wf[n & 1][2 * ((n >> 1) & 1) + i] = tr_frag(a, a + 512); }
            };
            rv(0); rv(1); rv(2);
            PIN(nd); PIN(no); PIN(lm);
            SCHED();
#pragma unroll
            for (int n = 0; n < 28; ++n) {
                if (n == BV1) { BAR(); SCHED(); }
                if (n == RAISE_B || n == RAISE_A) { if ((n == RAISE_B) == (br != 0)) { WAITV(0); if (lane == 0) raise(f1, tag); STAMP(9); } SCHED(); }
#pragma unroll
                for (int i = 0; i < 2; ++i) acc[2 + i] = MFMA(wf[n & 1][2 * ((n >> 1) & 1) + i], xf[n], n ? acc[2 + i] : zero);
                if (n + 3 < 28) rv(n + 3);
                if (n >= 2 && n < 18) mask_reg(n - 2);
                SG(0x8, 1); SG(0x100, 2); SG(0x2, 4); SG(0x8, 1); SG(0x100, 2); SG(0x2, 4);
                SCHED();
            }
            STAMP(4); CST(21);
            {
                u32x4 dv[2][2];
                put(acc[2], acc[3], 2, pk, dv);
                if (br) keep(LW + 12288 + w4 * 8192, dv);
            }
            STAMP(5);
            WAITV(0);
            if (lane == 0) raise(f1 + 8192, tag);
        } else {
            BAR();
#pragma unroll
            for (int r = 0; r < 16; ++r) mask_reg(r);
        }
        const char* tl = smem + LR + w4 * 4096;
        int fo[2] = {koff(c0, 0), koff(c0, 1)}, vo[2] = {voff(h, c0, 0) + 2048, voff(h, c0, 1) + 2048};
        PIN(fo[0]); PIN(fo[1]); PIN(vo[0]); PIN(vo[1]);
        STAMP(6);
        WAITL0(); BAR();
        STAMP(7);
        __builtin_amdgcn_s_setprio(1);
        u32x4 qf[2];
        qf[0] = *(const u32x4*)(tl + 2048 + fo[0]); qf[1] = *(const u32x4*)(tl + 2048 + fo[1]);
        auto kfrag = [&](int n, int s) { return *(const bf16x8*)(tl + (8 - n) * 4096 + fo[s]); };
        auto vfrag = [&](int n, int s) { const char* vb = tl + (8 - n) * 4096 + s * 256; return tr_frag(vb + vo[0], vb + vo[1]); };
        bf16x8 k0 = kfrag(0, 0), k1 = kfrag(0, 1), k2 = kfrag(8, 0), k3 = kfrag(8, 1);
        WAITL0(); BAR();
        CST(19); STAMP(10);
        qfix(h, qf[0], qf[1]);
        const bf16x8 q0 = __builtin_bit_cast(bf16x8, qf[0]), q1 = __builtin_bit_cast(bf16x8, qf[1]);
        auto qk = [&](int n, const f32x16& c) { f32x16 r = MFMA(kfrag(n, 0), q0, c); return MFMA(kfrag(n, 1), q1, r); };
        f32x16 cm, sa, sb;
        bf16x8 pp[8][2];
        cd = MFMA(k0, q0, cd); co = MFMA(k2, q0, co); cd = MFMA(k1, q1, cd); co = MFMA(k3, q1, co);
        k0 = kfrag(1, 0); k1 = kfrag(1, 1);
#pragma unroll
        for (int r = 0; r < 16; ++r) cd[r] = fmx(cd[r], co[r]);
        {
            const float m = -rowmax(cd);
#pragma unroll
            for (int r = 0; r < 16; ++r) cm[r] = m;
        }
        cd += cm;
        sa = cd;
        SCHED();
#define STG(n, cur, nxt) do { \
            if (n < 7) { nxt = MFMA(k0, q0, cm); nxt = MFMA(k1, q1, nxt); } \
            if (n < 6) { k0 = kfrag(n + 2, 0); k1 = kfrag(n + 2, 1); } \
            ex(cur); pp[n][0] = pack8(cur, 0); pp[n][1] = pack8(cur, 1); \
            if (n < 7) SG(0x8, 1); SG(0x400, 4); if (n < 7) SG(0x8, 1); if (n < 6) SG(0x100, 2); SG(0x400, 4); SG(0x2, 2); SG(0x400, 4); SG(0x2, 2); SG(0x400, 4); SG(0x2, 4); SCHED(); } while (0)
        STG(0, sa, sb); STG(1, sb, sa); STG(2, sa, sb); STG(3, sb, sa); STG(4, sa, sb); STG(5, sb, sa); STG(6, sa, sb); STG(7, sb, sa);
        bf16x8 pz[2];
#pragma unroll
        for (int s = 0; s < 2; ++s) {
            const u32x4 e = __builtin_bit_cast(u32x4, pp[0][s]);
            pp[0][s] = __builtin_bit_cast(bf16x8, e & pm[s]); pz[s] = __builtin_bit_cast(bf16x8, e & ~pm[s]); PIN(pz[s]);
        }
#pragma unroll
        for (int n = 0; n < 8; ++n) { PIN(pp[n][0]); PIN(pp[n][1]); }
        char* f2 = ws + WS_F2 + T * 128 + h * 8;
        int orec = (T * 16 + h) * 2048;
        const char* va = tl + vo[0]; const char* vb = tl + vo[1];
        asm volatile("" : "+s"(f2), "+s"(orec));
        STAMP(8);
#pragma unroll 1
        for (int i = 0; i < SPIN_MAX; ++i) if (__all(*(volatile LAS u64*)(wd + p * 16) == (tag | 1))) break;
        asm volatile("" ::: "memory");
        STAMP(14);
        f32x16 o = {};
        auto pv = [&](int n, bf16x8 p0, bf16x8 p1) { o = MFMA(vfrag(n, 0), p0, o); o = MFMA(vfrag(n, 1), p1, o); };
        {
            bf16x8 vf[18];
            auto rdv = [&](int i) { const int off = (8 - (i >> 1)) * 4096 + (i & 1) * 256; vf[i] = tr_frag(va + off, vb + off); };
#pragma unroll
            for (int i = 0; i < 5; ++i) { rdv(i); SCHED(); }
#pragma unroll
            for (int i = 0; i < 18; ++i) {
                o = MFMA(vf[i], i < 16 ? pp[i >> 1][i & 1] : pz[i & 1], o);
                if (i + 5 < 18) rdv(i + 5);
                SCHED();
            }
        }
        auto publish = [&]() {
            const float lf = o[12];
            const unsigned lu = __builtin_bit_cast(unsigned, lf);
            const unsigned lx = __builtin_amdgcn_permlane32_swap(lu, lu, false, false)[1];
            put_o(o, __builtin_bit_cast(float, lx), orec);
        };
        publish();
        STAMP(11); CST(22);
        float big = fmx(__builtin_fabsf(o[0]), __builtin_fabsf(o[1]));
#pragma unroll
        for (int r = 2; r < 16; r += 2) big = fmx(fmx(big, __builtin_fabsf(o[r])), __builtin_fabsf(o[r + 1]));
        if (__builtin_expect(!__all(big < 1.0e36f), 0)) {
            const int nmid = T >= 7 ? 7 : T;
            float m = 0.f;
#pragma unroll 1
            for (int i = 1; i <= nmid; ++i) m = fmx(m, rowmax(qk(i, cm)));
            cd -= m; cm -= m;
            ex(cd);
            o = (f32x16){};
            const u32x4 e0 = __builtin_bit_cast(u32x4, pack8(cd, 0)), e1 = __builtin_bit_cast(u32x4, pack8(cd, 1));
            pv(0, __builtin_bit_cast(bf16x8, e0 & pm[0]), __builtin_bit_cast(bf16x8, e1 & pm[1]));
            pv(8, __builtin_bit_cast(bf16x8, e0 & ~pm[0]), __builtin_bit_cast(bf16x8, e1 & ~pm[1]));
#pragma unroll 1
            for (int i = 1; i <= nmid; ++i) {
                f32x16 s = qk(i, cm);
                ex(s); pv(i, pack8(s, 0), pack8(s, 1));
            }
            WAITV(0);
            publish();
        }
        WAITV(0);
        if (lane == 0) raise(f2, tag);
        __builtin_amdgcn_s_setprio(0);
        if (ty == 7) {
            STAMP(13);
            STAMP_FLUSH();
            return;
        }
    }
    STAMP(13);
    const int kh = (wave >> 2) ^ (OROT & ty & 1), kq = (OROT >> 1) & (OROT == 2 ? ty : ty >> 1) & 1;
    int wq[2][2], l16 = lane * 16, os[2];
#pragma unroll
    for (int g = 0; g < 2; ++g) {
        wq[g][0] = LA + (kh * 8 + (g ^ kq) * 4) * (28 * 128) + wb0; wq[g][1] = wq[g][0] ^ 64; PIN(wq[g][0]); PIN(wq[g][1]);
        os[g] = ((br * 64 + T) * 16 + kh * 8 + (g ^ kq) * 4) * 2048;
    }
    const int tr = br ? 2 * (t & 15) + (t >> 4) : t;
    int part = LR + w4 * 8192 + l16, tw = LR + 32768 + w4 * 4096 + tr * 128 + ((hh ^ (tr & 7)) << 4), tg = LR + 32768 + w4 * 4096 + r3 * 128 + ((u ^ r3) << 4);
    int ov = r3 * 1792 + u * 16, oo = T * (32 * 1792) + br * 896 + ty * 128;
    asm volatile("" : "+v"(l16), "+v"(part), "+v"(tw), "+v"(tg), "+v"(ov)); asm volatile("" : "+s"(oo), "+s"(os[0]), "+s"(os[1]));
    const __amdgpu_buffer_rsrc_t rout = rsrc(out, 2048 * 1792);
    if (wave == 4) poll<false>(ws + WS_F2 + (br * 64 + 4 * j) * 128 + lane * 8, ~0ull, ~0ull, tag);
    STAMP(12);
    BAR();
    STAMP(15);
    f32x16 acc[2] = {};
    u32x4 of[16];
    bf16x8 wf[16][2];
    auto ld = [&](int ks) { of[ks] = __builtin_amdgcn_raw_buffer_load_b128(ros, l16 + (ks & 3) * 1024, os[ks >> 3] + ((ks >> 2) & 1) * 4096, 16); };
    auto rdw = [&](int ks) {
        const int row = ((ks >> 1) & 3) * (28 * 128) + (ks & 1) * 2048;
#pragma unroll
        for (int i = 0; i < 2; ++i) { const char* a = smem + wq[ks >> 3][i] + row; wf[ks][i] = tr_frag(a, a + 512); }
    };
    auto step = [&](int ks) {
        acc[0] = MFMA(wf[ks][0], __builtin_bit_cast(bf16x8, of[ks]), acc[0]);
        if (ks + OPRE < 16) ld(ks + OPRE);
        if (ks + 3 < 16) rdw(ks + 3);
        acc[1] = MFMA(wf[ks][1], __builtin_bit_cast(bf16x8, of[ks]), acc[1]);
        SCHED();
    };
    if (aux) __builtin_amdgcn_s_sleep(KH_DELAY); else __builtin_amdgcn_s_setprio(1);
#pragma unroll
    for (int ks = 0; ks < OPRE; ++ks) { ld(ks); SCHED(); }
#pragma unroll
    for (int ks = 0; ks < 3; ++ks) rdw(ks);
    SCHED();
#pragma unroll
    for (int ks = 0; ks < 13; ++ks) step(ks);
    if (!aux) {
        step(13); step(14); step(15);
        STAMP(16);
#pragma unroll
        for (int i = 0; i < 8; ++i) *(f32x4*)(smem + part + i * 1024) = (f32x4){acc[i >> 2][4 * (i & 3)], acc[i >> 2][4 * (i & 3) + 1], acc[i >> 2][4 * (i & 3) + 2], acc[i >> 2][4 * (i & 3) + 3]};
        WAITL0(); BAR();
    } else {
        BAR();
        f32x4 pv[8];
#pragma unroll
        for (int i = 0; i < 8; ++i) pv[i] = *(const f32x4*)(smem + part + i * 1024);
        SCHED();
        step(13); step(14); step(15);
        STAMP(16);
#pragma unroll
        for (int i = 0; i < 8; ++i) { acc[i >> 2][4 * (i & 3)] += pv[i][0]; acc[i >> 2][4 * (i & 3) + 1] += pv[i][1]; acc[i >> 2][4 * (i & 3) + 2] += pv[i][2]; acc[i >> 2][4 * (i & 3) + 3] += pv[i][3]; }
#pragma unroll
        for (int i = 0; i < 2; ++i)
#pragma unroll
            for (int s = 0; s < 2; ++s) *(bf16x8*)(smem + (tw ^ ((4 * i + 2 * s) << 4))) = pack8(acc[i], s);
        WAITL0();
        u32x4 v[4];
#pragma unroll
        for (int i = 0; i < 4; ++i) v[i] = *(const u32x4*)(smem + tg + i * 1024);
        SCHED();
#pragma unroll
        for (int i = 0; i < 4; ++i) store_dev(v[i], rout, ov, oo + i * (8 * 1792));
    }
    STAMP(17);
    STAMP_FLUSH();
}

extern "C" void kernel_launch(void* const* d_in, const int* in_sizes, int n_in, void* d_out, int out_size, void* d_ws, size_t ws_size, hipStream_t stream) {
    fused<<<256, 512, 0, stream>>>((const char*)d_in[0], (const char*)d_in[1], (const char*)d_in[2], (const char*)d_in[3], (const char*)d_in[4], (const char*)d_in[5], (char*)d_out, (char*)d_ws);
}
```

```cpp
#define DIAG 1
#include <hip/hip_runtime.h>
#include <hip/hip_bf16.h>

typedef __bf16 bf16x8 __attribute__((ext_vector_type(8)));
typedef short s16x4 __attribute__((ext_vector_type(4)));
typedef float f32x16 __attribute__((ext_vector_type(16)));
typedef float f32x8 __attribute__((ext_vector_type(8)));
typedef float f32x4 __attribute__((ext_vector_type(4)));
typedef unsigned u32x4 __attribute__((ext_vector_type(4)));
typedef unsigned u32x2 __attribute__((ext_vector_type(2)));
typedef unsigned long long u64;
typedef __attribute__((address_space(1))) u64 gu64;
#define LAS __attribute__((address_space(3)))
#define DI __device__ __forceinline__
#define MFMA(a, b, c) __builtin_amdgcn_mfma_f32_32x32x16_bf16((a), (b), (c), 0, 0, 0)
#define MFMA16(a, b, c) __builtin_amdgcn_mfma_f32_16x16x32_bf16((a), (b), (c), 0, 0, 0)
#define RLX __ATOMIC_RELAXED, __HIP_MEMORY_SCOPE_AGENT

constexpr int XB = 2048 * 896, WB = 448 * 2688, OB = 448 * 896;
constexpr int STAGE = 32768, RING = 65536, LA = 0, LW = 57344, LR = 114688, LDS_BYTES = 163840;
constexpr int WS_F1 = 4096, WS_F2 = 65536, WS_X = 1 << 20, XT = 168 * 512, X_BYTES = 2 * 64 * XT, WS_O = 16 << 20, O_BYTES = 2 * 64 * 16 * 2048;
constexpr int OOB = 0x40000000;
constexpr int CODE_LINES = 235;
#define ROT 2
#define PF0 2
#define RAISE_A 11
#define RAISE_B 2
#define BV1 5
#define KH_DELAY 8
#define OPRE 10
#define OROT 0
constexpr int SPIN_MAX = 1 << 18;

__device__ u64 g_diag[256 * 8 * 32];
#define STAMP(i) do { u64 t_; asm volatile("s_memrealtime %0\n\ts_waitcnt lgkmcnt(0)" : "=s"(t_) :: "memory"); stamps[i] = t_; } while (0)
#define CST(i) do { u64 t_; asm volatile("s_memtime %0\n\ts_waitcnt lgkmcnt(0)" : "=s"(t_) :: "memory"); stamps[i] = t_; } while (0)
#define STAMP_DECL u64 stamps[24]; _Pragma("unroll") for (int i_ = 0; i_ < 24; ++i_) stamps[i_] = 0
#define STAMP_FLUSH() do { if ((threadIdx.x & 63) == 0) { _Pragma("unroll") for (int i_ = 0; i_ < 24; ++i_) g_diag[(blockIdx.x * 8 + (threadIdx.x >> 6)) * 32 + i_] = stamps[i_]; } } while (0)

DI u32x4 srd(const void* p, unsigned bytes) { const u64 a = (u64)p; return (u32x4){(unsigned)a, (unsigned)(a >> 32) & 0xffffu, bytes, 0x00020000u}; }
DI __amdgpu_buffer_rsrc_t rsrc(const void* p, int bytes) { return __builtin_amdgcn_make_buffer_rsrc((void*)p, 0, bytes, 0x00020000); }
DI void dma16(u32x4 r, int lds, int voff, int soff) {
    asm volatile("s_mov_b32 m0, %0\n\ts_nop 0\n\tbuffer_load_dwordx4 %1, %2, %3 offen lds" :: "s"(lds), "v"(voff), "s"(r), "s"(soff) : "memory");
}
DI void dma16_dev(u32x4 r, int lds, int voff, int soff) {
    asm volatile("s_mov_b32 m0, %0\n\ts_nop 0\n\tbuffer_load_dwordx4 %1, %2, %3 offen sc1 lds" :: "s"(lds), "v"(voff), "s"(r), "s"(soff) : "memory");
}
DI void dma64_dev(u32x4 r, int lds, int voff, int soff) {
    asm volatile("s_mov_b32 m0, %0\n\ts_nop 0\n\tbuffer_load_dwordx4 %1, %2, %3 offen sc1 lds\n\tbuffer_load_dwordx4 %1, %2, %3 offen offset:1024 sc1 lds\n\t"
                 "buffer_load_dwordx4 %1, %2, %3 offen offset:2048 sc1 lds\n\tbuffer_load_dwordx4 %1, %2, %3 offen offset:3072 sc1 lds" :: "s"(lds), "v"(voff), "s"(r), "s"(soff) : "memory");
}
DI void dma32_dev(u32x4 r, int lds, int voff, int soff) {
    asm volatile("s_mov_b32 m0, %0\n\ts_nop 0\n\tbuffer_load_dwordx4 %1, %2, %3 offen sc1 lds\n\tbuffer_load_dwordx4 %1, %2, %3 offen offset:1024 sc1 lds"
                 :: "s"(lds), "v"(voff), "s"(r), "s"(soff) : "memory");
}
DI void store_dev(u32x4 v, __amdgpu_buffer_rsrc_t r, int voff, int soff) {
    __builtin_amdgcn_raw_buffer_store_b128(v, r, voff, soff, 16);
    asm volatile("s_nop 1" :: "v"(v) : "memory");
}
DI bf16x8 tr_frag(const char* a0, const char* a1) {
    s16x4 lo = __builtin_amdgcn_ds_read_tr16_b64_v4i16((LAS s16x4*)a0);
    s16x4 hi = __builtin_amdgcn_ds_read_tr16_b64_v4i16((LAS s16x4*)a1);
    return __builtin_bit_cast(bf16x8, __builtin_shufflevector(lo, hi, 0, 1, 2, 3, 4, 5, 6, 7));
}
DI bf16x8 pack8(const f32x16& v, int s) {
    f32x8 t = {v[8 * s], v[8 * s + 1], v[8 * s + 2], v[8 * s + 3], v[8 * s + 4], v[8 * s + 5], v[8 * s + 6], v[8 * s + 7]};
    return __builtin_convertvector(t, bf16x8);
}
DI float xhalf(float v) {
    const unsigned u = __builtin_bit_cast(unsigned, v);
    const auto r = __builtin_amdgcn_permlane32_swap(u, u, false, false);
    return __builtin_bit_cast(float, (threadIdx.x & 32) ? r[0] : r[1]);
}
#define WAITV(n) asm volatile("s_waitcnt vmcnt(" #n ")" ::: "memory")
#define WAITL0() asm volatile("s_waitcnt lgkmcnt(0)" ::: "memory")
#define BAR() do { asm volatile("" ::: "memory"); __builtin_amdgcn_s_barrier(); asm volatile("" ::: "memory"); } while (0)
#define SCHED() __builtin_amdgcn_sched_barrier(0)

#define SG(m, n) __builtin_amdgcn_sched_group_barrier(m, n, 0)
DI float fmx(float a, float b) { return __builtin_elementwise_maximum(a, b); }
typedef int i32x4 __attribute__((ext_vector_type(4)));
#define PIN(x) asm volatile("" : "+v"(x))
DI void raise(char* p, u64 tag) { (void)__hip_atomic_exchange((gu64*)p, tag, RLX); }
template <bool TWO_BARRIERS>
DI u64 poll(const char* p, u64 all, u64 need, u64 tag) {
    u64 v, zero, sv, left; unsigned cnt = SPIN_MAX;
#define POLL_LOOP ".Lpw%=:\n\ts_waitcnt vmcnt(0)\n\tv_cmp_ne_u64 %4, %6, %0\n\ts_and_b64 vcc, %4, %8\n\ts_cbranch_scc0 .Lpd%=\n\ts_sub_u32 %3, %3, 1\n\ts_cbranch_scc1 .Lpd%=\n\t" \
                  "global_atomic_add_x2 %0, %5, %1, off sc0\n\ts_branch .Lpw%=\n.Lpd%=:\n\ts_mov_b64 exec, %2"
#define POLL_OPS : "=&v"(v), "=&v"(zero), "=&s"(sv), "+s"(cnt), "=&s"(left) : "v"(p), "s"(tag), "s"(all), "s"(need) : "vcc", "scc", "memory"
    if (TWO_BARRIERS)
        asm volatile("s_mov_b64 %2, exec\n\ts_mov_b64 exec, %7\n\tv_mov_b64 %1, 0\n\tglobal_atomic_add_x2 %0, %5, %1, off sc0\n\ts_barrier\n\ts_barrier\n" POLL_LOOP POLL_OPS);
    else
        asm volatile("s_mov_b64 %2, exec\n\ts_mov_b64 exec, %7\n\tv_mov_b64 %1, 0\n\tglobal_atomic_add_x2 %0, %5, %1, off sc0\n" POLL_LOOP POLL_OPS);
    return left;
}

extern "C" __global__ void __launch_bounds__(512, 2)
fused(const char* xa, const char* wa, const char* woa, const char* xb, const char* wb, const char* wob, char* out, char* ws) {
    __shared__ __attribute__((aligned(16))) char smem[LDS_BYTES];
    STAMP_DECL;
    STAMP(0);
    const int b = blockIdx.x, xcd = b & 7, idx = b >> 3;
    const int br = xcd & 1, jj = idx >> 3, ty = idx & 7, j = (xcd >> 1) * 4 + jj;
    const int wave = __builtin_amdgcn_readfirstlane(threadIdx.x >> 6), lane = threadIdx.x & 63;
    const int r3 = lane >> 3, u = lane & 7;
    const int lbase = (int)(unsigned)(size_t)(LAS char*)smem;
    u64 tag = *(const __attribute__((address_space(4))) u64*)ws;
    auto make_tag = [&]() { asm volatile("" : "+s"(tag)); tag = tag * 0x9E3779B97F4A7C15ull + 0x7F4A7C159E3779B9ull; asm volatile("" : "+s"(tag)); };
    const int w4 = wave & 3;
    const bool aux = wave >= 4;
    const int hh = lane >> 5, t = lane & 31, blk = (lane >> 4) & 1, q = (lane >> 2) & 3, p = lane & 3;
    const int ha = (0x4ECA7530u >> (4 * ty)) & 15, hb = (0xBFD98621u >> (4 * ty)) & 15;
    const int ca = (7 * ha) >> 1, cb_ = (7 * hb) >> 1;
    const int cs = min(ca, cb_);
    const int T = 4 * j + w4;
    const bool local = br == 0 && ty < 7;
    const int h = br ? hb : ha, c0 = br ? cb_ : ca;
    const __amdgpu_buffer_rsrc_t rxs = rsrc(ws + WS_X, X_BYTES);
    const int wb0 = (8 * hh + q) * 128 + ((blk * 2 + (p & 1)) << 4) + (p >> 1) * 8 + ((q >> 1) << 6), wb1 = wb0 ^ 64;

    const u32x4 rx = srd(br ? xb : xa, XB), rw = srd(br ? wb : wa, WB);
    const int vx0 = r3 * 896 + ((u ^ (r3 >> 1)) << 4), vx1 = vx0 ^ 64;
    const int vw = r3 * 2688 + ((u ^ (((r3 >> 1) & 1) << 2)) << 4);
    const int rot = ROT == 2 ? ((ty + j) & 1) * 4 : 0;
    const int sx = j * (128 * 896), sw = ty * 128;
    if (ty < 7) {
        dma16(rx, lbase + RING + wave * 2048, vx0, sx + wave * 14336 + rot * 128);
        dma16(rx, lbase + RING + wave * 2048 + 1024, vx1, sx + wave * 14336 + 7168 + rot * 128);
#pragma unroll
        for (int cb = 0; cb < 2; ++cb) dma16(rw, lbase + RING + 16384 + cb * 8192 + wave * 1024, vw, sw + (rot * 64 + wave * 8) * 2688 + cb * 896);
    } else {
        u64 pc; unsigned pco;
        asm volatile("s_getpc_b64 %0\n.Lhere%=:\n\ts_mov_b32 %1, .Lhere%=-fused" : "=s"(pc), "=s"(pco));
        unsigned d0 = 0;
        if ((int)threadIdx.x < CODE_LINES) asm volatile("global_load_dword %0, %1, %2" : "+v"(d0) : "v"(threadIdx.x * 128), "s"(pc - pco) : "memory");
        {
            const int ln = jj * 392 + (threadIdx.x < 392 ? threadIdx.x : 0), row = (ln * 9363) >> 16;
            const int vl = row * 2688 + 1792 + (ln - row * 7) * 128;
            asm volatile("global_load_dword %0, %1, %2" : "+v"(d0) : "v"(vl), "s"(br ? wb : wa) : "memory");
            asm volatile("global_load_dword %0, %1, %2" : "+v"(d0) : "v"(vl + 224 * 2688), "s"(br ? wb : wa) : "memory");
        }
        const int wl = (jj * 784 + (threadIdx.x < 392 ? threadIdx.x : 0)) * 128;
        asm volatile("global_load_dword %0, %1, %2" : "+v"(d0) : "v"(wl), "s"(br ? wob : woa) : "memory");
        asm volatile("global_load_dword %0, %1, %2" : "+v"(d0) : "v"(wl + 392 * 128), "s"(br ? wob : woa) : "memory");
        asm volatile("s_waitcnt vmcnt(0)" : "+v"(d0) :: "memory");
    }

    const unsigned NEGB = 0xF149F2CAu;
    f32x16 cd, co;
    u32x4 pm[2];
    int nd, no, lm;
    auto mask_bits = [&]() {
        const unsigned lo = (2u << t) - 1u;
        nd = (int)(~lo >> (4 * hh)); lm = (int)(lo >> (4 * hh)); no = T < 8 ? -1 : lm;
    };
    auto mask_reg = [&](int r) {
        const int bit = (r & 3) + 8 * (r >> 2);
        float a = __builtin_bit_cast(float, __builtin_amdgcn_sbfe(nd, bit, 1) & NEGB), c = __builtin_bit_cast(float, __builtin_amdgcn_sbfe(no, bit, 1) & NEGB);
        PIN(a); PIN(c); cd[r] = a; co[r] = c;
        if (r & 1) { unsigned m = __builtin_amdgcn_perm(__builtin_amdgcn_sbfe(lm, bit, 1), __builtin_amdgcn_sbfe(lm, bit - 1, 1), 0x05040100u); PIN(m); pm[r >> 3][(r >> 1) & 3] = m; }
    };
    auto koff = [&](int cf, int s) { return (2 * s + hh) * 512 + ((((t >> 2) ^ ((cf + 2 * s + hh) & 3)) << 2) + (t & 3)) * 16; };
    auto voff = [&](int hd, int cf, int tt) {
        const int pc8 = (4 * blk + 2 * (p & 1) + (p >> 1) + (hd & 1)) & 7, cc = pc8 >> 1;
        return cc * 512 + ((((2 * tt + hh) ^ ((cf + cc) & 3)) << 2) + q) * 16 + (pc8 & 1) * 8;
    };
    auto qfix = [&](int hd, u32x4& q0, u32x4& q1) {
        const bool z0 = (hd & 1) && !hh, z1 = !(hd & 1) && hh;
        q0.x = z0 ? 0u : q0.x; q0.y = z0 ? 0u : q0.y; q1.z = z1 ? 0u : q1.z; q1.w = z1 ? 0u : q1.w;
    };
    auto ex = [&](f32x16& s) {
#pragma unroll
        for (int r = 0; r < 16; ++r) s[r] = __builtin_amdgcn_exp2f(s[r]);
    };
    auto rowmax = [&](const f32x16& s) {
        float m = fmx(s[0], s[1]);
#pragma unroll
        for (int r = 2; r < 16; r += 2) m = fmx(fmx(m, s[r]), s[r + 1]);
        const unsigned mu = __builtin_bit_cast(unsigned, m);
        const auto r = __builtin_amdgcn_permlane32_swap(mu, mu, false, false);
        const unsigned r0 = r[0], r1 = r[1];
        return fmx(__builtin_bit_cast(float, r0), __builtin_bit_cast(float, r1));
    };
    const __amdgpu_buffer_rsrc_t ros = rsrc(ws + WS_O, O_BYTES);
    auto put_o = [&](f32x16 o, float l, int rec) {
        o = o * __builtin_amdgcn_rcpf(l);
        u32x4 o1 = __builtin_bit_cast(u32x4, pack8(o, 1));
        o1.z = hh ? 0u : o1.z; o1.w = hh ? 0u : o1.w;
        store_dev(__builtin_bit_cast(u32x4, pack8(o, 0)), ros, lane * 16, rec);
        store_dev(o1, ros, lane * 16 + 1024, rec);
    };
    auto ones = [&](int back, bool two) {
        const int bb = back + (two ? hh : 0);
        if (two || !hh) *(u32x2*)(smem + LR + w4 * 4096 + (2 - bb) * 16384 + 2048 + ((h & 1) ? 0 : 1544) + t * 16) = (u32x2){j >= bb ? 0x3F80u : 0u, 0u};
    };
    LAS char* wd = (LAS char*)(smem + LW + 40960 + 7 * 512 + 8);

    if (__builtin_expect(aux, 1)) {
        int ch = rot, sg = 0;
        auto chunk = [&]() {
            if (ty < 7) {
                ++ch; if (ch == 7) ch = 0;
                ++sg; if (sg == 3) sg = 0;
                const int st = lbase + RING + sg * STAGE;
#pragma unroll
                for (int i = 0; i < 4; ++i) dma16(rx, st + w4 * 4096 + i * 1024, (i & 1) ? vx1 : vx0, sx + (w4 * 4 + i) * 7168 + ch * 128);
#pragma unroll
                for (int cb = 0; cb < 2; ++cb)
#pragma unroll
                    for (int i = 0; i < 2; ++i) dma16(rw, st + 16384 + cb * 8192 + w4 * 2048 + i * 1024, vw, sw + (ch * 64 + w4 * 16 + i * 8) * 2688 + cb * 896);
            }
        };
        auto landed = [&]() { if (ty < 7) { WAITV(8); BAR(); } };
#pragma unroll 1
        for (int c = 1; c < 3; ++c) { chunk(); landed(); }
        chunk();
        make_tag();
        int tz = ty, lz = lane;
        asm volatile("" : "+s"(tz)); PIN(lz);
        const bool x7 = tz == 7;
        int nb = x7 ? 15 : (0xF63F30Fu >> (4 * tz)) & 15;
        int sl = (tz == 2 || tz == 5) ? 3 : 0;
        const char* f1;
        u64 mall, mb;
        {
            const int bk = (lz >> 3) & 3, pi = (lz >> 2) & 1;
            const int pg = pi * (x7 ? 1 : (br ? nb : 15) - tz) + (x7 ? 1 + 3 * br : tz);
            const u64 two = (x7 || (br && nb != 15)) ? 0xFFull : 0x0Full;
            mb = local ? 0 : ((x7 ? 0xFFull : two & 0xF0ull) | (j ? (two & 0x88ull) << 8 : 0)) << 32;
            mall = mb | two | (j >= 1 ? two << 8 : 0) | (j >= 2 ? two << 16 : 0);
            f1 = ws + WS_F1 + ((lz >> 5) * 16 + j - bk) * 256 + pg * 32 + (lz & 3) * 8;
            PIN(f1); asm volatile("" : "+s"(mall), "+s"(mb), "+s"(nb), "+s"(sl));
        }
        landed();
        chunk();
        asm volatile("" : "+s"(tz));
        int hd[2] = {x7 ? h : ha, x7 ? h : hb}, cf[2] = {x7 ? c0 : ca, x7 ? c0 : cb_};
        int po[2] = {x7 ? 0 : (cf[0] - cs) * 512, x7 ? 0 : (cf[1] - cs) * 512};
        const int rt = (64 + T) * XT;
        int ra0 = T * XT + (56 + c0) * 512, ra1 = j >= 1 ? ra0 - 4 * XT : OOB, ra2 = j >= 2 ? ra0 - 8 * XT : OOB, lh = lbase + LR + w4 * 4096;
        int rb0 = rt + (56 + (x7 ? c0 : cb_ + sl)) * 512, rb1 = T == 0 ? OOB : (!x7 && w4 == 0 ? rt + (56 + cs) * 512 : rb0) - XT;
        int lb0 = lbase + LW + w4 * 8192 + 8192 + (x7 ? 0 : po[1] + sl * 512), lb1 = (!x7 && w4 == 0) ? lbase + LW : lb0 - 8192;
        int rq = rt + cf[1] * 512, l16 = lz * 16;
        asm volatile("" : "+s"(ra0), "+s"(ra1), "+s"(ra2), "+s"(lh), "+s"(rb0), "+s"(rb1), "+s"(lb0), "+s"(lb1), "+s"(rq)); PIN(l16);
        asm volatile("" : "+s"(hd[0]), "+s"(hd[1]), "+s"(cf[0]), "+s"(cf[1]), "+s"(po[0]), "+s"(po[1]));
        landed();
        chunk();
        const int g4 = lz >> 4, c16 = lz & 15;
        int fo[2], ko[2], qo[2], vo[2][2], qs;
        f32x4 md, mo;
        const bool pt = x7 || (nb != 15 && g4 == sl);
#pragma unroll
        for (int e = 0; e < 2; ++e) {
            fo[e] = g4 * 512 + (((c16 >> 1) ^ ((cf[e] + g4) & 3)) << 6) + (c16 & 1) * 32;
            ko[e] = LW + w4 * 8192 + po[e] + fo[e]; qo[e] = LW + 40960 + w4 * 4096 + po[e] + fo[e]; PIN(ko[e]); PIN(qo[e]);
#pragma unroll
            for (int s = 0; s < 2; ++s) {
                const int pc8 = (2 * p + s + (hd[e] & 1)) & 7, cc = pc8 >> 1;
                vo[e][s] = LW + w4 * 8192 + 4096 + po[e] + cc * 512 + (((2 * g4 + (q >> 1)) ^ ((cf[e] + cc) & 3)) << 6) + (q & 1) * 32 + (pc8 & 1) * 8; PIN(vo[e][s]);
            }
        }
        qs = pt ? fo[1] : OOB; PIN(qs);
        landed();
        chunk();
        landed();
        if (ty < 7) {
#pragma unroll 1
            for (int c = 0; c < 7; ++c) {
                ++ch; if (ch == 7) ch = 0;
#pragma unroll
                for (int i = 0; i < 2; ++i) dma16(rw, lbase + LA + c * 8192 + w4 * 2048 + i * 1024, vw, sw + (ch * 64 + w4 * 16 + i * 8) * 2688 + 1792);
                if (c == 3) { WAITV(8); BAR(); }
            }
            BAR();
            WAITV(6); BAR();
        }
        if (wave == 4 && lane < 7) *(volatile LAS u64*)(wd + lane * 16) = 0;
        asm volatile("s_waitcnt vmcnt(0) lgkmcnt(0)" ::: "memory"); BAR();
        STAMP(4);
#pragma unroll
        for (int r = 0; r < 4; ++r) {
            const int d = c16 - 4 * g4 - r;
            md[r] = (unsigned)d < 8u ? 0.f : -1.0e30f; mo[r] = (d < -8 && T) ? 0.f : -1.0e30f;
        }
        PIN(md); PIN(mo);
        const u32x4 rxx = srd(ws + WS_X, X_BYTES);
        auto tell = [&](int n) { if (lane == 0) *(volatile LAS u64*)(wd + 64 + n * 16) = tag | 1; };
        auto hear = [&](int n) {
#pragma unroll 1
            for (int i = 0; i < SPIN_MAX * 4; ++i) { if (__all(*(volatile LAS u64*)(wd + 64 + n * 16) == (tag | 1))) break; __builtin_amdgcn_s_sleep(1); }
            asm volatile("" ::: "memory");
        };
        auto fetch_b = [&](int v) {
            const int go = v * (56 * 512), lo = v * 4096;
            if (x7) { dma32_dev(rxx, lb0 + lo, l16, rb0 + go); dma32_dev(rxx, lb1 + lo, l16, rb1 + go); }
            else {
                if (w4 == 0) dma64_dev(rxx, lb1 + lo, l16, rb1 + go);
                if (nb != 15 && lane < 32) { dma16_dev(rxx, lb0 + lo, l16, rb0 + go); if (w4) dma16_dev(rxx, lb1 + lo, l16, rb1 + go); }
            }
        };
        auto fetch_a = [&](int v) {
            const int go = v * (56 * 512), lo = v * 2048;
            dma32_dev(rxx, lh + 32768 + lo, l16, ra0 + go);
            dma32_dev(rxx, lh + 16384 + lo, l16, ra1 + go);
            dma32_dev(rxx, lh + lo, l16, ra2 + go);
        };
        STAMP(5);
        unsigned left = 1;
        if (!local) { if (wave == 4) { left = __builtin_amdgcn_readfirstlane((unsigned)poll<false>(f1, mall, mb, tag)); tell(0); if (!left) tell(1); } else hear(0); }
        STAMP(2);
        u32x4 qp[2];
        asm volatile("buffer_load_dwordx4 %0, %2, %3, %4 offen sc1\n\tbuffer_load_dwordx4 %1, %2, %3, %4 offen offset:16 sc1"
                     : "=&v"(qp[0]), "=&v"(qp[1]) : "v"(qs), "s"(rxx), "s"(local ? OOB : rq) : "memory");
        if (!local) fetch_b(0);
        if (wave == 4) { if (left) { poll<false>(f1, mall ^ mb, mall ^ mb, tag); tell(1); } } else hear(1);
        STAMP(6);
        dma32_dev(rxx, lh + 2048, l16, ra0 - 56 * 512);
        fetch_a(0);
        WAITV(8);
        PIN(qp[0]); PIN(qp[1]);
        STAMP(3);
        bf16x8 pe[2][2];
        float ls[2][2];
        if (!local) {
            f32x4 sd[2][2], so[2][2];
#pragma unroll
            for (int e = 0; e < 2; ++e)
#pragma unroll
                for (int y = 0; y < 2; ++y) {
                    const char* kb = smem + ko[e] + y * 16;
                    u32x4 qv = *(const u32x4*)(smem + qo[e] + y * 16);
                    const bool a = pt && (e || x7), z0 = (hd[e] & 1) && g4 == 0, z1 = !(hd[e] & 1) && g4 == 3;
                    qv = (u32x4){a ? qp[y].x : qv.x, a ? qp[y].y : qv.y, a ? qp[y].z : qv.z, a ? qp[y].w : qv.w};
                    qv = (u32x4){z0 ? 0u : qv.x, z0 ? 0u : qv.y, z1 ? 0u : qv.z, z1 ? 0u : qv.w};
                    sd[e][y] = MFMA16(*(const bf16x8*)(kb + 8192), __builtin_bit_cast(bf16x8, qv), md);
                    so[e][y] = MFMA16(*(const bf16x8*)kb, __builtin_bit_cast(bf16x8, qv), mo);
                }
            const bf16x8 one = __builtin_bit_cast(bf16x8, (u32x4){0x3F803F80u, 0x3F803F80u, 0x3F803F80u, 0x3F803F80u});
            float m[4];
#pragma unroll
            for (int i = 0; i < 4; ++i) {
                const f32x4 a = sd[i >> 1][i & 1], c = so[i >> 1][i & 1];
                m[i] = fmx(fmx(fmx(fmx(a[0], a[1]), a[2]), fmx(fmx(a[3], c[0]), c[1])), fmx(c[2], c[3]));
            }
#pragma unroll
            for (int i = 0; i < 4; ++i) {
                const unsigned mu = __builtin_bit_cast(unsigned, m[i]);
                const auto r = __builtin_amdgcn_permlane32_swap(mu, mu, false, false);
                const unsigned r0 = r[0], r1 = r[1];
                m[i] = fmx(__builtin_bit_cast(float, r0), __builtin_bit_cast(float, r1));
            }
#pragma unroll
            for (int i = 0; i < 4; ++i) {
                const unsigned mu = __builtin_bit_cast(unsigned, m[i]);
                const auto r = __builtin_amdgcn_permlane16_swap(mu, mu, false, false);
                const unsigned r0 = r[0], r1 = r[1];
                m[i] = fmx(__builtin_bit_cast(float, r0), __builtin_bit_cast(float, r1));
            }
            SCHED();
            f32x4 sm[4];
#pragma unroll
            for (int i = 0; i < 4; ++i) {
                const f32x4 a = sd[i >> 1][i & 1], c = so[i >> 1][i & 1];
                f32x8 x = {a[0], a[1], a[2], a[3], c[0], c[1], c[2], c[3]};
                x -= m[i];
#pragma unroll
                for (int r = 0; r < 8; ++r) x[r] = __builtin_amdgcn_exp2f(x[r]);
                pe[i >> 1][i & 1] = __builtin_convertvector(x, bf16x8);
                sm[i] = MFMA16(one, pe[i >> 1][i & 1], (f32x4){});
                SCHED();
            }
#pragma unroll
            for (int i = 0; i < 4; ++i) ls[i >> 1][i & 1] = __builtin_amdgcn_rcpf(sm[i][0]);
#pragma unroll
            for (int i = 0; i < 4; ++i) { PIN(pe[i >> 1][i & 1]); PIN(ls[i >> 1][i & 1]); }
        }
        STAMP(10);
        asm volatile("s_waitcnt vmcnt(0) lgkmcnt(0)" ::: "memory");
        STAMP(7);
        __builtin_amdgcn_s_setprio(2);
        if (wave == 4) { poll<true>(f1 + 8192, mall, mall, tag); tell(2); } else { BAR(); BAR(); hear(2); }
        STAMP(14);
        if (!local) fetch_b(1);
        fetch_a(1);
        if (ty < 7) {
            const int vwo = r3 * 896 + ((u ^ (((r3 >> 1) & 1) << 2)) << 4);
            const u32x4 rwo = srd(br ? wob : woa, OB);
#pragma unroll 1
            for (int n = w4; n < 24; n += 4) dma16(rwo, lbase + LA + n * 1024, vwo, n * 7168 + ty * 128);
            WAITV(6);
        } else WAITV(0);
        ones(0, true); ones(2, false);
        WAITL0();
        if (lane == 0) *(volatile LAS u64*)(wd + w4 * 16) = tag | 1;
        STAMP(9);
        if (!local) {
            u32x4 ov[2][2];
#pragma unroll
            for (int e = 0; e < 2; ++e)
#pragma unroll
                for (int y = 0; y < 2; ++y) {
                    const char* v0 = smem + vo[e][0] + y * 16; const char* v1 = smem + vo[e][1] + y * 16;
                    const f32x4 a = MFMA16(tr_frag(v0 + 8192, v0), pe[e][y], (f32x4){}), c = MFMA16(tr_frag(v1 + 8192, v1), pe[e][y], (f32x4){});
                    f32x8 x = {a[0], a[1], a[2], a[3], c[0], c[1], c[2], c[3]};
                    x *= ls[e][y];
                    ov[e][y] = __builtin_bit_cast(u32x4, __builtin_convertvector(x, bf16x8));
                    ov[e][y].z = g4 == 3 ? 0u : ov[e][y].z; ov[e][y].w = g4 == 3 ? 0u : ov[e][y].w;
                }
#pragma unroll
            for (int e = 0; e < 2; ++e)
#pragma unroll
                for (int y = 0; y < 2; ++y) store_dev(ov[e][y], ros, g4 * 512 + c16 * 16 + y * 256, ((64 + T) * 16 + hd[e]) * 2048);
        }
        STAMP(11);
        __builtin_amdgcn_s_setprio(0);
        if (ty < 7) {
            const int vwo = r3 * 896 + ((u ^ (((r3 >> 1) & 1) << 2)) << 4);
            const u32x4 rwo = srd(br ? wob : woa, OB);
#pragma unroll 1
            for (int n = 24 + w4; n < 57 - br; n += 4) dma16(rwo, lbase + LA + n * 1024, vwo, n * 7168 + ty * 128);
            if (br) { WAITV(8); if (lane < 2) raise(ws + WS_F2 + (64 + T) * 128 + (lane ? hb : ha) * 8, tag); }
            WAITV(0);
        } else {
            WAITV(0);
            if (lane == 0) raise(ws + WS_F2 + (64 + T) * 128 + h * 8, tag);
            STAMP(13);
            if (br == 0 && j == 0 && wave == 4) {
                poll<false>(ws + WS_F2 + (4 * j + lane) * 128 + h * 8, 15, 15, tag);
                __builtin_amdgcn_s_sleep(16);
#pragma unroll 1
                for (int n = 0; n < SPIN_MAX; ++n) {
                    u64 v[16];
                    const u64 zero = 0;
#pragma unroll
                    for (int i = 0; i < 16; ++i) asm volatile("global_atomic_add_x2 %0, %1, %2, %3 offset:%4 sc0" : "=&v"(v[i]) : "v"(lane * 8 + (i >> 3) * 4096), "v"(zero), "s"(ws + WS_F2), "i"((i & 7) * 512) : "memory");
                    asm volatile("s_waitcnt vmcnt(0)" : "+v"(v[0]), "+v"(v[1]), "+v"(v[2]), "+v"(v[3]), "+v"(v[4]), "+v"(v[5]), "+v"(v[6]), "+v"(v[7]),
                                 "+v"(v[8]), "+v"(v[9]), "+v"(v[10]), "+v"(v[11]), "+v"(v[12]), "+v"(v[13]), "+v"(v[14]), "+v"(v[15]) :: "memory");
                    bool ok = true;
#pragma unroll
                    for (int i = 0; i < 16; ++i) ok &= v[i] == tag;
                    if (__all(ok)) break;
                    __builtin_amdgcn_s_sleep(8);
                }
                if (lane == 0) __hip_atomic_store((gu64*)ws, tag, RLX);
            }
            STAMP(18);
            STAMP_FLUSH();
            return;
        }
    } else {
        make_tag();
        mask_bits();
        if (ty < 7) {
            int xo[2][4], wo_[2][2];
#pragma unroll
            for (int g = 0; g < 2; ++g) {
#pragma unroll
                for (int ks = 0; ks < 4; ++ks) { xo[g][ks] = RING + g * 65536 + (w4 * 32 + t) * 128 + (((2 * ks + hh) ^ ((t >> 1) & 7)) << 4); PIN(xo[g][ks]); }
                wo_[g][0] = RING + g * 65536 + wb0; wo_[g][1] = RING + g * 65536 + wb1; PIN(wo_[g][0]); PIN(wo_[g][1]);
            }
            int es[2], el[2][2];
#pragma unroll
            for (int s = 0; s < 2; ++s) {
                const int sw16 = ((((t >> 2) ^ (2 * s + hh)) << 2) + (t & 3)) * 16;
                es[s] = (2 * s + hh) * 512 + sw16; PIN(es[s]);
#pragma unroll
                for (int e = 0; e < 2; ++e) {
                    const int ps = 8 * ty + 4 * e + 2 * s + hh - cs;
                    el[e][s] = (unsigned)ps < 8u ? ps * 512 + sw16 : -1; PIN(el[e][s]);
                }
            }
            unsigned pq = br ? (0x1189188u >> (4 * ty)) & 15u : 15u, pk = w4 != 3 ? pq : 15u;
            int g0 = (br * 64 + T) * XT + ty * 4096;
            char* f1 = ws + WS_F1 + (br * 16 + j) * 256 + ty * 32 + w4 * 8;
            asm volatile("" : "+s"(pq), "+s"(pk), "+s"(g0), "+s"(f1));
            auto put = [&](const f32x16& a0, const f32x16& a1, int sec, unsigned pub, u32x4 (&d)[2][2]) {
#pragma unroll
                for (int e = 0; e < 2; ++e)
#pragma unroll
                    for (int s = 0; s < 2; ++s) {
                        d[e][s] = __builtin_bit_cast(u32x4, pack8(e ? a1 : a0, s));
                        if ((pub >> (2 * e + s)) & 1) store_dev(d[e][s], rxs, es[s] + e * 2048, g0 + sec * (56 * 512));
                    }
            };
            const int spare = LR + (4 + w4) * 4096 + 2048 + lane * 16;
            auto keep1 = [&](int lo, int e, int s, const u32x4& d) { *(u32x4*)(smem + (el[e][s] >= 0 ? el[e][s] + lo : spare)) = d; };
            auto keep = [&](int lo, const u32x4 (&d)[2][2]) {
#pragma unroll
                for (int e = 0; e < 2; ++e)
#pragma unroll
                    for (int s = 0; s < 2; ++s) keep1(lo, e, s, d[e][s]);
            };
            f32x16 acc[4];
            bf16x8 xf[28], wf[2][4];
            const f32x16 zero = {};
            WAITV(0); BAR();
            STAMP(1); CST(20);
            auto rd = [&](int n) {
                const int sg = (n >> 2) % 3, ks = n & 3;
                const char* st = smem + (sg & 1) * STAGE;
#pragma unroll
                for (int i = 3; i >= 0; --i) {
                    const char* a = st + wo_[sg >> 1][i & 1] + 16384 + (i >> 1) * 8192 + ks * 2048;
                    wf[n & 1][i] = tr_frag(a, a + 512);
                    if (i == 3) xf[n] = *(const bf16x8*)(st + xo[sg >> 1][ks]);
                }
            };
            rd(0);
            SCHED();
#pragma unroll
            for (int n = 0; n < 28; ++n) {
                if ((n & 3) == 3) { WAITL0(); BAR(); SCHED(); }
                if (n < 27) rd(n + 1);
#pragma unroll
                for (int i = 3; i >= 0; --i) acc[i] = MFMA(wf[n & 1][i], xf[n], n ? acc[i] : zero);
                if (n < 27) { SG(0x8, 1); SG(0x100, 3); SG(0x8, 1); SG(0x100, 3); SG(0x8, 1); SG(0x100, 3); SG(0x8, 1); }
                SCHED();
            }
            STAMP(2);
            const float qs = 0.18898223650461361f * 1.4426950408889634f;
            {
                u32x4 dk[2][2], dq[2][2];
                put(acc[2], acc[3], 1, pk, dk);
                put(acc[0] * qs, acc[1] * qs, 0, pq, dq);
                if (br) { keep(LW + 8192 + w4 * 8192, dk); keep(LW + 40960 + w4 * 4096, dq); }
            }
            STAMP(3);
            BAR();
            auto rv = [&](int n) {
#pragma unroll
                for (int i = 0; i < 2; ++i) { const char* a = smem + LA + n * 2048 + (i ? wb1 : wb0); wf[n & 1][2 * ((n >> 1) & 1) + i] = tr_frag(a, a + 512); }
            };
            rv(0); rv(1); rv(2);
            PIN(nd); PIN(no); PIN(lm);
            SCHED();
#pragma unroll
            for (int n = 0; n < 28; ++n) {
                if (n == BV1) { BAR(); SCHED(); }
                if (n == RAISE_B || n == RAISE_A) { if ((n == RAISE_B) == (br != 0)) { WAITV(0); if (lane == 0) raise(f1, tag); STAMP(9); } SCHED(); }
#pragma unroll
                for (int i = 0; i < 2; ++i) acc[2 + i] = MFMA(wf[n & 1][2 * ((n >> 1) & 1) + i], xf[n], n ? acc[2 + i] : zero);
                if (n + 3 < 28) rv(n + 3);
                if (n >= 2 && n < 18) mask_reg(n - 2);
                SG(0x8, 1); SG(0x100, 2); SG(0x2, 4); SG(0x8, 1); SG(0x100, 2); SG(0x2, 4);
                SCHED();
            }
            STAMP(4); CST(21);
            {
                u32x4 dv[2][2];
                put(acc[2], acc[3], 2, pk, dv);
                if (br) keep(LW + 12288 + w4 * 8192, dv);
            }
            STAMP(5);
            WAITV(0);
            if (lane == 0) raise(f1 + 8192, tag);
        } else {
            BAR();
#pragma unroll
            for (int r = 0; r < 16; ++r) mask_reg(r);
        }
        const char* tl = smem + LR + w4 * 4096;
        int fo[2] = {koff(c0, 0), koff(c0, 1)}, vo[2] = {voff(h, c0, 0) + 2048, voff(h, c0, 1) + 2048};
        PIN(fo[0]); PIN(fo[1]); PIN(vo[0]); PIN(vo[1]);
        STAMP(6);
        WAITL0(); BAR();
        STAMP(7);
        __builtin_amdgcn_s_setprio(1);
        u32x4 qf[2];
        qf[0] = *(const u32x4*)(tl + 2048 + fo[0]); qf[1] = *(const u32x4*)(tl + 2048 + fo[1]);
        auto kfrag = [&](int n, int s) { return *(const bf16x8*)(tl + (8 - n) * 4096 + fo[s]); };
        auto vfrag = [&](int n, int s) { const char* vb = tl + (8 - n) * 4096 + s * 256; return tr_frag(vb + vo[0], vb + vo[1]); };
        bf16x8 k0 = kfrag(0, 0), k1 = kfrag(0, 1), k2 = kfrag(8, 0), k3 = kfrag(8, 1);
        WAITL0(); BAR();
        CST(19); STAMP(10);
        qfix(h, qf[0], qf[1]);
        const bf16x8 q0 = __builtin_bit_cast(bf16x8, qf[0]), q1 = __builtin_bit_cast(bf16x8, qf[1]);
        auto qk = [&](int n, const f32x16& c) { f32x16 r = MFMA(kfrag(n, 0), q0, c); return MFMA(kfrag(n, 1), q1, r); };
        f32x16 cm, sa, sb;
        bf16x8 pp[8][2];
        cd = MFMA(k0, q0, cd); co = MFMA(k2, q0, co); cd = MFMA(k1, q1, cd); co = MFMA(k3, q1, co);
        k0 = kfrag(1, 0); k1 = kfrag(1, 1);
#pragma unroll
        for (int r = 0; r < 16; ++r) cd[r] = fmx(cd[r], co[r]);
        {
            const float m = -rowmax(cd);
#pragma unroll
            for (int r = 0; r < 16; ++r) cm[r] = m;
        }
        cd += cm;
        sa = cd;
        SCHED();
#define STG(n, cur, nxt) do { \
            if (n < 7) { nxt = MFMA(k0, q0, cm); nxt = MFMA(k1, q1, nxt); } \
            if (n < 6) { k0 = kfrag(n + 2, 0); k1 = kfrag(n + 2, 1); } \
            ex(cur); pp[n][0] = pack8(cur, 0); pp[n][1] = pack8(cur, 1); \
            if (n < 7) SG(0x8, 1); SG(0x400, 4); if (n < 7) SG(0x8, 1); if (n < 6) SG(0x100, 2); SG(0x400, 4); SG(0x2, 2); SG(0x400, 4); SG(0x2, 2); SG(0x400, 4); SG(0x2, 4); SCHED(); } while (0)
        STG(0, sa, sb); STG(1, sb, sa); STG(2, sa, sb); STG(3, sb, sa); STG(4, sa, sb); STG(5, sb, sa); STG(6, sa, sb); STG(7, sb, sa);
        bf16x8 pz[2];
#pragma unroll
        for (int s = 0; s < 2; ++s) {
            const u32x4 e = __builtin_bit_cast(u32x4, pp[0][s]);
            pp[0][s] = __builtin_bit_cast(bf16x8, e & pm[s]); pz[s] = __builtin_bit_cast(bf16x8, e & ~pm[s]); PIN(pz[s]);
        }
#pragma unroll
        for (int n = 0; n < 8; ++n) { PIN(pp[n][0]); PIN(pp[n][1]); }
        char* f2 = ws + WS_F2 + T * 128 + h * 8;
        int orec = (T * 16 + h) * 2048;
        const char* va = tl + vo[0]; const char* vb = tl + vo[1];
        asm volatile("" : "+s"(f2), "+s"(orec));
        STAMP(8);
#pragma unroll 1
        for (int i = 0; i < SPIN_MAX; ++i) if (__all(*(volatile LAS u64*)(wd + p * 16) == (tag | 1))) break;
        asm volatile("" ::: "memory");
        STAMP(14);
        f32x16 o = {};
        auto pv = [&](int n, bf16x8 p0, bf16x8 p1) { o = MFMA(vfrag(n, 0), p0, o); o = MFMA(vfrag(n, 1), p1, o); };
        {
            bf16x8 vf[18];
            auto rdv = [&](int i) { const int off = (8 - (i >> 1)) * 4096 + (i & 1) * 256; vf[i] = tr_frag(va + off, vb + off); };
#pragma unroll
            for (int i = 0; i < 5; ++i) { rdv(i); SCHED(); }
#pragma unroll
            for (int i = 0; i < 18; ++i) {
                o = MFMA(vf[i], i < 16 ? pp[i >> 1][i & 1] : pz[i & 1], o);
                if (i + 5 < 18) rdv(i + 5);
                SCHED();
            }
        }
        auto publish = [&]() {
            const float lf = o[12];
            const unsigned lu = __builtin_bit_cast(unsigned, lf);
            const unsigned lx = __builtin_amdgcn_permlane32_swap(lu, lu, false, false)[1];
            put_o(o, __builtin_bit_cast(float, lx), orec);
        };
        publish();
        STAMP(11); CST(22);
        float big = fmx(__builtin_fabsf(o[0]), __builtin_fabsf(o[1]));
#pragma unroll
        for (int r = 2; r < 16; r += 2) big = fmx(fmx(big, __builtin_fabsf(o[r])), __builtin_fabsf(o[r + 1]));
        if (__builtin_expect(!__all(big < 1.0e36f), 0)) {
            const int nmid = T >= 7 ? 7 : T;
            float m = 0.f;
#pragma unroll 1
            for (int i = 1; i <= nmid; ++i) m = fmx(m, rowmax(qk(i, cm)));
            cd -= m; cm -= m;
            ex(cd);
            o = (f32x16){};
            const u32x4 e0 = __builtin_bit_cast(u32x4, pack8(cd, 0)), e1 = __builtin_bit_cast(u32x4, pack8(cd, 1));
            pv(0, __builtin_bit_cast(bf16x8, e0 & pm[0]), __builtin_bit_cast(bf16x8, e1 & pm[1]));
            pv(8, __builtin_bit_cast(bf16x8, e0 & ~pm[0]), __builtin_bit_cast(bf16x8, e1 & ~pm[1]));
#pragma unroll 1
            for (int i = 1; i <= nmid; ++i) {
                f32x16 s = qk(i, cm);
                ex(s); pv(i, pack8(s, 0), pack8(s, 1));
            }
            WAITV(0);
            publish();
        }
        WAITV(0);
        if (lane == 0) raise(f2, tag);
        __builtin_amdgcn_s_setprio(0);
        if (ty == 7) {
            STAMP(13);
            STAMP_FLUSH();
            return;
        }
    }
    STAMP(13);
    const int kh = (wave >> 2) ^ (OROT & ty & 1), kq = (OROT >> 1) & (OROT == 2 ? ty : ty >> 1) & 1;
    int wq[2][2], l16 = lane * 16, os[2];
#pragma unroll
    for (int g = 0; g < 2; ++g) {
        wq[g][0] = LA + (kh * 8 + (g ^ kq) * 4) * (28 * 128) + wb0; wq[g][1] = wq[g][0] ^ 64; PIN(wq[g][0]); PIN(wq[g][1]);
        os[g] = ((br * 64 + T) * 16 + kh * 8 + (g ^ kq) * 4) * 2048;
    }
    const int tr = br ? 2 * (t & 15) + (t >> 4) : t;
    int part = LR + w4 * 8192 + l16, tw = LR + 32768 + w4 * 4096 + tr * 128 + ((hh ^ (tr & 7)) << 4), tg = LR + 32768 + w4 * 4096 + r3 * 128 + ((u ^ r3) << 4);
    int ov = r3 * 1792 + u * 16, oo = T * (32 * 1792) + br * 896 + ty * 128;
    asm volatile("" : "+v"(l16), "+v"(part), "+v"(tw), "+v"(tg), "+v"(ov)); asm volatile("" : "+s"(oo), "+s"(os[0]), "+s"(os[1]));
    const __amdgpu_buffer_rsrc_t rout = rsrc(out, 2048 * 1792);
    if (wave == 4) poll<false>(ws + WS_F2 + (br * 64 + 4 * j) * 128 + lane * 8, ~0ull, ~0ull, tag);
    STAMP(12);
    BAR();
    STAMP(15);
    f32x16 acc[2] = {};
    u32x4 of[16];
    bf16x8 wf[16][2];
    auto ld = [&](int ks) { of[ks] = __builtin_amdgcn_raw_buffer_load_b128(ros, l16 + (ks & 3) * 1024, os[ks >> 3] + ((ks >> 2) & 1) * 4096, 16); };
    auto rdw = [&](int ks) {
        const int row = ((ks >> 1) & 3) * (28 * 128) + (ks & 1) * 2048;
#pragma unroll
        for (int i = 0; i < 2; ++i) { const char* a = smem + wq[ks >> 3][i] + row; wf[ks][i] = tr_frag(a, a + 512); }
    };
    auto step = [&](int ks) {
        acc[0] = MFMA(wf[ks][0], __builtin_bit_cast(bf16x8, of[ks]), acc[0]);
        if (ks + OPRE < 16) ld(ks + OPRE);
        if (ks + 3 < 16) rdw(ks + 3);
        acc[1] = MFMA(wf[ks][1], __builtin_bit_cast(bf16x8, of[ks]), acc[1]);
        SCHED();
    };
    if (aux) __builtin_amdgcn_s_sleep(KH_DELAY); else __builtin_amdgcn_s_setprio(1);
#pragma unroll
    for (int ks = 0; ks < OPRE; ++ks) { ld(ks); SCHED(); }
#pragma unroll
    for (int ks = 0; ks < 3; ++ks) rdw(ks);
    SCHED();
#pragma unroll
    for (int ks = 0; ks < 13; ++ks) step(ks);
    if (!aux) {
        step(13); step(14); step(15);
        STAMP(16);
#pragma unroll
        for (int i = 0; i < 8; ++i) *(f32x4*)(smem + part + i * 1024) = (f32x4){acc[i >> 2][4 * (i & 3)], acc[i >> 2][4 * (i & 3) + 1], acc[i >> 2][4 * (i & 3) + 2], acc[i >> 2][4 * (i & 3) + 3]};
        WAITL0(); BAR();
    } else {
        BAR();
        f32x4 pv[8];
#pragma unroll
        for (int i = 0; i < 8; ++i) pv[i] = *(const f32x4*)(smem + part + i * 1024);
        SCHED();
        step(13); step(14); step(15);
        STAMP(16);
#pragma unroll
        for (int i = 0; i < 8; ++i) { acc[i >> 2][4 * (i & 3)] += pv[i][0]; acc[i >> 2][4 * (i & 3) + 1] += pv[i][1]; acc[i >> 2][4 * (i & 3) + 2] += pv[i][2]; acc[i >> 2][4 * (i & 3) + 3] += pv[i][3]; }
#pragma unroll
        for (int i = 0; i < 2; ++i)
#pragma unroll
            for (int s = 0; s < 2; ++s) *(bf16x8*)(smem + (tw ^ ((4 * i + 2 * s) << 4))) = pack8(acc[i], s);
        WAITL0();
        u32x4 v[4];
#pragma unroll
        for (int i = 0; i < 4; ++i) v[i] = *(const u32x4*)(smem + tg + i * 1024);
        SCHED();
#pragma unroll
        for (int i = 0; i < 4; ++i) store_dev(v[i], rout, ov, oo + i * (8 * 1792));
    }
    STAMP(17);
    WAITV(0);
    STAMP(18);
    STAMP_FLUSH();
}

__global__ void k_probe(int ia, int maxa) {
    __shared__ unsigned long long sa[1024];
    const int b = threadIdx.x;
    unsigned long long x0 = g_diag[(b * 2) * 32 + ia], x1 = g_diag[(b * 2 + 1) * 32 + ia];
    if (!maxa) { if (x0 == 0) x0 = ~0ull; if (x1 == 0) x1 = ~0ull; }
    sa[b] = maxa ? (x0 > x1 ? x0 : x1) : (x0 < x1 ? x0 : x1); __syncthreads();
    for (int o = 512; o > 0; o >>= 1) { if (b < o) { unsigned long long x = sa[b], y = sa[b + o]; sa[b] = maxa ? (x > y ? x : y) : (x < y ? x : y); } __syncthreads(); }
    if (b == 0) {
        const unsigned long long A = sa[0];
        unsigned long long t; int n = 0; do { asm volatile("s_memrealtime %0\n\ts_waitcnt lgkmcnt(0)" : "=s"(t) :: "memory"); } while (t - A < 6000 && ++n < 100000);
    }
}
extern "C" void kernel_launch(void* const* d_in, const int* in_sizes, int n_in, void* d_out, int out_size, void* d_ws, size_t ws_size, hipStream_t stream) {
    fused<<<256, 512, 0, stream>>>((const char*)d_in[0], (const char*)d_in[1], (const char*)d_in[2], (const char*)d_in[3], (const char*)d_in[4], (const char*)d_in[5], (char*)d_out, (char*)d_ws);
    k_probe<<<1, 1024, 0, stream>>>(1,1);
}
```

```cpp
#include <hip/hip_runtime.h>
#include <hip/hip_bf16.h>

typedef __bf16 bf16x8 __attribute__((ext_vector_type(8)));
typedef short s16x4 __attribute__((ext_vector_type(4)));
typedef float f32x16 __attribute__((ext_vector_type(16)));
typedef float f32x8 __attribute__((ext_vector_type(8)));
typedef float f32x4 __attribute__((ext_vector_type(4)));
typedef unsigned u32x4 __attribute__((ext_vector_type(4)));
typedef unsigned u32x2 __attribute__((ext_vector_type(2)));
typedef unsigned long long u64;
typedef __attribute__((address_space(1))) u64 gu64;
#define LAS __attribute__((address_space(3)))
#define DI __device__ __forceinline__
#define MFMA(a, b, c) __builtin_amdgcn_mfma_f32_32x32x16_bf16((a), (b), (c), 0, 0, 0)
#define MFMA16(a, b, c) __builtin_amdgcn_mfma_f32_16x16x32_bf16((a), (b), (c), 0, 0, 0)
#define RLX __ATOMIC_RELAXED, __HIP_MEMORY_SCOPE_AGENT

constexpr int XB = 2048 * 896, WB = 448 * 2688, OB = 448 * 896;
constexpr int STAGE = 32768, RING = 65536, LA = 0, LW = 57344, LR = 114688, LDS_BYTES = 163840;
constexpr int WS_F1 = 4096, WS_F2 = 65536, WS_X = 1 << 20, XT = 168 * 512, X_BYTES = 2 * 64 * XT, WS_O = 16 << 20, O_BYTES = 2 * 64 * 16 * 2048;
constexpr int OOB = 0x40000000;
constexpr int CODE_LINES = 214;
#define ROT 2
#define PF0 2
#define RAISE_A 11
#define RAISE_B 2
#define BV1 5
#define KH_DELAY 8
#define OPRE 10
#define OROT 0
constexpr int SPIN_MAX = 1 << 18;

#define STAMP(i)
#define CST(i)
#define STAMP_DECL
#define STAMP_FLUSH()

DI u32x4 srd(const void* p, unsigned bytes) { const u64 a = (u64)p; return (u32x4){(unsigned)a, (unsigned)(a >> 32) & 0xffffu, bytes, 0x00020000u}; }
DI __amdgpu_buffer_rsrc_t rsrc(const void* p, int bytes) { return __builtin_amdgcn_make_buffer_rsrc((void*)p, 0, bytes, 0x00020000); }
DI void dma16(u32x4 r, int lds, int voff, int soff) {
    asm volatile("s_mov_b32 m0, %0\n\ts_nop 0\n\tbuffer_load_dwordx4 %1, %2, %3 offen lds" :: "s"(lds), "v"(voff), "s"(r), "s"(soff) : "memory");
}
DI void dma16_dev(u32x4 r, int lds, int voff, int soff) {
    asm volatile("s_mov_b32 m0, %0\n\ts_nop 0\n\tbuffer_load_dwordx4 %1, %2, %3 offen sc1 lds" :: "s"(lds), "v"(voff), "s"(r), "s"(soff) : "memory");
}
DI void dma64_dev(u32x4 r, int lds, int voff, int soff) {
    asm volatile("s_mov_b32 m0, %0\n\ts_nop 0\n\tbuffer_load_dwordx4 %1, %2, %3 offen sc1 lds\n\tbuffer_load_dwordx4 %1, %2, %3 offen offset:1024 sc1 lds\n\t"
                 "buffer_load_dwordx4 %1, %2, %3 offen offset:2048 sc1 lds\n\tbuffer_load_dwordx4 %1, %2, %3 offen offset:3072 sc1 lds" :: "s"(lds), "v"(voff), "s"(r), "s"(soff) : "memory");
}
DI void dma32_dev(u32x4 r, int lds, int voff, int soff) {
    asm volatile("s_mov_b32 m0, %0\n\ts_nop 0\n\tbuffer_load_dwordx4 %1, %2, %3 offen sc1 lds\n\tbuffer_load_dwordx4 %1, %2, %3 offen offset:1024 sc1 lds"
                 :: "s"(lds), "v"(voff), "s"(r), "s"(soff) : "memory");
}
DI void store_dev(u32x4 v, __amdgpu_buffer_rsrc_t r, int voff, int soff) {
    __builtin_amdgcn_raw_buffer_store_b128(v, r, voff, soff, 16);
    asm volatile("s_nop 1" :: "v"(v) : "memory");
}
DI bf16x8 tr_frag(const char* a0, const char* a1) {
    s16x4 lo = __builtin_amdgcn_ds_read_tr16_b64_v4i16((LAS s16x4*)a0);
    s16x4 hi = __builtin_amdgcn_ds_read_tr16_b64_v4i16((LAS s16x4*)a1);
    return __builtin_bit_cast(bf16x8, __builtin_shufflevector(lo, hi, 0, 1, 2, 3, 4, 5, 6, 7));
}
DI bf16x8 pack8(const f32x16& v, int s) {
    f32x8 t = {v[8 * s], v[8 * s + 1], v[8 * s + 2], v[8 * s + 3], v[8 * s + 4], v[8 * s + 5], v[8 * s + 6], v[8 * s + 7]};
    return __builtin_convertvector(t, bf16x8);
}
DI float xhalf(float v) {
    const unsigned u = __builtin_bit_cast(unsigned, v);
    const auto r = __builtin_amdgcn_permlane32_swap(u, u, false, false);
    return __builtin_bit_cast(float, (threadIdx.x & 32) ? r[0] : r[1]);
}
#define WAITV(n) asm volatile("s_waitcnt vmcnt(" #n ")" ::: "memory")
#define WAITL0() asm volatile("s_waitcnt lgkmcnt(0)" ::: "memory")
#define BAR() do { asm volatile("" ::: "memory"); __builtin_amdgcn_s_barrier(); asm volatile("" ::: "memory"); } while (0)
#define SCHED() __builtin_amdgcn_sched_barrier(0)

#define SG(m, n) __builtin_amdgcn_sched_group_barrier(m, n, 0)
DI float fmx(float a, float b) { return __builtin_elementwise_maximum(a, b); }
typedef int i32x4 __attribute__((ext_vector_type(4)));
#define PIN(x) asm volatile("" : "+v"(x))
DI void raise(char* p, u64 tag) { (void)__hip_atomic_exchange((gu64*)p, tag, RLX); }
template <bool TWO_BARRIERS>
DI u64 poll(const char* p, u64 all, u64 need, u64 tag) {
    u64 v, zero, sv, left; unsigned cnt = SPIN_MAX;
#define POLL_LOOP ".Lpw%=:\n\ts_waitcnt vmcnt(0)\n\tv_cmp_ne_u64 %4, %6, %0\n\ts_and_b64 vcc, %4, %8\n\ts_cbranch_scc0 .Lpd%=\n\ts_sub_u32 %3, %3, 1\n\ts_cbranch_scc1 .Lpd%=\n\t" \
                  "global_atomic_add_x2 %0, %5, %1, off sc0\n\ts_branch .Lpw%=\n.Lpd%=:\n\ts_mov_b64 exec, %2"
#define POLL_OPS : "=&v"(v), "=&v"(zero), "=&s"(sv), "+s"(cnt), "=&s"(left) : "v"(p), "s"(tag), "s"(all), "s"(need) : "vcc", "scc", "memory"
    if (TWO_BARRIERS)
        asm volatile("s_mov_b64 %2, exec\n\ts_mov_b64 exec, %7\n\tv_mov_b64 %1, 0\n\tglobal_atomic_add_x2 %0, %5, %1, off sc0\n\ts_barrier\n\ts_barrier\n" POLL_LOOP POLL_OPS);
    else
        asm volatile("s_mov_b64 %2, exec\n\ts_mov_b64 exec, %7\n\tv_mov_b64 %1, 0\n\tglobal_atomic_add_x2 %0, %5, %1, off sc0\n" POLL_LOOP POLL_OPS);
    return left;
}

extern "C" __global__ void __launch_bounds__(512, 2)
fused(const char* xa, const char* wa, const char* woa, const char* xb, const char* wb, const char* wob, char* out, char* ws) {
    __shared__ __attribute__((aligned(16))) char smem[LDS_BYTES];
    STAMP_DECL;
    STAMP(0);
    const int b = blockIdx.x, xcd = b & 7, idx = b >> 3;
    const int br = xcd & 1, jj = idx >> 3, ty = idx & 7, j = (xcd >> 1) * 4 + jj;
    const int wave = __builtin_amdgcn_readfirstlane(threadIdx.x >> 6), lane = threadIdx.x & 63;
    const int r3 = lane >> 3, u = lane & 7;
    const int lbase = (int)(unsigned)(size_t)(LAS char*)smem;
    u64 tag = *(const __attribute__((address_space(4))) u64*)ws;
    auto make_tag = [&]() { asm volatile("" : "+s"(tag)); tag = tag * 0x9E3779B97F4A7C15ull + 0x7F4A7C159E3779B9ull; asm volatile("" : "+s"(tag)); };
    const int w4 = wave & 3;
    const bool aux = wave >= 4;
    const int hh = lane >> 5, t = lane & 31, blk = (lane >> 4) & 1, q = (lane >> 2) & 3, p = lane & 3;
    const int ha = (0x4ECA7530u >> (4 * ty)) & 15, hb = (0xBFD98621u >> (4 * ty)) & 15;
    const int ca = (7 * ha) >> 1, cb_ = (7 * hb) >> 1;
    const int cs = min(ca, cb_);
    const int T = 4 * j + w4;
    const bool local = br == 0 && ty < 7;
    const int h = br ? hb : ha, c0 = br ? cb_ : ca;
    const __amdgpu_buffer_rsrc_t rxs = rsrc(ws + WS_X, X_BYTES);
    const int wb0 = (8 * hh + q) * 128 + ((blk * 2 + (p & 1)) << 4) + (p >> 1) * 8 + ((q >> 1) << 6), wb1 = wb0 ^ 64;

    const u32x4 rx = srd(br ? xb : xa, XB), rw = srd(br ? wb : wa, WB);
    const int vx0 = r3 * 896 + ((u ^ (r3 >> 1)) << 4), vx1 = vx0 ^ 64;
    const int vw = r3 * 2688 + ((u ^ (((r3 >> 1) & 1) << 2)) << 4);
    const int rot = ROT == 2 ? ((ty + j) & 1) * 4 : 0;
    const int sx = j * (128 * 896), sw = ty * 128;
    if (ty < 7) {
        dma16(rx, lbase + RING + wave * 2048, vx0, sx + wave * 14336 + rot * 128);
        dma16(rx, lbase + RING + wave * 2048 + 1024, vx1, sx + wave * 14336 + 7168 + rot * 128);
#pragma unroll
        for (int cb = 0; cb < 2; ++cb) dma16(rw, lbase + RING + 16384 + cb * 8192 + wave * 1024, vw, sw + (rot * 64 + wave * 8) * 2688 + cb * 896);
    } else {
        u64 pc; unsigned pco;
        asm volatile("s_getpc_b64 %0\n.Lhere%=:\n\ts_mov_b32 %1, .Lhere%=-fused" : "=s"(pc), "=s"(pco));
        unsigned d0 = 0;
        if ((int)threadIdx.x < CODE_LINES) asm volatile("global_load_dword %0, %1, %2" : "+v"(d0) : "v"(threadIdx.x * 128), "s"(pc - pco) : "memory");
        {
            const int ln = jj * 392 + (threadIdx.x < 392 ? threadIdx.x : 0), row = (ln * 9363) >> 16;
            const int vl = row * 2688 + 1792 + (ln - row * 7) * 128;
            asm volatile("global_load_dword %0, %1, %2" : "+v"(d0) : "v"(vl), "s"(br ? wb : wa) : "memory");
            asm volatile("global_load_dword %0, %1, %2" : "+v"(d0) : "v"(vl + 224 * 2688), "s"(br ? wb : wa) : "memory");
        }
        const int wl = (jj * 784 + (threadIdx.x < 392 ? threadIdx.x : 0)) * 128;
        asm volatile("global_load_dword %0, %1, %2" : "+v"(d0) : "v"(wl), "s"(br ? wob : woa) : "memory");
        asm volatile("global_load_dword %0, %1, %2" : "+v"(d0) : "v"(wl + 392 * 128), "s"(br ? wob : woa) : "memory");
        asm volatile("s_waitcnt vmcnt(0)" : "+v"(d0) :: "memory");
    }

    const unsigned NEGB = 0xF149F2CAu;
    f32x16 cd, co;
    u32x4 pm[2];
    int nd, no, lm;
    auto mask_bits = [&]() {
        const unsigned lo = (2u << t) - 1u;
        nd = (int)(~lo >> (4 * hh)); lm = (int)(lo >> (4 * hh)); no = T < 8 ? -1 : lm;
    };
    auto mask_reg = [&](int r) {
        const int bit = (r & 3) + 8 * (r >> 2);
        float a = __builtin_bit_cast(float, __builtin_amdgcn_sbfe(nd, bit, 1) & NEGB), c = __builtin_bit_cast(float, __builtin_amdgcn_sbfe(no, bit, 1) & NEGB);
        PIN(a); PIN(c); cd[r] = a; co[r] = c;
        if (r & 1) { unsigned m = __builtin_amdgcn_perm(__builtin_amdgcn_sbfe(lm, bit, 1), __builtin_amdgcn_sbfe(lm, bit - 1, 1), 0x05040100u); PIN(m); pm[r >> 3][(r >> 1) & 3] = m; }
    };
    auto koff = [&](int cf, int s) { return (2 * s + hh) * 512 + ((((t >> 2) ^ ((cf + 2 * s + hh) & 3)) << 2) + (t & 3)) * 16; };
    auto voff = [&](int hd, int cf, int tt) {
        const int pc8 = (4 * blk + 2 * (p & 1) + (p >> 1) + (hd & 1)) & 7, cc = pc8 >> 1;
        return cc * 512 + ((((2 * tt + hh) ^ ((cf + cc) & 3)) << 2) + q) * 16 + (pc8 & 1) * 8;
    };
    auto qfix = [&](int hd, u32x4& q0, u32x4& q1) {
        const bool z0 = (hd & 1) && !hh, z1 = !(hd & 1) && hh;
        q0.x = z0 ? 0u : q0.x; q0.y = z0 ? 0u : q0.y; q1.z = z1 ? 0u : q1.z; q1.w = z1 ? 0u : q1.w;
    };
    auto ex = [&](f32x16& s) {
#pragma unroll
        for (int r = 0; r < 16; ++r) s[r] = __builtin_amdgcn_exp2f(s[r]);
    };
    auto rowmax = [&](const f32x16& s) {
        float m = fmx(s[0], s[1]);
#pragma unroll
        for (int r = 2; r < 16; r += 2) m = fmx(fmx(m, s[r]), s[r + 1]);
        const unsigned mu = __builtin_bit_cast(unsigned, m);
        const auto r = __builtin_amdgcn_permlane32_swap(mu, mu, false, false);
        const unsigned r0 = r[0], r1 = r[1];
        return fmx(__builtin_bit_cast(float, r0), __builtin_bit_cast(float, r1));
    };
    const __amdgpu_buffer_rsrc_t ros = rsrc(ws + WS_O, O_BYTES);
    auto put_o = [&](f32x16 o, float l, int rec) {
        o = o * __builtin_amdgcn_rcpf(l);
        u32x4 o1 = __builtin_bit_cast(u32x4, pack8(o, 1));
        o1.z = hh ? 0u : o1.z; o1.w = hh ? 0u : o1.w;
        store_dev(__builtin_bit_cast(u32x4, pack8(o, 0)), ros, lane * 16, rec);
        store_dev(o1, ros, lane * 16 + 1024, rec);
    };
    auto ones = [&](int back, bool two) {
        const int bb = back + (two ? hh : 0);
        if (two || !hh) *(u32x2*)(smem + LR + w4 * 4096 + (2 - bb) * 16384 + 2048 + ((h & 1) ? 0 : 1544) + t * 16) = (u32x2){j >= bb ? 0x3F80u : 0u, 0u};
    };
    LAS char* wd = (LAS char*)(smem + LW + 40960 + 7 * 512 + 8);

    if (__builtin_expect(aux, 1)) {
        int ch = rot, sg = 0;
        auto chunk = [&]() {
            if (ty < 7) {
                ++ch; if (ch == 7) ch = 0;
                ++sg; if (sg == 3) sg = 0;
                const int st = lbase + RING + sg * STAGE;
#pragma unroll
                for (int i = 0; i < 4; ++i) dma16(rx, st + w4 * 4096 + i * 1024, (i & 1) ? vx1 : vx0, sx + (w4 * 4 + i) * 7168 + ch * 128);
#pragma unroll
                for (int cb = 0; cb < 2; ++cb)
#pragma unroll
                    for (int i = 0; i < 2; ++i) dma16(rw, st + 16384 + cb * 8192 + w4 * 2048 + i * 1024, vw, sw + (ch * 64 + w4 * 16 + i * 8) * 2688 + cb * 896);
            }
        };
        auto landed = [&]() { if (ty < 7) { WAITV(8); BAR(); } };
#pragma unroll 1
        for (int c = 1; c < 3; ++c) { chunk(); landed(); }
        chunk();
        make_tag();
        int tz = ty, lz = lane;
        asm volatile("" : "+s"(tz)); PIN(lz);
        const bool x7 = tz == 7;
        int nb = x7 ? 15 : (0xF63F30Fu >> (4 * tz)) & 15;
        int sl = (tz == 2 || tz == 5) ? 3 : 0;
        const char* f1;
        u64 mall, mb;
        {
            const int bk = (lz >> 3) & 3, pi = (lz >> 2) & 1;
            const int pg = pi * (x7 ? 1 : (br ? nb : 15) - tz) + (x7 ? 1 + 3 * br : tz);
            const u64 two = (x7 || (br && nb != 15)) ? 0xFFull : 0x0Full;
            mb = local ? 0 : ((x7 ? 0xFFull : two & 0xF0ull) | (j ? (two & 0x88ull) << 8 : 0)) << 32;
            mall = mb | two | (j >= 1 ? two << 8 : 0) | (j >= 2 ? two << 16 : 0);
            f1 = ws + WS_F1 + ((lz >> 5) * 16 + j - bk) * 256 + pg * 32 + (lz & 3) * 8;
            PIN(f1); asm volatile("" : "+s"(mall), "+s"(mb), "+s"(nb), "+s"(sl));
        }
        landed();
        chunk();
        asm volatile("" : "+s"(tz));
        int hd[2] = {x7 ? h : ha, x7 ? h : hb}, cf[2] = {x7 ? c0 : ca, x7 ? c0 : cb_};
        int po[2] = {x7 ? 0 : (cf[0] - cs) * 512, x7 ? 0 : (cf[1] - cs) * 512};
        const int rt = (64 + T) * XT;
        int ra0 = T * XT + (56 + c0) * 512, ra1 = j >= 1 ? ra0 - 4 * XT : OOB, ra2 = j >= 2 ? ra0 - 8 * XT : OOB, lh = lbase + LR + w4 * 4096;
        int rb0 = rt + (56 + (x7 ? c0 : cb_ + sl)) * 512, rb1 = T == 0 ? OOB : (!x7 && w4 == 0 ? rt + (56 + cs) * 512 : rb0) - XT;
        int lb0 = lbase + LW + w4 * 8192 + 8192 + (x7 ? 0 : po[1] + sl * 512), lb1 = (!x7 && w4 == 0) ? lbase + LW : lb0 - 8192;
        int rq = rt + cf[1] * 512, l16 = lz * 16;
        asm volatile("" : "+s"(ra0), "+s"(ra1), "+s"(ra2), "+s"(lh), "+s"(rb0), "+s"(rb1), "+s"(lb0), "+s"(lb1), "+s"(rq)); PIN(l16);
        asm volatile("" : "+s"(hd[0]), "+s"(hd[1]), "+s"(cf[0]), "+s"(cf[1]), "+s"(po[0]), "+s"(po[1]));
        landed();
        chunk();
        const int g4 = lz >> 4, c16 = lz & 15;
        int fo[2], ko[2], qo[2], vo[2][2], qs;
        f32x4 md, mo;
        const bool pt = x7 || (nb != 15 && g4 == sl);
#pragma unroll
        for (int e = 0; e < 2; ++e) {
            fo[e] = g4 * 512 + (((c16 >> 1) ^ ((cf[e] + g4) & 3)) << 6) + (c16 & 1) * 32;
            ko[e] = LW + w4 * 8192 + po[e] + fo[e]; qo[e] = LW + 40960 + w4 * 4096 + po[e] + fo[e]; PIN(ko[e]); PIN(qo[e]);
#pragma unroll
            for (int s = 0; s < 2; ++s) {
                const int pc8 = (2 * p + s + (hd[e] & 1)) & 7, cc = pc8 >> 1;
                vo[e][s] = LW + w4 * 8192 + 4096 + po[e] + cc * 512 + (((2 * g4 + (q >> 1)) ^ ((cf[e] + cc) & 3)) << 6) + (q & 1) * 32 + (pc8 & 1) * 8; PIN(vo[e][s]);
            }
        }
        qs = pt ? fo[1] : OOB; PIN(qs);
        landed();
        chunk();
        landed();
        if (ty < 7) {
#pragma unroll 1
            for (int c = 0; c < 7; ++c) {
                ++ch; if (ch == 7) ch = 0;
#pragma unroll
                for (int i = 0; i < 2; ++i) dma16(rw, lbase + LA + c * 8192 + w4 * 2048 + i * 1024, vw, sw + (ch * 64 + w4 * 16 + i * 8) * 2688 + 1792);
                if (c == 3) { WAITV(8); BAR(); }
            }
            BAR();
            WAITV(6); BAR();
        }
        if (wave == 4 && lane < 7) *(volatile LAS u64*)(wd + lane * 16) = 0;
        asm volatile("s_waitcnt vmcnt(0) lgkmcnt(0)" ::: "memory"); BAR();
        STAMP(4);
#pragma unroll
        for (int r = 0; r < 4; ++r) {
            const int d = c16 - 4 * g4 - r;
            md[r] = (unsigned)d < 8u ? 0.f : -1.0e30f; mo[r] = (d < -8 && T) ? 0.f : -1.0e30f;
        }
        PIN(md); PIN(mo);
        const u32x4 rxx = srd(ws + WS_X, X_BYTES);
        auto tell = [&](int n) { if (lane == 0) *(volatile LAS u64*)(wd + 64 + n * 16) = tag | 1; };
        auto hear = [&](int n) {
#pragma unroll 1
            for (int i = 0; i < SPIN_MAX * 4; ++i) { if (__all(*(volatile LAS u64*)(wd + 64 + n * 16) == (tag | 1))) break; __builtin_amdgcn_s_sleep(1); }
            asm volatile("" ::: "memory");
        };
        auto fetch_b = [&](int v) {
            const int go = v * (56 * 512), lo = v * 4096;
            if (x7) { dma32_dev(rxx, lb0 + lo, l16, rb0 + go); dma32_dev(rxx, lb1 + lo, l16, rb1 + go); }
            else {
                if (w4 == 0) dma64_dev(rxx, lb1 + lo, l16, rb1 + go);
                if (nb != 15 && lane < 32) { dma16_dev(rxx, lb0 + lo, l16, rb0 + go); if (w4) dma16_dev(rxx, lb1 + lo, l16, rb1 + go); }
            }
        };
        auto fetch_a = [&](int v) {
            const int go = v * (56 * 512), lo = v * 2048;
            dma32_dev(rxx, lh + 32768 + lo, l16, ra0 + go);
            dma32_dev(rxx, lh + 16384 + lo, l16, ra1 + go);
            dma32_dev(rxx, lh + lo, l16, ra2 + go);
        };
        STAMP(5);
        unsigned left = 1;
        if (!local) { if (wave == 4) { left = __builtin_amdgcn_readfirstlane((unsigned)poll<false>(f1, mall, mb, tag)); tell(0); if (!left) tell(1); } else hear(0); }
        STAMP(2);
        u32x4 qp[2];
        asm volatile("buffer_load_dwordx4 %0, %2, %3, %4 offen sc1\n\tbuffer_load_dwordx4 %1, %2, %3, %4 offen offset:16 sc1"
                     : "=&v"(qp[0]), "=&v"(qp[1]) : "v"(qs), "s"(rxx), "s"(local ? OOB : rq) : "memory");
        if (!local) fetch_b(0);
        if (wave == 4) { if (left) { poll<false>(f1, mall ^ mb, mall ^ mb, tag); tell(1); } } else hear(1);
        STAMP(6);
        dma32_dev(rxx, lh + 2048, l16, ra0 - 56 * 512);
        fetch_a(0);
        WAITV(8);
        PIN(qp[0]); PIN(qp[1]);
        STAMP(3);
        bf16x8 pe[2][2];
        float ls[2][2];
        if (!local) {
            f32x4 sd[2][2], so[2][2];
#pragma unroll
            for (int e = 0; e < 2; ++e)
#pragma unroll
                for (int y = 0; y < 2; ++y) {
                    const char* kb = smem + ko[e] + y * 16;
                    u32x4 qv = *(const u32x4*)(smem + qo[e] + y * 16);
                    const bool a = pt && (e || x7), z0 = (hd[e] & 1) && g4 == 0, z1 = !(hd[e] & 1) && g4 == 3;
                    qv = (u32x4){a ? qp[y].x : qv.x, a ? qp[y].y : qv.y, a ? qp[y].z : qv.z, a ? qp[y].w : qv.w};
                    qv = (u32x4){z0 ? 0u : qv.x, z0 ? 0u : qv.y, z1 ? 0u : qv.z, z1 ? 0u : qv.w};
                    sd[e][y] = MFMA16(*(const bf16x8*)(kb + 8192), __builtin_bit_cast(bf16x8, qv), md);
                    so[e][y] = MFMA16(*(const bf16x8*)kb, __builtin_bit_cast(bf16x8, qv), mo);
                }
            const bf16x8 one = __builtin_bit_cast(bf16x8, (u32x4){0x3F803F80u, 0x3F803F80u, 0x3F803F80u, 0x3F803F80u});
            float m[4];
#pragma unroll
            for (int i = 0; i < 4; ++i) {
                const f32x4 a = sd[i >> 1][i & 1], c = so[i >> 1][i & 1];
                m[i] = fmx(fmx(fmx(fmx(a[0], a[1]), a[2]), fmx(fmx(a[3], c[0]), c[1])), fmx(c[2], c[3]));
            }
#pragma unroll
            for (int i = 0; i < 4; ++i) {
                const unsigned mu = __builtin_bit_cast(unsigned, m[i]);
                const auto r = __builtin_amdgcn_permlane32_swap(mu, mu, false, false);
                const unsigned r0 = r[0], r1 = r[1];
                m[i] = fmx(__builtin_bit_cast(float, r0), __builtin_bit_cast(float, r1));
            }
#pragma unroll
            for (int i = 0; i < 4; ++i) {
                const unsigned mu = __builtin_bit_cast(unsigned, m[i]);
                const auto r = __builtin_amdgcn_permlane16_swap(mu, mu, false, false);
                const unsigned r0 = r[0], r1 = r[1];
                m[i] = fmx(__builtin_bit_cast(float, r0), __builtin_bit_cast(float, r1));
            }
            SCHED();
            f32x4 sm[4];
#pragma unroll
            for (int i = 0; i < 4; ++i) {
                const f32x4 a = sd[i >> 1][i & 1], c = so[i >> 1][i & 1];
                f32x8 x = {a[0], a[1], a[2], a[3], c[0], c[1], c[2], c[3]};
                x -= m[i];
#pragma unroll
                for (int r = 0; r < 8; ++r) x[r] = __builtin_amdgcn_exp2f(x[r]);
                pe[i >> 1][i & 1] = __builtin_convertvector(x, bf16x8);
                sm[i] = MFMA16(one, pe[i >> 1][i & 1], (f32x4){});
                SCHED();
            }
#pragma unroll
            for (int i = 0; i < 4; ++i) ls[i >> 1][i & 1] = __builtin_amdgcn_rcpf(sm[i][0]);
#pragma unroll
            for (int i = 0; i < 4; ++i) { PIN(pe[i >> 1][i & 1]); PIN(ls[i >> 1][i & 1]); }
        }
        STAMP(10);
        asm volatile("s_waitcnt vmcnt(0) lgkmcnt(0)" ::: "memory");
        STAMP(7);
        __builtin_amdgcn_s_setprio(2);
        if (wave == 4) { poll<true>(f1 + 8192, mall, mall, tag); tell(2); } else { BAR(); BAR(); hear(2); }
        STAMP(14);
        if (!local) fetch_b(1);
        fetch_a(1);
        if (ty < 7) {
            const int vwo = r3 * 896 + ((u ^ (((r3 >> 1) & 1) << 2)) << 4);
            const u32x4 rwo = srd(br ? wob : woa, OB);
#pragma unroll 1
            for (int n = w4; n < 24; n += 4) dma16(rwo, lbase + LA + n * 1024, vwo, n * 7168 + ty * 128);
            WAITV(6);
        } else WAITV(0);
        ones(0, true); ones(2, false);
        WAITL0();
        if (lane == 0) *(volatile LAS u64*)(wd + w4 * 16) = tag | 1;
        STAMP(9);
        if (!local) {
            u32x4 ov[2][2];
#pragma unroll
            for (int e = 0; e < 2; ++e)
#pragma unroll
                for (int y = 0; y < 2; ++y) {
                    const char* v0 = smem + vo[e][0] + y * 16; const char* v1 = smem + vo[e][1] + y * 16;
                    const f32x4 a = MFMA16(tr_frag(v0 + 8192, v0), pe[e][y], (f32x4){}), c = MFMA16(tr_frag(v1 + 8192, v1), pe[e][y], (f32x4){});
                    f32x8 x = {a[0], a[1], a[2], a[3], c[0], c[1], c[2], c[3]};
                    x *= ls[e][y];
                    ov[e][y] = __builtin_bit_cast(u32x4, __builtin_convertvector(x, bf16x8));
                    ov[e][y].z = g4 == 3 ? 0u : ov[e][y].z; ov[e][y].w = g4 == 3 ? 0u : ov[e][y].w;
                }
#pragma unroll
            for (int e = 0; e < 2; ++e)
#pragma unroll
                for (int y = 0; y < 2; ++y) store_dev(ov[e][y], ros, g4 * 512 + c16 * 16 + y * 256, ((64 + T) * 16 + hd[e]) * 2048);
        }
        STAMP(11);
        __builtin_amdgcn_s_setprio(0);
        if (ty < 7) {
            const int vwo = r3 * 896 + ((u ^ (((r3 >> 1) & 1) << 2)) << 4);
            const u32x4 rwo = srd(br ? wob : woa, OB);
#pragma unroll 1
            for (int n = 24 + w4; n < 57 - br; n += 4) dma16(rwo, lbase + LA + n * 1024, vwo, n * 7168 + ty * 128);
            if (br) { WAITV(8); if (lane < 2) raise(ws + WS_F2 + (64 + T) * 128 + (lane ? hb : ha) * 8, tag); }
            WAITV(0);
        } else {
            WAITV(0);
            if (lane == 0) raise(ws + WS_F2 + (64 + T) * 128 + h * 8, tag);
            STAMP(13);
            if (br == 0 && j == 0 && wave == 4) {
                poll<false>(ws + WS_F2 + (4 * j + lane) * 128 + h * 8, 15, 15, tag);
                __builtin_amdgcn_s_sleep(16);
#pragma unroll 1
                for (int n = 0; n < SPIN_MAX; ++n) {
                    u64 v[16];
                    const u64 zero = 0;
#pragma unroll
                    for (int i = 0; i < 16; ++i) asm volatile("global_atomic_add_x2 %0, %1, %2, %3 offset:%4 sc0" : "=&v"(v[i]) : "v"(lane * 8 + (i >> 3) * 4096), "v"(zero), "s"(ws + WS_F2), "i"((i & 7) * 512) : "memory");
                    asm volatile("s_waitcnt vmcnt(0)" : "+v"(v[0]), "+v"(v[1]), "+v"(v[2]), "+v"(v[3]), "+v"(v[4]), "+v"(v[5]), "+v"(v[6]), "+v"(v[7]),
                                 "+v"(v[8]), "+v"(v[9]), "+v"(v[10]), "+v"(v[11]), "+v"(v[12]), "+v"(v[13]), "+v"(v[14]), "+v"(v[15]) :: "memory");
                    bool ok = true;
#pragma unroll
                    for (int i = 0; i < 16; ++i) ok &= v[i] == tag;
                    if (__all(ok)) break;
                    __builtin_amdgcn_s_sleep(8);
                }
                if (lane == 0) __hip_atomic_store((gu64*)ws, tag, RLX);
            }
            STAMP(18);
            STAMP_FLUSH();
            return;
        }
    } else {
        make_tag();
        mask_bits();
        if (ty < 7) {
            int xo[2][4], wo_[2][2];
#pragma unroll
            for (int g = 0; g < 2; ++g) {
#pragma unroll
                for (int ks = 0; ks < 4; ++ks) { xo[g][ks] = RING + g * 65536 + (w4 * 32 + t) * 128 + (((2 * ks + hh) ^ ((t >> 1) & 7)) << 4); PIN(xo[g][ks]); }
                wo_[g][0] = RING + g * 65536 + wb0; wo_[g][1] = RING + g * 65536 + wb1; PIN(wo_[g][0]); PIN(wo_[g][1]);
            }
            int es[2], el[2][2];
#pragma unroll
            for (int s = 0; s < 2; ++s) {
                const int sw16 = ((((t >> 2) ^ (2 * s + hh)) << 2) + (t & 3)) * 16;
                es[s] = (2 * s + hh) * 512 + sw16; PIN(es[s]);
#pragma unroll
                for (int e = 0; e < 2; ++e) {
                    const int ps = 8 * ty + 4 * e + 2 * s + hh - cs;
                    el[e][s] = (unsigned)ps < 8u ? ps * 512 + sw16 : -1; PIN(el[e][s]);
                }
            }
            unsigned pq = br ? (0x1189188u >> (4 * ty)) & 15u : 15u, pk = w4 != 3 ? pq : 15u;
            int g0 = (br * 64 + T) * XT + ty * 4096;
            char* f1 = ws + WS_F1 + (br * 16 + j) * 256 + ty * 32 + w4 * 8;
            asm volatile("" : "+s"(pq), "+s"(pk), "+s"(g0), "+s"(f1));
            auto put = [&](const f32x16& a0, const f32x16& a1, int sec, unsigned pub, u32x4 (&d)[2][2]) {
#pragma unroll
                for (int e = 0; e < 2; ++e)
#pragma unroll
                    for (int s = 0; s < 2; ++s) {
                        d[e][s] = __builtin_bit_cast(u32x4, pack8(e ? a1 : a0, s));
                        if ((pub >> (2 * e + s)) & 1) store_dev(d[e][s], rxs, es[s] + e * 2048, g0 + sec * (56 * 512));
                    }
            };
            const int spare = LR + (4 + w4) * 4096 + 2048 + lane * 16;
            auto keep1 = [&](int lo, int e, int s, const u32x4& d) { *(u32x4*)(smem + (el[e][s] >= 0 ? el[e][s] + lo : spare)) = d; };
            auto keep = [&](int lo, const u32x4 (&d)[2][2]) {
#pragma unroll
                for (int e = 0; e < 2; ++e)
#pragma unroll
                    for (int s = 0; s < 2; ++s) keep1(lo, e, s, d[e][s]);
            };
            f32x16 acc[4];
            bf16x8 xf[28], wf[2][4];
            const f32x16 zero = {};
            WAITV(0); BAR();
            STAMP(1); CST(20);
            auto rd = [&](int n) {
                const int sg = (n >> 2) % 3, ks = n & 3;
                const char* st = smem + (sg & 1) * STAGE;
#pragma unroll
                for (int i = 3; i >= 0; --i) {
                    const char* a = st + wo_[sg >> 1][i & 1] + 16384 + (i >> 1) * 8192 + ks * 2048;
                    wf[n & 1][i] = tr_frag(a, a + 512);
                    if (i == 3) xf[n] = *(const bf16x8*)(st + xo[sg >> 1][ks]);
                }
            };
            rd(0);
            SCHED();
#pragma unroll
            for (int n = 0; n < 28; ++n) {
                if ((n & 3) == 3) { WAITL0(); BAR(); SCHED(); }
                if (n < 27) rd(n + 1);
#pragma unroll
                for (int i = 3; i >= 0; --i) acc[i] = MFMA(wf[n & 1][i], xf[n], n ? acc[i] : zero);
                if (n < 27) { SG(0x8, 1); SG(0x100, 3); SG(0x8, 1); SG(0x100, 3); SG(0x8, 1); SG(0x100, 3); SG(0x8, 1); }
                SCHED();
            }
            STAMP(2);
            const float qs = 0.18898223650461361f * 1.4426950408889634f;
            {
                u32x4 dk[2][2], dq[2][2];
                put(acc[2], acc[3], 1, pk, dk);
                put(acc[0] * qs, acc[1] * qs, 0, pq, dq);
                if (br) { keep(LW + 8192 + w4 * 8192, dk); keep(LW + 40960 + w4 * 4096, dq); }
            }
            STAMP(3);
            BAR();
            auto rv = [&](int n) {
#pragma unroll
                for (int i = 0; i < 2; ++i) { const char* a = smem + LA + n * 2048 + (i ? wb1 : wb0); wf[n & 1][2 * ((n >> 1) & 1) + i] = tr_frag(a, a + 512); }
            };
            rv(0); rv(1); rv(2);
            PIN(nd); PIN(no); PIN(lm);
            SCHED();
#pragma unroll
            for (int n = 0; n < 28; ++n) {
                if (n == BV1) { BAR(); SCHED(); }
                if (n == RAISE_B || n == RAISE_A) { if ((n == RAISE_B) == (br != 0)) { WAITV(0); if (lane == 0) raise(f1, tag); STAMP(9); } SCHED(); }
#pragma unroll
                for (int i = 0; i < 2; ++i) acc[2 + i] = MFMA(wf[n & 1][2 * ((n >> 1) & 1) + i], xf[n], n ? acc[2 + i] : zero);
                if (n + 3 < 28) rv(n + 3);
                if (n >= 2 && n < 18) mask_reg(n - 2);
                SG(0x8, 1); SG(0x100, 2); SG(0x2, 4); SG(0x8, 1); SG(0x100, 2); SG(0x2, 4);
                SCHED();
            }
            STAMP(4); CST(21);
            {
                u32x4 dv[2][2];
                put(acc[2], acc[3], 2, pk, dv);
                if (br) keep(LW + 12288 + w4 * 8192, dv);
            }
            STAMP(5);
            WAITV(0);
            if (lane == 0) raise(f1 + 8192, tag);
        } else {
            BAR();
#pragma unroll
            for (int r = 0; r < 16; ++r) mask_reg(r);
        }
        const char* tl = smem + LR + w4 * 4096;
        int fo[2] = {koff(c0, 0), koff(c0, 1)}, vo[2] = {voff(h, c0, 0) + 2048, voff(h, c0, 1) + 2048};
        PIN(fo[0]); PIN(fo[1]); PIN(vo[0]); PIN(vo[1]);
        STAMP(6);
        WAITL0(); BAR();
        STAMP(7);
        __builtin_amdgcn_s_setprio(1);
        u32x4 qf[2];
        qf[0] = *(const u32x4*)(tl + 2048 + fo[0]); qf[1] = *(const u32x4*)(tl + 2048 + fo[1]);
        auto kfrag = [&](int n, int s) { return *(const bf16x8*)(tl + (8 - n) * 4096 + fo[s]); };
        auto vfrag = [&](int n, int s) { const char* vb = tl + (8 - n) * 4096 + s * 256; return tr_frag(vb + vo[0], vb + vo[1]); };
        bf16x8 k0 = kfrag(0, 0), k1 = kfrag(0, 1), k2 = kfrag(8, 0), k3 = kfrag(8, 1);
        WAITL0(); BAR();
        CST(19); STAMP(10);
        qfix(h, qf[0], qf[1]);
        const bf16x8 q0 = __builtin_bit_cast(bf16x8, qf[0]), q1 = __builtin_bit_cast(bf16x8, qf[1]);
        auto qk = [&](int n, const f32x16& c) { f32x16 r = MFMA(kfrag(n, 0), q0, c); return MFMA(kfrag(n, 1), q1, r); };
        f32x16 cm, sa, sb;
        bf16x8 pp[8][2];
        cd = MFMA(k0, q0, cd); co = MFMA(k2, q0, co); cd = MFMA(k1, q1, cd); co = MFMA(k3, q1, co);
        k0 = kfrag(1, 0); k1 = kfrag(1, 1);
#pragma unroll
        for (int r = 0; r < 16; ++r) cd[r] = fmx(cd[r], co[r]);
        {
            const float m = -rowmax(cd);
#pragma unroll
            for (int r = 0; r < 16; ++r) cm[r] = m;
        }
        cd += cm;
        sa = cd;
        SCHED();
#define STG(n, cur, nxt) do { \
            if (n < 7) { nxt = MFMA(k0, q0, cm); nxt = MFMA(k1, q1, nxt); } \
            if (n < 6) { k0 = kfrag(n + 2, 0); k1 = kfrag(n + 2, 1); } \
            ex(cur); pp[n][0] = pack8(cur, 0); pp[n][1] = pack8(cur, 1); \
            if (n < 7) SG(0x8, 1); SG(0x400, 4); if (n < 7) SG(0x8, 1); if (n < 6) SG(0x100, 2); SG(0x400, 4); SG(0x2, 2); SG(0x400, 4); SG(0x2, 2); SG(0x400, 4); SG(0x2, 4); SCHED(); } while (0)
        STG(0, sa, sb); STG(1, sb, sa); STG(2, sa, sb); STG(3, sb, sa); STG(4, sa, sb); STG(5, sb, sa); STG(6, sa, sb); STG(7, sb, sa);
        bf16x8 pz[2];
#pragma unroll
        for (int s = 0; s < 2; ++s) {
            const u32x4 e = __builtin_bit_cast(u32x4, pp[0][s]);
            pp[0][s] = __builtin_bit_cast(bf16x8, e & pm[s]); pz[s] = __builtin_bit_cast(bf16x8, e & ~pm[s]); PIN(pz[s]);
        }
#pragma unroll
        for (int n = 0; n < 8; ++n) { PIN(pp[n][0]); PIN(pp[n][1]); }
        char* f2 = ws + WS_F2 + T * 128 + h * 8;
        int orec = (T * 16 + h) * 2048;
        const char* va = tl + vo[0]; const char* vb = tl + vo[1];
        asm volatile("" : "+s"(f2), "+s"(orec));
        STAMP(8);
#pragma unroll 1
        for (int i = 0; i < SPIN_MAX; ++i) if (__all(*(volatile LAS u64*)(wd + p * 16) == (tag | 1))) break;
        asm volatile("" ::: "memory");
        STAMP(14);
        f32x16 o = {};
        auto pv = [&](int n, bf16x8 p0, bf16x8 p1) { o = MFMA(vfrag(n, 0), p0, o); o = MFMA(vfrag(n, 1), p1, o); };
        {
            bf16x8 vf[18];
            auto rdv = [&](int i) { const int off = (8 - (i >> 1)) * 4096 + (i & 1) * 256; vf[i] = tr_frag(va + off, vb + off); };
#pragma unroll
            for (int i = 0; i < 5; ++i) { rdv(i); SCHED(); }
#pragma unroll
            for (int i = 0; i < 18; ++i) {
                o = MFMA(vf[i], i < 16 ? pp[i >> 1][i & 1] : pz[i & 1], o);
                if (i + 5 < 18) rdv(i + 5);
                SCHED();
            }
        }
        auto publish = [&]() {
            const float lf = o[12];
            const unsigned lu = __builtin_bit_cast(unsigned, lf);
            const unsigned lx = __builtin_amdgcn_permlane32_swap(lu, lu, false, false)[1];
            put_o(o, __builtin_bit_cast(float, lx), orec);
        };
        publish();
        STAMP(11); CST(22);
        float big = fmx(__builtin_fabsf(o[0]), __builtin_fabsf(o[1]));
#pragma unroll
        for (int r = 2; r < 16; r += 2) big = fmx(fmx(big, __builtin_fabsf(o[r])), __builtin_fabsf(o[r + 1]));
        if (__builtin_expect(!__all(big < 1.0e36f), 0)) {
            const int nmid = T >= 7 ? 7 : T;
            float m = 0.f;
#pragma unroll 1
            for (int i = 1; i <= nmid; ++i) m = fmx(m, rowmax(qk(i, cm)));
            cd -= m; cm -= m;
            ex(cd);
            o = (f32x16){};
            const u32x4 e0 = __builtin_bit_cast(u32x4, pack8(cd, 0)), e1 = __builtin_bit_cast(u32x4, pack8(cd, 1));
            pv(0, __builtin_bit_cast(bf16x8, e0 & pm[0]), __builtin_bit_cast(bf16x8, e1 & pm[1]));
            pv(8, __builtin_bit_cast(bf16x8, e0 & ~pm[0]), __builtin_bit_cast(bf16x8, e1 & ~pm[1]));
#pragma unroll 1
            for (int i = 1; i <= nmid; ++i) {
                f32x16 s = qk(i, cm);
                ex(s); pv(i, pack8(s, 0), pack8(s, 1));
            }
            WAITV(0);
            publish();
        }
        WAITV(0);
        if (lane == 0) raise(f2, tag);
        __builtin_amdgcn_s_setprio(0);
        if (ty == 7) {
            STAMP(13);
            STAMP_FLUSH();
            return;
        }
    }
    STAMP(13);
    const int kh = (wave >> 2) ^ (OROT & ty & 1), kq = (OROT >> 1) & (OROT == 2 ? ty : ty >> 1) & 1;
    int wq[2][2], l16 = lane * 16, os[2];
#pragma unroll
    for (int g = 0; g < 2; ++g) {
        wq[g][0] = LA + (kh * 8 + (g ^ kq) * 4) * (28 * 128) + wb0; wq[g][1] = wq[g][0] ^ 64; PIN(wq[g][0]); PIN(wq[g][1]);
        os[g] = ((br * 64 + T) * 16 + kh * 8 + (g ^ kq) * 4) * 2048;
    }
    const int tr = br ? 2 * (t & 15) + (t >> 4) : t;
    int part = LR + w4 * 8192 + l16, tw = LR + 32768 + w4 * 4096 + tr * 128 + ((hh ^ (tr & 7)) << 4), tg = LR + 32768 + w4 * 4096 + r3 * 128 + ((u ^ r3) << 4);
    int ov = r3 * 1792 + u * 16, oo = T * (32 * 1792) + br * 896 + ty * 128;
    asm volatile("" : "+v"(l16), "+v"(part), "+v"(tw), "+v"(tg), "+v"(ov)); asm volatile("" : "+s"(oo), "+s"(os[0]), "+s"(os[1]));
    const __amdgpu_buffer_rsrc_t rout = rsrc(out, 2048 * 1792);
    if (wave == 4) poll<false>(ws + WS_F2 + (br * 64 + 4 * j) * 128 + lane * 8, ~0ull, ~0ull, tag);
    STAMP(12);
    BAR();
    STAMP(15);
    f32x16 acc[2] = {};
    u32x4 of[16];
    bf16x8 wf[16][2];
    auto ld = [&](int ks) { of[ks] = __builtin_amdgcn_raw_buffer_load_b128(ros, l16 + (ks & 3) * 1024, os[ks >> 3] + ((ks >> 2) & 1) * 4096, 16); };
    auto rdw = [&](int ks) {
        const int row = ((ks >> 1) & 3) * (28 * 128) + (ks & 1) * 2048;
#pragma unroll
        for (int i = 0; i < 2; ++i) { const char* a = smem + wq[ks >> 3][i] + row; wf[ks][i] = tr_frag(a, a + 512); }
    };
    auto step = [&](int ks) {
        acc[0] = MFMA(wf[ks][0], __builtin_bit_cast(bf16x8, of[ks]), acc[0]);
        if (ks + OPRE < 16) ld(ks + OPRE);
        if (ks + 3 < 16) rdw(ks + 3);
        acc[1] = MFMA(wf[ks][1], __builtin_bit_cast(bf16x8, of[ks]), acc[1]);
        SCHED();
    };
    if (aux) __builtin_amdgcn_s_sleep(KH_DELAY); else __builtin_amdgcn_s_setprio(1);
#pragma unroll
    for (int ks = 0; ks < OPRE; ++ks) { ld(ks); SCHED(); }
#pragma unroll
    for (int ks = 0; ks < 3; ++ks) rdw(ks);
    SCHED();
#pragma unroll
    for (int ks = 0; ks < 13; ++ks) step(ks);
    if (!aux) {
        step(13); step(14); step(15);
        STAMP(16);
#pragma unroll
        for (int i = 0; i < 8; ++i) *(f32x4*)(smem + part + i * 1024) = (f32x4){acc[i >> 2][4 * (i & 3)], acc[i >> 2][4 * (i & 3) + 1], acc[i >> 2][4 * (i & 3) + 2], acc[i >> 2][4 * (i & 3) + 3]};
        WAITL0(); BAR();
    } else {
        BAR();
        f32x4 pv[8];
#pragma unroll
        for (int i = 0; i < 8; ++i) pv[i] = *(const f32x4*)(smem + part + i * 1024);
        SCHED();
        step(13); step(14); step(15);
        STAMP(16);
#pragma unroll
        for (int i = 0; i < 8; ++i) { acc[i >> 2][4 * (i & 3)] += pv[i][0]; acc[i >> 2][4 * (i & 3) + 1] += pv[i][1]; acc[i >> 2][4 * (i & 3) + 2] += pv[i][2]; acc[i >> 2][4 * (i & 3) + 3] += pv[i][3]; }
#pragma unroll
        for (int i = 0; i < 2; ++i)
#pragma unroll
            for (int s = 0; s < 2; ++s) *(bf16x8*)(smem + (tw ^ ((4 * i + 2 * s) << 4))) = pack8(acc[i], s);
        WAITL0();
        u32x4 v[4];
#pragma unroll
        for (int i = 0; i < 4; ++i) v[i] = *(const u32x4*)(smem + tg + i * 1024);
        SCHED();
#pragma unroll
        for (int i = 0; i < 4; ++i) __builtin_amdgcn_raw_buffer_store_b128(v[i], rout, ov, oo + i * (8 * 1792), 16);
    }
    STAMP(17);
    STAMP_FLUSH();
}

extern "C" void kernel_launch(void* const* d_in, const int* in_sizes, int n_in, void* d_out, int out_size, void* d_ws, size_t ws_size, hipStream_t stream) {
    fused<<<256, 512, 0, stream>>>((const char*)d_in[0], (const char*)d_in[1], (const char*)d_in[2], (const char*)d_in[3], (const char*)d_in[4], (const char*)d_in[5], (char*)d_out, (char*)d_ws);
}
```

```cpp
#include <hip/hip_runtime.h>
#include <hip/hip_bf16.h>

typedef __bf16 bf16x8 __attribute__((ext_vector_type(8)));
typedef short s16x4 __attribute__((ext_vector_type(4)));
typedef float f32x16 __attribute__((ext_vector_type(16)));
typedef float f32x8 __attribute__((ext_vector_type(8)));
typedef float f32x4 __attribute__((ext_vector_type(4)));
typedef unsigned u32x4 __attribute__((ext_vector_type(4)));
typedef unsigned u32x2 __attribute__((ext_vector_type(2)));
typedef unsigned long long u64;
typedef __attribute__((address_space(1))) u64 gu64;
#define LAS __attribute__((address_space(3)))
#define DI __device__ __forceinline__
#define MFMA(a, b, c) __builtin_amdgcn_mfma_f32_32x32x16_bf16((a), (b), (c), 0, 0, 0)
#define MFMA16(a, b, c) __builtin_amdgcn_mfma_f32_16x16x32_bf16((a), (b), (c), 0, 0, 0)
#define RLX __ATOMIC_RELAXED, __HIP_MEMORY_SCOPE_AGENT

constexpr int XB = 2048 * 896, WB = 448 * 2688, OB = 448 * 896;
constexpr int STAGE = 32768, RING = 65536, LA = 0, LW = 57344, LR = 114688, LX = 106496, LDS_BYTES = 163840;
constexpr int WS_F1 = 4096, WS_F2 = 65536, WS_X = 1 << 20, XT = 168 * 512, X_BYTES = 2 * 64 * XT, WS_O = 16 << 20, O_BYTES = 2 * 64 * 16 * 2048;
constexpr int OOB = 0x40000000;
#define ROT 0
#define PF0 2
#define RAISE_A 27
#define RAISE_B 4
#define BV1 10
#define KH_DELAY 8
#define OPRE 10
#define OROT 0
#define XB0 5
#define YB0 3
#define YEARLY 1
#define PFA 0
constexpr int SPIN_MAX = 1 << 18;

enum { E_RA0, E_RA1, E_RA2, E_RB0, E_RB1, E_RQ, E_LB0, E_LB1, E_MA, E_MB, E_PG0, E_PGD, E_HD0, E_HD1, E_CF0, E_CF1, E_PO0, E_PO1, E_SL, E_H, E_C0, E_PS, E_PQ, E_PK };
struct Tab {
    int e[192][32];
    constexpr Tab() : e() {
        for (int i = 0; i < 192; ++i) {
            const int w4 = i & 3, ty = (i >> 2) & 7, br = (i >> 5) & 1, jc = i >> 6;
            const bool x7 = ty == 7, local = br == 0 && !x7;
            const int ha = (0x4ECA7530u >> (4 * ty)) & 15, hb = (0xBFD98621u >> (4 * ty)) & 15;
            const int ca = (7 * ha) >> 1, cb = (7 * hb) >> 1, cs = ca < cb ? ca : cb;
            const int h = br ? hb : ha, c0 = br ? cb : ca;
            const int nb = x7 ? 15 : (0xF63F30Fu >> (4 * ty)) & 15;
            const int sl = (ty == 2 || ty == 5) ? 3 : 0;
            const int two = (x7 || (br && nb != 15)) ? 0xFF : 0x0F;
            const int rt = (64 + w4) * XT;
            int* v = e[i];
            v[E_HD0] = x7 ? h : ha; v[E_HD1] = x7 ? h : hb; v[E_CF0] = x7 ? c0 : ca; v[E_CF1] = x7 ? c0 : cb;
            v[E_PO0] = x7 ? 0 : (ca - cs) * 512; v[E_PO1] = x7 ? 0 : (cb - cs) * 512;
            v[E_RA0] = w4 * XT + (56 + c0) * 512; v[E_RA1] = jc >= 1 ? v[E_RA0] - 4 * XT : OOB; v[E_RA2] = jc >= 2 ? v[E_RA0] - 8 * XT : OOB;
            v[E_RB0] = rt + (56 + (x7 ? c0 : cb + sl)) * 512;
            v[E_RB1] = (jc == 0 && w4 == 0) ? OOB : ((!x7 && w4 == 0) ? rt + (56 + cs) * 512 : v[E_RB0]) - XT;
            v[E_RQ] = local ? OOB : rt + v[E_CF1] * 512;
            v[E_LB0] = LW + 40960 - w4 * 8192 + (x7 ? 0 : v[E_PO1] + sl * 512); v[E_LB1] = (!x7 && w4 == 0) ? LX : v[E_LB0] + 8192;
            v[E_MA] = two | (jc >= 1 ? two << 8 : 0) | (jc >= 2 ? two << 16 : 0);
            v[E_MB] = local ? 0 : (x7 ? 0xFF : two & 0xF0) | (jc ? (two & 0x88) << 8 : 0);
            v[E_PG0] = (x7 ? 1 + 3 * br : ty) * 32; v[E_PGD] = (x7 ? 1 : (nb == 15 ? 0 : nb - ty)) * 8;
            v[E_SL] = nb == 15 ? 8 : sl;
            v[E_H] = h; v[E_C0] = c0; v[E_PS] = 8 * ty - cs;
            v[E_PQ] = br ? (0x1189188u >> (4 * ty)) & 15 : 15; v[E_PK] = w4 != 3 ? v[E_PQ] : 15;
        }
    }
};
__device__ const Tab g_tab = Tab();

#define STAMP(i)
#define CST(i)
#define STAMP_DECL
#define STAMP_FLUSH()

DI u32x4 srd(const void* p, unsigned bytes) { const u64 a = (u64)p; return (u32x4){(unsigned)a, (unsigned)(a >> 32) & 0xffffu, bytes, 0x00020000u}; }
DI __amdgpu_buffer_rsrc_t rsrc(const void* p, int bytes) { return __builtin_amdgcn_make_buffer_rsrc((void*)p, 0, bytes, 0x00020000); }
DI void dma16(u32x4 r, int lds, int voff, int soff) {
    asm volatile("s_mov_b32 m0, %0\n\ts_nop 0\n\tbuffer_load_dwordx4 %1, %2, %3 offen lds" :: "s"(lds), "v"(voff), "s"(r), "s"(soff) : "memory");
}
DI void dma16_dev(u32x4 r, int lds, int voff, int soff) {
    asm volatile("s_mov_b32 m0, %0\n\ts_nop 0\n\tbuffer_load_dwordx4 %1, %2, %3 offen sc1 lds" :: "s"(lds), "v"(voff), "s"(r), "s"(soff) : "memory");
}
DI void dma64_dev(u32x4 r, int lds, int voff, int soff) {
    asm volatile("s_mov_b32 m0, %0\n\ts_nop 0\n\tbuffer_load_dwordx4 %1, %2, %3 offen sc1 lds\n\tbuffer_load_dwordx4 %1, %2, %3 offen offset:1024 sc1 lds\n\t"
                 "buffer_load_dwordx4 %1, %2, %3 offen offset:2048 sc1 lds\n\tbuffer_load_dwordx4 %1, %2, %3 offen offset:3072 sc1 lds" :: "s"(lds), "v"(voff), "s"(r), "s"(soff) : "memory");
}
DI void dma32_dev(u32x4 r, int lds, int voff, int soff) {
    asm volatile("s_mov_b32 m0, %0\n\ts_nop 0\n\tbuffer_load_dwordx4 %1, %2, %3 offen sc1 lds\n\tbuffer_load_dwordx4 %1, %2, %3 offen offset:1024 sc1 lds"
                 :: "s"(lds), "v"(voff), "s"(r), "s"(soff) : "memory");
}
DI void store_dev(u32x4 v, __amdgpu_buffer_rsrc_t r, int voff, int soff) {
    __builtin_amdgcn_raw_buffer_store_b128(v, r, voff, soff, 16);
    asm volatile("s_nop 1" :: "v"(v) : "memory");
}
template <int N>
DI void dma_rows(u32x4 r, int lds, int voff, int soff) {
    asm volatile("s_mov_b32 m0, %0\n\ts_nop 0\n\tbuffer_load_dwordx4 %2, %3, %1 offen lds\n.rept %4\n\ts_add_u32 m0, m0, 0x1000\n\ts_add_u32 %1, %1, 0x7000\n\tbuffer_load_dwordx4 %2, %3, %1 offen lds\n.endr"
                 : "+s"(lds), "+s"(soff) : "v"(voff), "s"(r), "i"(N - 1) : "scc", "memory");
}
DI bf16x8 tr_frag(const char* a0, const char* a1) {
    s16x4 lo = __builtin_amdgcn_ds_read_tr16_b64_v4i16((LAS s16x4*)a0);
    s16x4 hi = __builtin_amdgcn_ds_read_tr16_b64_v4i16((LAS s16x4*)a1);
    return __builtin_bit_cast(bf16x8, __builtin_shufflevector(lo, hi, 0, 1, 2, 3, 4, 5, 6, 7));
}
DI bf16x8 pack8(const f32x16& v, int s) {
    f32x8 t = {v[8 * s], v[8 * s + 1], v[8 * s + 2], v[8 * s + 3], v[8 * s + 4], v[8 * s + 5], v[8 * s + 6], v[8 * s + 7]};
    return __builtin_convertvector(t, bf16x8);
}
DI float xhalf(float v) {
    const unsigned u = __builtin_bit_cast(unsigned, v);
    const auto r = __builtin_amdgcn_permlane32_swap(u, u, false, false);
    return __builtin_bit_cast(float, (threadIdx.x & 32) ? r[0] : r[1]);
}
#define WAITV(n) asm volatile("s_waitcnt vmcnt(" #n ")" ::: "memory")
#define WAITL0() asm volatile("s_waitcnt lgkmcnt(0)" ::: "memory")
#define BAR() do { asm volatile("" ::: "memory"); __builtin_amdgcn_s_barrier(); asm volatile("" ::: "memory"); } while (0)
#define SCHED() __builtin_amdgcn_sched_barrier(0)

#define SG(m, n) __builtin_amdgcn_sched_group_barrier(m, n, 0)
DI float fmx(float a, float b) { return __builtin_elementwise_maximum(a, b); }
typedef int i32x4 __attribute__((ext_vector_type(4)));
#define PIN(x) asm volatile("" : "+v"(x))
DI void raise(char* p, u64 tag) { (void)__hip_atomic_exchange((gu64*)p, tag, RLX); }
template <bool TWO_BARRIERS>
DI u64 poll(const char* p, u64 all, u64 need, u64 tag) {
    u64 v, zero, sv, left; unsigned cnt = SPIN_MAX;
#define POLL_LOOP ".Lpw%=:\n\ts_waitcnt vmcnt(0)\n\tv_cmp_ne_u64 %4, %6, %0\n\ts_and_b64 vcc, %4, %8\n\ts_cbranch_scc0 .Lpd%=\n\ts_sub_u32 %3, %3, 1\n\ts_cbranch_scc1 .Lpd%=\n\t" \
                  "global_atomic_add_x2 %0, %5, %1, off sc0\n\ts_branch .Lpw%=\n.Lpd%=:\n\ts_mov_b64 exec, %2"
#define POLL_OPS : "=&v"(v), "=&v"(zero), "=&s"(sv), "+s"(cnt), "=&s"(left) : "v"(p), "s"(tag), "s"(all), "s"(need) : "vcc", "scc", "memory"
    if (TWO_BARRIERS)
        asm volatile("s_mov_b64 %2, exec\n\ts_mov_b64 exec, %7\n\tv_mov_b64 %1, 0\n\tglobal_atomic_add_x2 %0, %5, %1, off sc0\n\ts_barrier\n\ts_barrier\n" POLL_LOOP POLL_OPS);
    else
        asm volatile("s_mov_b64 %2, exec\n\ts_mov_b64 exec, %7\n\tv_mov_b64 %1, 0\n\tglobal_atomic_add_x2 %0, %5, %1, off sc0\n" POLL_LOOP POLL_OPS);
    return left;
}

extern "C" __global__ void __launch_bounds__(512, 2)
fused(const char* xa, const char* wa, const char* woa, const char* xb, const char* wb, const char* wob, char* out, char* ws) {
    __shared__ __attribute__((aligned(16))) char smem[LDS_BYTES];
    STAMP_DECL;
    STAMP(0);
    const int b = blockIdx.x, xcd = b & 7, idx = b >> 3;
    const int br = xcd & 1, jj = idx >> 3, ty = idx & 7, j = (xcd >> 1) * 4 + jj;
    unsigned d0 = threadIdx.x * 128;
    {
        u64 pc;
        asm volatile("s_cmp_lg_u32 %2, 7\n\ts_cbranch_scc1 .Lskip%=\n\tv_cmp_gt_u32 vcc, fused_end-.Lhere%=, %1\n\ts_cbranch_vccz .Lskip%=\n\ts_mov_b64 exec, vcc\n\t"
                     "s_getpc_b64 %0\n.Lhere%=:\n\tglobal_load_dword %1, %1, %0\n\ts_mov_b64 exec, -1\n.Lskip%=:"
                     : "=&s"(pc), "+v"(d0) : "s"(ty) : "vcc", "scc", "memory");
    }
    const int wave = __builtin_amdgcn_readfirstlane(threadIdx.x >> 6), lane = threadIdx.x & 63;
    const int r3 = lane >> 3, u = lane & 7;
    const int lbase = (int)(unsigned)(size_t)(LAS char*)smem;
    int vx0 = r3 * 896 + ((u ^ (r3 >> 1)) << 4), vx1 = vx0 ^ 64;
    int vw = r3 * 2688 + ((u ^ (((r3 >> 1) & 1) << 2)) << 4);
    const int rot = ROT == 2 ? ((ty + j) & 1) * 4 : 0;
    const int sx = j * (128 * 896), sw = ty * 128;
    int e0 = sx + wave * 14336 + rot * 128, e1 = sw + (rot * 64 + wave * 8) * 2688, e2 = lbase + RING + wave * 2048, e3 = lbase + RING + 16384 + wave * 1024;
    asm volatile("" : "+s"(e0), "+s"(e1), "+s"(e2), "+s"(e3)); asm volatile("" : "+v"(vx0), "+v"(vx1), "+v"(vw));
    SCHED();
    const u32x4 rx = srd(br ? xb : xa, XB), rw = srd(br ? wb : wa, WB);
    if (ty < 7) {
        dma16(rx, e2, vx0, e0);
        dma16(rw, e3, vw, e1);
        dma16(rx, e2 + 1024, vx1, e0 + 7168);
        dma16(rw, e3 + 8192, vw, e1 + 896);
    } else {
        if (PFA) {
            const int ln = jj * 352 + (threadIdx.x < 352 ? threadIdx.x : 0), m = ln - 512, row = (m * 4682) >> 16;
            const char* tp = ln < 512 ? (br ? xb : xa) + ((xcd >> 1) * 512 + ln) * 896 : (br ? wb : wa) + row * 2688 + (m - row * 14) * 128;
            const int ts = ln < 512 ? 128 : 172032;
            if (PFA & 1) asm volatile("global_load_dword %0, %1, off" : "+v"(d0) : "v"(tp + 2 * ts) : "memory");
            if (PFA & 2) asm volatile("global_load_dword %0, %1, off" : "+v"(d0) : "v"(tp + 6 * ts) : "memory");
            if (PFA & 4) asm volatile("global_load_dword %0, %1, off" : "+v"(d0) : "v"(tp + 3 * ts) : "memory");
        }
        {
            const int ln = jj * 392 + (threadIdx.x < 392 ? threadIdx.x : 0), row = (ln * 9363) >> 16;
            const int vl = row * 2688 + 1792 + (ln - row * 7) * 128;
            asm volatile("global_load_dword %0, %1, %2" : "+v"(d0) : "v"(vl), "s"(br ? wb : wa) : "memory");
            asm volatile("global_load_dword %0, %1, %2" : "+v"(d0) : "v"(vl + 224 * 2688), "s"(br ? wb : wa) : "memory");
        }
        const int wl = (jj * 784 + (threadIdx.x < 392 ? threadIdx.x : 0)) * 128;
        asm volatile("global_load_dword %0, %1, %2" : "+v"(d0) : "v"(wl), "s"(br ? wob : woa) : "memory");
        asm volatile("global_load_dword %0, %1, %2" : "+v"(d0) : "v"(wl + 392 * 128), "s"(br ? wob : woa) : "memory");
        asm volatile("s_waitcnt vmcnt(0)" : "+v"(d0) :: "memory");
    }
    u64 tag = *(const __attribute__((address_space(4))) u64*)ws;
    auto make_tag = [&]() { asm volatile("" : "+s"(tag)); tag = tag * 0x9E3779B97F4A7C15ull + 0x7F4A7C159E3779B9ull; asm volatile("" : "+s"(tag)); };
    const int w4 = wave & 3;
    const bool aux = wave >= 4;
    const int hh = lane >> 5, t = lane & 31, blk = (lane >> 4) & 1, q = (lane >> 2) & 3, p = lane & 3;
    const int T = 4 * j + w4;
    const bool local = br == 0 && ty < 7;
    typedef int i32x8 __attribute__((ext_vector_type(8)));
    typedef int i32x16 __attribute__((ext_vector_type(16)));
    const __attribute__((address_space(4))) char* te = (const __attribute__((address_space(4))) char*)g_tab.e[(min(j, 2) * 2 + br) * 32 + ty * 4 + w4];
    i32x8 tb8 = *(const __attribute__((address_space(4))) i32x8*)(te + 64);
    int h, c0;
    auto use_tab = [&]() { asm volatile("" : "+s"(tb8)); h = tb8[E_H - 16]; c0 = tb8[E_C0 - 16]; };
    const __amdgpu_buffer_rsrc_t rxs = rsrc(ws + WS_X, X_BYTES);
    const int wb0 = (8 * hh + q) * 128 + ((blk * 2 + (p & 1)) << 4) + (p >> 1) * 8 + ((q >> 1) << 6), wb1 = wb0 ^ 64;

    const unsigned NEGB = 0xF149F2CAu;
    f32x16 cd, co;
    u32x4 pm[2];
    int nd, no, lm;
    auto mask_bits = [&]() {
        const unsigned lo = (2u << t) - 1u;
        nd = (int)(~lo >> (4 * hh)); lm = (int)(lo >> (4 * hh)); no = T < 8 ? -1 : lm;
    };
    auto mask_reg = [&](int r) {
        const int bit = (r & 3) + 8 * (r >> 2);
        float a = __builtin_bit_cast(float, __builtin_amdgcn_sbfe(nd, bit, 1) & NEGB), c = __builtin_bit_cast(float, __builtin_amdgcn_sbfe(no, bit, 1) & NEGB);
        PIN(a); PIN(c); cd[r] = a; co[r] = c;
        if (r & 1) { unsigned m = __builtin_amdgcn_perm(__builtin_amdgcn_sbfe(lm, bit, 1), __builtin_amdgcn_sbfe(lm, bit - 1, 1), 0x05040100u); PIN(m); pm[r >> 3][(r >> 1) & 3] = m; }
    };
    auto koff = [&](int cf, int s) { return (2 * s + hh) * 512 + ((((t >> 2) ^ ((cf + 2 * s + hh) & 3)) << 2) + (t & 3)) * 16; };
    auto voff = [&](int hd, int cf, int tt) {
        const int pc8 = (4 * blk + 2 * (p & 1) + (p >> 1) + (hd & 1)) & 7, cc = pc8 >> 1;
        return cc * 512 + ((((2 * tt + hh) ^ ((cf + cc) & 3)) << 2) + q) * 16 + (pc8 & 1) * 8;
    };
    auto qfix = [&](int hd, u32x4& q0, u32x4& q1) {
        const bool z0 = (hd & 1) && !hh, z1 = !(hd & 1) && hh;
        q0.x = z0 ? 0u : q0.x; q0.y = z0 ? 0u : q0.y; q1.z = z1 ? 0u : q1.z; q1.w = z1 ? 0u : q1.w;
    };
    auto ex = [&](f32x16& s) {
#pragma unroll
        for (int r = 0; r < 16; ++r) s[r] = __builtin_amdgcn_exp2f(s[r]);
    };
    auto rowmax = [&](const f32x16& s) {
        float m = fmx(s[0], s[1]);
#pragma unroll
        for (int r = 2; r < 16; r += 2) m = fmx(fmx(m, s[r]), s[r + 1]);
        const unsigned mu = __builtin_bit_cast(unsigned, m);
        const auto r = __builtin_amdgcn_permlane32_swap(mu, mu, false, false);
        const unsigned r0 = r[0], r1 = r[1];
        return fmx(__builtin_bit_cast(float, r0), __builtin_bit_cast(float, r1));
    };
    const __amdgpu_buffer_rsrc_t ros = rsrc(ws + WS_O, O_BYTES);
    auto put_o = [&](f32x16 o, float l, int rec) {
        o = o * __builtin_amdgcn_rcpf(l);
        store_dev(__builtin_bit_cast(u32x4, pack8(o, 0)), ros, lane * 16, rec);
        store_dev(__builtin_bit_cast(u32x4, pack8(o, 1)), ros, lane * 16 + 1024, rec);
    };
    auto ones = [&](int back, bool two) {
        const int bb = back + (two ? hh : 0);
        if (two || !hh) *(u32x2*)(smem + LR + w4 * 4096 + (2 - bb) * 16384 + 2048 + ((h & 1) ? 0 : 1544) + t * 16) = (u32x2){j >= bb ? 0x3F80u : 0u, 0u};
    };
    LAS char* wd = (LAS char*)(smem + LW + 7 * 512 + 8);

    #define wk ((LAS char*)(smem + LR + 32768 + ((h & 1) ? 0 : 1544)))
#define FTAG ((tag | 1) & 0xBFFFBFFFBFFFBFFFull)
    auto arrive = [&](int n) { if (lane == 0) *(volatile LAS u64*)(wk + (n + w4) * 16) = FTAG; };
    auto await4 = [&](int n) {
#pragma unroll 1
        for (int i = 0; i < SPIN_MAX * 4; ++i) if (__all(*(volatile LAS u64*)(wk + (n + p) * 16) == FTAG)) break;
        asm volatile("" ::: "memory");
    };
    bf16x8 wo[28];
    auto read_wo = [&](int base) {
        int a = base + ((w4 >> 1) ? wb1 : wb0) + hh * (20 * 128);
        PIN(a);
#pragma unroll
        for (int ks = 0; ks < 28; ++ks) {
            const int i = ks % 7, row = (ks / 7) * 112 + (i < 6 ? (i / 3) * 56 + (i % 3) * 8 : 24);
            wo[ks] = tr_frag(smem + a + row * 128, smem + a + (row + (i < 6 ? 4 : 56)) * 128);
        }
    };
    const int vwo = r3 * 896 + ((u ^ (((r3 >> 1) & 1) << 2)) << 4);
    const u32x4 rwo = srd(br ? wob : woa, OB);

    if (__builtin_expect(aux, 1)) {
        const bool x7 = ty == 7;
        i32x16 tb = *(const __attribute__((address_space(4))) i32x16*)te;
        if (ty < 7) {
            int ch = rot, sg = 0;
#pragma unroll 1
            for (int c = 1; c < 7; ++c) {
                ++ch; if (ch == 7) ch = 0;
                ++sg; if (sg == 3) sg = 0;
                const int st = lbase + RING + sg * STAGE;
#pragma unroll
                for (int i = 0; i < 4; ++i) dma16(rx, st + w4 * 4096 + i * 1024, (i & 1) ? vx1 : vx0, sx + (w4 * 4 + i) * 7168 + ch * 128);
#pragma unroll
                for (int cb = 0; cb < 2; ++cb)
#pragma unroll
                    for (int i = 0; i < 2; ++i) dma16(rw, st + 16384 + cb * 8192 + w4 * 2048 + i * 1024, vw, sw + (ch * 64 + w4 * 16 + i * 8) * 2688 + cb * 896);
                WAITV(8); BAR();
            }
#pragma unroll 1
            for (int c = 0; c < 7; ++c) {
                ++ch; if (ch == 7) ch = 0;
#pragma unroll
                for (int i = 0; i < 2; ++i) dma16(rw, lbase + LA + c * 8192 + w4 * 2048 + i * 1024, vw, sw + (ch * 64 + w4 * 16 + i * 8) * 2688 + 1792);
                if (c == 3) { WAITV(8); BAR(); }
            }
            BAR();
        }
        make_tag(); use_tab();
        asm volatile("" : "+s"(tb));
        const int jo = j * (4 * XT), sl = tb8[E_SL - 16];
        int ra0 = tb[E_RA0] + jo, ra1 = tb[E_RA1] + jo, ra2 = tb[E_RA2] + jo, rb0 = tb[E_RB0] + jo, rb1 = tb[E_RB1] + jo, rq = tb[E_RQ] + jo, lh = lbase + LR + w4 * 4096;
        asm volatile("" : "+s"(ra0), "+s"(ra1), "+s"(ra2), "+s"(rb0), "+s"(rb1), "+s"(rq), "+s"(lh));
        const int lb0 = lbase + tb[E_LB0], lb1 = lbase + tb[E_LB1];
        const int hd[2] = {tb[E_HD0], tb[E_HD1]}, cf[2] = {tb[E_CF0], tb[E_CF1]}, po[2] = {tb8[E_PO0 - 16], tb8[E_PO1 - 16]};
        const u64 mb = (u64)(unsigned)tb[E_MB] << 32, mall = mb | (unsigned)tb[E_MA];
        const char* f1 = ws + (WS_F1 + j * 256 + tb[E_PG0]) + (hh * 4096 - (r3 & 3) * 256 + (lane & 4) * tb[E_PGD] + p * 8);
        int l16 = lane * 16;
        PIN(f1); PIN(l16);
        int lz = lane; PIN(lz);
        const int g4 = lz >> 4, c16 = lz & 15;
        int fo[2], ko[2], qo[2], vo[2][2];
        f32x4 md, mo;
        const bool pt = x7 || g4 == sl;
#pragma unroll
        for (int e = 0; e < 2; ++e) fo[e] = g4 * 512 + (((c16 >> 1) ^ ((cf[e] + g4) & 3)) << 6) + (c16 & 1) * 32;
        int qs = pt ? fo[1] : OOB; PIN(qs);
        if (!local) {
#pragma unroll
            for (int e = 0; e < 2; ++e) { ko[e] = LW + 40960 - w4 * 8192 + po[e] + fo[e]; qo[e] = LW + w4 * 4096 + po[e] + fo[e]; PIN(ko[e]); PIN(qo[e]); }
#pragma unroll
            for (int r = 0; r < 4; ++r) {
                const int d = c16 - 4 * g4 - r;
                md[r] = (unsigned)d < 8u ? 0.f : -1.0e30f; mo[r] = (d < -8 && T) ? 0.f : -1.0e30f;
            }
            PIN(md); PIN(mo);
        }
        if (ty < 7) { WAITV(6); BAR(); }
        if (wave == 4 && lane < 2) *(volatile LAS u64*)(wd + lane * 16) = 0;
        asm volatile("s_waitcnt vmcnt(0) lgkmcnt(0)" ::: "memory"); BAR();
        STAMP(4);
        const u32x4 rxx = srd(ws + WS_X, X_BYTES);
        auto tell = [&](int n) { if (lane == 0) *(volatile LAS u64*)(n < 2 ? wd + n * 16 : wk + 64) = FTAG; };
        auto hear = [&](int n) {
#pragma unroll 1
            for (int i = 0; i < SPIN_MAX * 4; ++i) { if (__all(*(volatile LAS u64*)(n < 2 ? wd + n * 16 : wk + 64) == FTAG)) break; __builtin_amdgcn_s_sleep(1); }
            asm volatile("" ::: "memory");
        };
        auto fetch_b = [&](int v) {
            const int go = v * (56 * 512), lo = v * 4096;
            if (x7) { dma32_dev(rxx, lb0 + lo, l16, rb0 + go); dma32_dev(rxx, lb1 + lo, l16, rb1 + go); }
            else {
                if (w4 == 0) dma64_dev(rxx, lb1 + lo, l16, rb1 + go);
                if (sl != 8 && lane < 32) { dma16_dev(rxx, lb0 + lo, l16, rb0 + go); if (w4) dma16_dev(rxx, lb1 + lo, l16, rb1 + go); }
            }
        };
        auto fetch_a = [&](int v) {
            const int go = v * (56 * 512), lo = v * 2048;
            dma32_dev(rxx, lh + 32768 + lo, l16, ra0 + go);
            dma32_dev(rxx, lh + 16384 + lo, l16, ra1 + go);
            dma32_dev(rxx, lh + lo, l16, ra2 + go);
        };
        STAMP(5);
        unsigned left = 1;
        if (!local) { if (wave == 4) { left = __builtin_amdgcn_readfirstlane((unsigned)poll<false>(f1, mall, mb, tag)); tell(0); if (!left) tell(1); } else hear(0); }
        STAMP(2);
        u32x4 qp[2];
        asm volatile("buffer_load_dwordx4 %0, %2, %3, %4 offen sc1\n\tbuffer_load_dwordx4 %1, %2, %3, %4 offen offset:16 sc1"
                     : "=&v"(qp[0]), "=&v"(qp[1]) : "v"(qs), "s"(rxx), "s"(rq) : "memory");
        if (!local) fetch_b(0);
        if (wave == 4) { if (left) { poll<false>(f1, mall ^ mb, mall ^ mb, tag); tell(1); } } else hear(1);
        STAMP(6);
        dma32_dev(rxx, lh + 2048, l16, ra0 - 56 * 512);
        fetch_a(0);
        WAITV(8);
        PIN(qp[0]); PIN(qp[1]);
        STAMP(3);
        bf16x8 pe[2][2];
        float ls[2][2];
        if (!local) {
            f32x4 sd[2][2], so[2][2];
#pragma unroll
            for (int e = 0; e < 2; ++e)
#pragma unroll
                for (int y = 0; y < 2; ++y) {
                    const char* kb = smem + ko[e] + y * 16;
                    u32x4 qv = *(const u32x4*)(smem + qo[e] + y * 16);
                    const bool a = pt && (e || x7), z0 = (hd[e] & 1) && g4 == 0, z1 = !(hd[e] & 1) && g4 == 3;
                    qv = (u32x4){a ? qp[y].x : qv.x, a ? qp[y].y : qv.y, a ? qp[y].z : qv.z, a ? qp[y].w : qv.w};
                    qv = (u32x4){z0 ? 0u : qv.x, z0 ? 0u : qv.y, z1 ? 0u : qv.z, z1 ? 0u : qv.w};
                    sd[e][y] = MFMA16(*(const bf16x8*)kb, __builtin_bit_cast(bf16x8, qv), md);
                    so[e][y] = MFMA16(*(const bf16x8*)(kb + 8192), __builtin_bit_cast(bf16x8, qv), mo);
                }
            const bf16x8 one = __builtin_bit_cast(bf16x8, (u32x4){0x3F803F80u, 0x3F803F80u, 0x3F803F80u, 0x3F803F80u});
            float m[4];
#pragma unroll
            for (int i = 0; i < 4; ++i) {
                const f32x4 a = sd[i >> 1][i & 1], c = so[i >> 1][i & 1];
                m[i] = fmx(fmx(fmx(fmx(a[0], a[1]), a[2]), fmx(fmx(a[3], c[0]), c[1])), fmx(c[2], c[3]));
            }
#pragma unroll
            for (int i = 0; i < 4; ++i) {
                const unsigned mu = __builtin_bit_cast(unsigned, m[i]);
                const auto r = __builtin_amdgcn_permlane32_swap(mu, mu, false, false);
                const unsigned r0 = r[0], r1 = r[1];
                m[i] = fmx(__builtin_bit_cast(float, r0), __builtin_bit_cast(float, r1));
            }
#pragma unroll
            for (int i = 0; i < 4; ++i) {
                const unsigned mu = __builtin_bit_cast(unsigned, m[i]);
                const auto r = __builtin_amdgcn_permlane16_swap(mu, mu, false, false);
                const unsigned r0 = r[0], r1 = r[1];
                m[i] = fmx(__builtin_bit_cast(float, r0), __builtin_bit_cast(float, r1));
            }
            SCHED();
            f32x4 sm[4];
#pragma unroll
            for (int i = 0; i < 4; ++i) {
                const f32x4 a = sd[i >> 1][i & 1], c = so[i >> 1][i & 1];
                f32x8 x = {a[0], a[1], a[2], a[3], c[0], c[1], c[2], c[3]};
                x -= m[i];
#pragma unroll
                for (int r = 0; r < 8; ++r) x[r] = __builtin_amdgcn_exp2f(x[r]);
                pe[i >> 1][i & 1] = __builtin_convertvector(x, bf16x8);
                sm[i] = MFMA16(one, pe[i >> 1][i & 1], (f32x4){});
                SCHED();
            }
#pragma unroll
            for (int i = 0; i < 4; ++i) ls[i >> 1][i & 1] = __builtin_amdgcn_rcpf(sm[i][0]);
#pragma unroll
            for (int i = 0; i < 4; ++i) { PIN(pe[i >> 1][i & 1]); PIN(ls[i >> 1][i & 1]); }
        }
        STAMP(10);
        asm volatile("s_waitcnt vmcnt(0) lgkmcnt(0)" ::: "memory");
        STAMP(7);
        __builtin_amdgcn_s_setprio(2);
        if (wave == 4) { poll<true>(f1 + 8192, mall, mall, tag); tell(2); }
        else {
            BAR(); BAR();
            dma_rows<14>(rwo, lbase + LA + w4 * 1024, vwo, w4 * 7168 + (ty & 6) * 128);
            hear(2);
        }
        STAMP(14);
        if (!local) fetch_b(1);
        fetch_a(1);
        if (!YEARLY) dma_rows<YB0>(rwo, lbase + LA + w4 * 1024, vwo, w4 * 7168 + (ty & 6) * 128);
        if (!local) {
            PIN(lz);
#pragma unroll
            for (int e = 0; e < 2; ++e)
#pragma unroll
                for (int s = 0; s < 2; ++s) {
                    const int pc8 = (2 * (lz & 3) + s + (hd[e] & 1)) & 7, cc = pc8 >> 1;
                    vo[e][s] = LW + 40960 - w4 * 8192 + 4096 + po[e] + cc * 512 + (((lz >> 3) ^ ((cf[e] + cc) & 3)) << 6) + (q & 1) * 32 + (pc8 & 1) * 8; PIN(vo[e][s]);
                }
        }
        asm volatile("s_waitcnt vmcnt(%0)" :: "i"(YEARLY ? 0 : YB0) : "memory");
        ones(0, true); ones(2, false);
        WAITL0();
        arrive(0);
        if (!YEARLY) dma_rows<14 - YB0>(rwo, lbase + LA + (w4 + 4 * YB0) * 1024, vwo, (w4 + 4 * YB0) * 7168 + (ty & 6) * 128);
        else if (wave == 4) dma_rows<14>(rwo, lbase + LA, vwo, (ty & 6) * 128);
        STAMP(9);
        if (!local) {
            u32x4 ov[2][2];
#pragma unroll
            for (int e = 0; e < 2; ++e)
#pragma unroll
                for (int y = 0; y < 2; ++y) {
                    const char* v0 = smem + vo[e][0] + y * 16; const char* v1 = smem + vo[e][1] + y * 16;
                    const f32x4 a = MFMA16(tr_frag(v0, v0 + 8192), pe[e][y], (f32x4){}), c = MFMA16(tr_frag(v1, v1 + 8192), pe[e][y], (f32x4){});
                    f32x8 x = {a[0], a[1], a[2], a[3], c[0], c[1], c[2], c[3]};
                    x *= ls[e][y];
                    ov[e][y] = __builtin_bit_cast(u32x4, __builtin_convertvector(x, bf16x8));
                }
#pragma unroll
            for (int e = 0; e < 2; ++e)
#pragma unroll
                for (int y = 0; y < 2; ++y) store_dev(ov[e][y], ros, g4 * 512 + c16 * 16 + y * 256, ((64 + T) * 16 + hd[e]) * 2048);
        }
        STAMP(11);
        __builtin_amdgcn_s_setprio(0);
        if (!local) { arrive(8); WAITV(4); arrive(16); await4(8); } else { WAITV(0); arrive(16); }
        dma_rows<6>(rwo, lbase + LW + w4 * 1024, vwo, ty < 6 ? w4 * 7168 + (ty | 1) * 128 : OOB);
        if (!local) { WAITV(6); if (lane < 2) raise(ws + WS_F2 + (64 + T) * 128 + (lane ? hd[1] : hd[0]) * 8, tag); }
        if (ty < 6) dma_rows<8>(rwo, lbase + LW + (w4 + 24) * 1024, vwo, (w4 + 24) * 7168 + (ty | 1) * 128);
        if (b == 0) {
            poll<false>(ws + WS_F1 + w4 * 2048 + lane * 32, ~0ull, ~0ull, tag);
            arrive(12);
            if (wave == 4) { await4(12); if (lane == 0) __hip_atomic_store((gu64*)ws, tag, RLX); }
        }
        WAITV(0);
    } else {
        mask_bits();
        if (ty < 7) {
            int xo[2][4], wo_[2][2];
#pragma unroll
            for (int g = 0; g < 2; ++g) {
#pragma unroll
                for (int ks = 0; ks < 4; ++ks) { xo[g][ks] = RING + g * 65536 + (w4 * 32 + t) * 128 + (((2 * ks + hh) ^ ((t >> 1) & 7)) << 4); PIN(xo[g][ks]); }
                wo_[g][0] = RING + g * 65536 + wb0; wo_[g][1] = RING + g * 65536 + wb1; PIN(wo_[g][0]); PIN(wo_[g][1]);
            }
            int es[2], el[2][2], g0;
            unsigned pq, pk;
            char* f1;
            auto put = [&](const f32x16& a0, const f32x16& a1, int sec, unsigned pub, u32x4 (&d)[2][2]) {
#pragma unroll
                for (int e = 0; e < 2; ++e)
#pragma unroll
                    for (int s = 0; s < 2; ++s) {
                        d[e][s] = __builtin_bit_cast(u32x4, pack8(e ? a1 : a0, s));
                        if ((pub >> (2 * e + s)) & 1) store_dev(d[e][s], rxs, es[s] + e * 2048, g0 + sec * (56 * 512));
                    }
            };
            auto keep = [&](int lo, const u32x4 (&d)[2][2]) {
#pragma unroll
                for (int e = 0; e < 2; ++e)
#pragma unroll
                    for (int s = 0; s < 2; ++s) if (el[e][s] >= 0) *(u32x4*)(smem + el[e][s] + lo) = d[e][s];
            };
            f32x16 acc[4];
            bf16x8 xf[28], wf[2][4];
            const f32x16 zero = {};
            WAITV(0); BAR();
            STAMP(1); CST(20);
            auto rd = [&](int n) {
                const int sg = (n >> 2) % 3, ks = n & 3;
                const char* st = smem + (sg & 1) * STAGE;
#pragma unroll
                for (int i = 3; i >= 0; --i) {
                    const char* a = st + wo_[sg >> 1][i & 1] + 16384 + (i >> 1) * 8192 + ks * 2048;
                    wf[n & 1][i] = tr_frag(a, a + 512);
                    if (i == 3) xf[n] = *(const bf16x8*)(st + xo[sg >> 1][ks]);
                }
            };
            rd(0);
            SCHED();
#pragma unroll
            for (int n = 0; n < 28; ++n) {
                if (n == 11) {
                    make_tag(); use_tab();
#pragma unroll
            for (int s = 0; s < 2; ++s) {
                const int sw16 = ((((t >> 2) ^ (2 * s + hh)) << 2) + (t & 3)) * 16;
                es[s] = (2 * s + hh) * 512 + sw16; PIN(es[s]);
#pragma unroll
                for (int e = 0; e < 2; ++e) {
                    const int ps = tb8[E_PS - 16] + 4 * e + 2 * s + hh;
                    el[e][s] = (unsigned)ps < 8u ? ps * 512 + sw16 : -1; PIN(el[e][s]);
                }
            }
            pq = tb8[E_PQ - 16]; pk = tb8[E_PK - 16];
            g0 = (br * 64 + T) * XT + ty * 4096;
            f1 = ws + WS_F1 + (br * 16 + j) * 256 + ty * 32 + w4 * 8;
            asm volatile("" : "+s"(g0), "+s"(f1));
                    SCHED();
                }
                if ((n & 3) == 3) { WAITL0(); BAR(); SCHED(); }
                if (n < 27) rd(n + 1);
#pragma unroll
                for (int i = 3; i >= 0; --i) acc[i] = MFMA(wf[n & 1][i], xf[n], n ? acc[i] : zero);
                if (n < 27) { SG(0x8, 1); SG(0x100, 3); SG(0x8, 1); SG(0x100, 3); SG(0x8, 1); SG(0x100, 3); SG(0x8, 1); }
                SCHED();
            }
            STAMP(2);
            const float qs = 0.18898223650461361f * 1.4426950408889634f;
            {
                u32x4 dk[2][2], dq[2][2];
                put(acc[2], acc[3], 1, pk, dk);
                put(acc[0] * qs, acc[1] * qs, 0, pq, dq);
                if (br) { keep(LW + 40960 - w4 * 8192, dk); keep(LW + w4 * 4096, dq); }
            }
            STAMP(3);
            BAR();
            bf16x8 wv[8];
            u32x4 dv[2][2];
            auto rv = [&](int n) { const char* a = smem + LA + (n % 28) * 2048 + (n >= 28 ? wb1 : wb0); wv[n & 7] = tr_frag(a, a + 512); };
            auto putv = [&](int e, int s) {
                dv[e][s] = __builtin_bit_cast(u32x4, pack8(acc[2 + e], s));
                store_dev(dv[e][s], rxs, es[s] + e * 2048, ((pk >> (2 * e + s)) & 1) ? g0 + 112 * 512 : OOB);
            };
#pragma unroll
            for (int n = 0; n < 6; ++n) rv(n);
            PIN(nd); PIN(no); PIN(lm);
            SCHED();
#pragma unroll
            for (int n = 0; n < 56; ++n) {
                if (n == BV1) { BAR(); SCHED(); }
                if (n == RAISE_B || n == RAISE_A) { if ((n == RAISE_B) == (br != 0)) { WAITV(0); if (lane == 0) raise(f1, tag); STAMP(9); } SCHED(); }
                acc[2 + n / 28] = MFMA(wv[n & 7], xf[n % 28], n % 28 ? acc[2 + n / 28] : zero);
                if (n + 6 < 56) rv(n + 6);
                if (n >= 36 && n < 52) mask_reg(n - 36);
                SG(0x8, 1); SG(0x100, 2); SG(0x2, 8);
                SCHED();
                if (n == 30 || n == 32) { putv(0, (n - 30) >> 1); SCHED(); }
            }
            STAMP(4); CST(21);
            putv(1, 0); putv(1, 1);
            if (br) keep(LW + 45056 - w4 * 8192, dv);
            STAMP(5);
            WAITV(0);
            if (lane == 0) raise(f1 + 8192, tag);
        } else {
            make_tag(); use_tab();
            BAR();
            if (threadIdx.x == 0) raise(ws + WS_F1 + (br * 16 + j) * 256 + 7 * 32, tag);
#pragma unroll
            for (int r = 0; r < 16; ++r) mask_reg(r);
        }
        const char* tl = smem + LR + w4 * 4096;
        int fo[2] = {koff(c0, 0), koff(c0, 1)}, vo[2] = {voff(h, c0, 0) + 2048, voff(h, c0, 1) + 2048};
        PIN(fo[0]); PIN(fo[1]); PIN(vo[0]); PIN(vo[1]);
        STAMP(6);
        WAITL0(); BAR();
        STAMP(7);
        __builtin_amdgcn_s_setprio(1);
        u32x4 qf[2];
        qf[0] = *(const u32x4*)(tl + 2048 + fo[0]); qf[1] = *(const u32x4*)(tl + 2048 + fo[1]);
        auto kfrag = [&](int n, int s) { return *(const bf16x8*)(tl + (8 - n) * 4096 + fo[s]); };
        auto vfrag = [&](int n, int s) { const char* vb = tl + (8 - n) * 4096 + s * 256; return tr_frag(vb + vo[0], vb + vo[1]); };
        bf16x8 k0 = kfrag(0, 0), k1 = kfrag(0, 1), k2 = kfrag(8, 0), k3 = kfrag(8, 1);
        WAITL0(); BAR();
        CST(19); STAMP(10);
        qfix(h, qf[0], qf[1]);
        const bf16x8 q0 = __builtin_bit_cast(bf16x8, qf[0]), q1 = __builtin_bit_cast(bf16x8, qf[1]);
        auto qk = [&](int n, const f32x16& c) { f32x16 r = MFMA(kfrag(n, 0), q0, c); return MFMA(kfrag(n, 1), q1, r); };
        f32x16 cm, sa, sb;
        bf16x8 pp[8][2];
        cd = MFMA(k0, q0, cd); co = MFMA(k2, q0, co); cd = MFMA(k1, q1, cd); co = MFMA(k3, q1, co);
        k0 = kfrag(1, 0); k1 = kfrag(1, 1);
#pragma unroll
        for (int r = 0; r < 16; ++r) cd[r] = fmx(cd[r], co[r]);
        {
            const float m = -rowmax(cd);
#pragma unroll
            for (int r = 0; r < 16; ++r) cm[r] = m;
        }
        cd += cm;
        sa = cd;
        SCHED();
#define STG(n, cur, nxt) do { \
            if (n < 7) { nxt = MFMA(k0, q0, cm); nxt = MFMA(k1, q1, nxt); } \
            if (n < 6) { k0 = kfrag(n + 2, 0); k1 = kfrag(n + 2, 1); } \
            ex(cur); pp[n][0] = pack8(cur, 0); pp[n][1] = pack8(cur, 1); \
            if (n < 7) SG(0x8, 1); SG(0x400, 4); if (n < 7) SG(0x8, 1); if (n < 6) SG(0x100, 2); SG(0x400, 4); SG(0x2, 2); SG(0x400, 4); SG(0x2, 2); SG(0x400, 4); SG(0x2, 4); SCHED(); } while (0)
        STG(0, sa, sb); STG(1, sb, sa); STG(2, sa, sb); STG(3, sb, sa); STG(4, sa, sb); STG(5, sb, sa); STG(6, sa, sb); STG(7, sb, sa);
        bf16x8 pz[2];
#pragma unroll
        for (int s = 0; s < 2; ++s) {
            const u32x4 e = __builtin_bit_cast(u32x4, pp[0][s]);
            pp[0][s] = __builtin_bit_cast(bf16x8, e & pm[s]); pz[s] = __builtin_bit_cast(bf16x8, e & ~pm[s]); PIN(pz[s]);
        }
#pragma unroll
        for (int n = 0; n < 8; ++n) { PIN(pp[n][0]); PIN(pp[n][1]); }
        char* f2 = ws + WS_F2 + T * 128 + h * 8;
        int orec = (T * 16 + h) * 2048;
        const char* va = tl + vo[0]; const char* vb = tl + vo[1];
        asm volatile("" : "+s"(f2), "+s"(orec));
        STAMP(8);
        await4(0);
        STAMP(14);
        f32x16 o = {};
        auto pv = [&](int n, bf16x8 p0, bf16x8 p1) { o = MFMA(vfrag(n, 0), p0, o); o = MFMA(vfrag(n, 1), p1, o); };
        {
            bf16x8 vf[18];
            auto rdv = [&](int i) { const int off = (8 - (i >> 1)) * 4096 + (i & 1) * 256; vf[i] = tr_frag(va + off, vb + off); };
#pragma unroll
            for (int i = 0; i < 5; ++i) { rdv(i); SCHED(); }
#pragma unroll
            for (int i = 0; i < 18; ++i) {
                o = MFMA(vf[i], i < 16 ? pp[i >> 1][i & 1] : pz[i & 1], o);
                if (i + 5 < 18) rdv(i + 5);
                SCHED();
            }
        }
        auto publish = [&]() {
            const float lf = o[12];
            const unsigned lu = __builtin_bit_cast(unsigned, lf);
            const unsigned lx = __builtin_amdgcn_permlane32_swap(lu, lu, false, false)[1];
            put_o(o, __builtin_bit_cast(float, lx), orec);
        };
        publish();
        STAMP(11); CST(22);
        float big = fmx(__builtin_fabsf(o[0]), __builtin_fabsf(o[1]));
#pragma unroll
        for (int r = 2; r < 16; r += 2) big = fmx(fmx(big, __builtin_fabsf(o[r])), __builtin_fabsf(o[r + 1]));
        if (__builtin_expect(!__all(big < 1.0e36f), 0)) {
            const int nmid = T >= 7 ? 7 : T;
            float m = 0.f;
#pragma unroll 1
            for (int i = 1; i <= nmid; ++i) m = fmx(m, rowmax(qk(i, cm)));
            cd -= m; cm -= m;
            ex(cd);
            o = (f32x16){};
            const u32x4 e0 = __builtin_bit_cast(u32x4, pack8(cd, 0)), e1 = __builtin_bit_cast(u32x4, pack8(cd, 1));
            pv(0, __builtin_bit_cast(bf16x8, e0 & pm[0]), __builtin_bit_cast(bf16x8, e1 & pm[1]));
            pv(8, __builtin_bit_cast(bf16x8, e0 & ~pm[0]), __builtin_bit_cast(bf16x8, e1 & ~pm[1]));
#pragma unroll 1
            for (int i = 1; i <= nmid; ++i) {
                f32x16 s = qk(i, cm);
                ex(s); pv(i, pack8(s, 0), pack8(s, 1));
            }
            WAITV(0);
            publish();
        }
        WAITV(0);
        if (lane == 0) raise(f2, tag);
        __builtin_amdgcn_s_setprio(0);
        STAMP(12);
        await4(16);
        read_wo(LA);
    }
    STAMP(13);
    const int To = br * 64 + 4 * j + 2 * (ty & 1);
    const int tr = br ? 2 * (t & 15) + (t >> 4) : t;
    const int tb = LR + 32768 + ((wave >> 2) * 2 + (w4 & 1)) * 4096;
    int l16 = lane * 16, oa = (w4 & 1) * LR + hh * 2048 + t * 16, tw = tb + tr * 128 + (((4 * (w4 >> 1) + hh) ^ (tr & 7)) << 4), tg = tb + (w4 >> 1) * 2048 + r3 * 128 + ((u ^ r3) << 4);
    int ov = r3 * 1792 + u * 16;
    int oo = ((4 * j + 2 * (ty & 1) + (w4 & 1)) * 32 + (w4 >> 1) * 16) * 1792 + br * 896 + ((ty & 6) + (wave >> 2)) * 128;
    const int os = (To * 16 + w4) * 2048, ol = lbase + w4 * 2048;
    asm volatile("" : "+v"(l16), "+v"(oa), "+v"(tw), "+v"(tg), "+v"(ov)); asm volatile("" : "+s"(oo));
    const __amdgpu_buffer_rsrc_t rout = rsrc(out, 2048 * 1792);
    const u32x4 roo = srd(ws + WS_O, O_BYTES);
    const bool idle = aux && ty >= 6;
    if (wave == 0) poll<false>(ws + WS_F2 + To * 128 + lane * 8, 0xFFFFFFFFull, 0xFFFFFFFFull, tag);
    WAITL0(); BAR();
    STAMP(15);
    if (idle) { STAMP_FLUSH(); return; }
    auto geto = [&](int qd) { dma32_dev(roo, ol + qd * 8192, l16, os + qd * 8192); dma32_dev(roo, ol + qd * 8192 + LR, l16, os + qd * 8192 + 32768); };
    f32x16 acc = {};
    {
        bf16x8 of[2][7];
        auto rdo = [&](int qd) {
            const char* a = smem + oa + qd * 8192;
#pragma unroll
            for (int i = 0; i < 6; ++i) of[qd & 1][i] = *(const bf16x8*)(a + (i / 3) * 4096 + (i % 3) * 512);
            const u32x2 lo = *(const u32x2*)(a + 1536), hi = *(const u32x2*)(a + 4096 + 1536);
            of[qd & 1][6] = __builtin_bit_cast(bf16x8, (u32x4){lo.x, lo.y, hi.x, hi.y});
        };
        auto mmo = [&](int qd, int i0, int i1) {
#pragma unroll
            for (int i = i0; i < i1; ++i) acc = MFMA(wo[qd * 7 + i], of[qd & 1][i], acc);
            SCHED();
        };
        if (aux) read_wo(LW); else { geto(0); geto(1); geto(2); WAITV(8); }
        BAR(); rdo(0); SCHED();
        if (!aux) geto(3);
        mmo(0, 0, 3);
        WAITV(8);
        BAR(); rdo(1); SCHED();
        mmo(0, 3, 7); mmo(1, 0, 3);
        WAITV(4);
        BAR(); rdo(2); SCHED();
        mmo(1, 3, 7); mmo(2, 0, 3);
        WAITV(0);
        BAR(); rdo(3); SCHED();
        mmo(2, 3, 7); mmo(3, 0, 7);
    }
    STAMP(16);
    {
#pragma unroll
        for (int s = 0; s < 2; ++s) *(bf16x8*)(smem + (tw ^ (s << 5))) = pack8(acc, s);
        WAITL0(); BAR();
        const u32x4 v0 = *(const u32x4*)(smem + tg), v1 = *(const u32x4*)(smem + tg + 1024);
        SCHED();
        store_dev(v0, rout, ov, oo); store_dev(v1, rout, ov, oo + 8 * 1792);
    }
    STAMP(17);
    STAMP_FLUSH();
}

extern "C" __device__ __attribute__((noinline, used)) void fused_end() {}

extern "C" void kernel_launch(void* const* d_in, const int* in_sizes, int n_in, void* d_out, int out_size, void* d_ws, size_t ws_size, hipStream_t stream) {
    fused<<<256, 512, 0, stream>>>((const char*)d_in[0], (const char*)d_in[1], (const char*)d_in[2], (const char*)d_in[3], (const char*)d_in[4], (const char*)d_in[5], (char*)d_out, (char*)d_ws);
}
```

```cpp
#include <hip/hip_runtime.h>
#include <hip/hip_bf16.h>

typedef __bf16 bf16x8 __attribute__((ext_vector_type(8)));
typedef short s16x4 __attribute__((ext_vector_type(4)));
typedef float f32x16 __attribute__((ext_vector_type(16)));
typedef float f32x8 __attribute__((ext_vector_type(8)));
typedef float f32x4 __attribute__((ext_vector_type(4)));
typedef unsigned u32x4 __attribute__((ext_vector_type(4)));
typedef unsigned u32x2 __attribute__((ext_vector_type(2)));
typedef unsigned long long u64;
typedef __attribute__((address_space(1))) u64 gu64;
#define LAS __attribute__((address_space(3)))
#define DI __device__ __forceinline__
#define MFMA(a, b, c) __builtin_amdgcn_mfma_f32_32x32x16_bf16((a), (b), (c), 0, 0, 0)
#define MFMA16(a, b, c) __builtin_amdgcn_mfma_f32_16x16x32_bf16((a), (b), (c), 0, 0, 0)
#define RLX __ATOMIC_RELAXED, __HIP_MEMORY_SCOPE_AGENT

constexpr int XB = 2048 * 896, WB = 448 * 2688, OB = 448 * 896;
constexpr int STAGE = 32768, RING = 65536, LA = 0, LW = 57344, LR = 114688, LX = 106496, LDS_BYTES = 163840;
constexpr int WS_F1 = 4096, WS_F2 = 65536, WS_X = 1 << 20, XT = 168 * 512, X_BYTES = 2 * 64 * XT, WS_O = 16 << 20, O_BYTES = 2 * 64 * 16 * 2048;
constexpr int OOB = 0x40000000;
#define ROT 2
#define PF0 2
#define RAISE_A 27
#define RAISE_B 4
#define BV1 10
#define KH_DELAY 8
#define OPRE 10
#define OROT 0
#define XB0 5
#define YB0 3
#define YEARLY 1
#define HSL 0
#define F64 1
#define PPV 0
#define TCH 0
constexpr int SPIN_MAX = 1 << 18;

enum { E_RA0, E_RA1, E_RA2, E_RB0, E_RB1, E_RQ, E_LB0, E_LB1, E_MA, E_MB, E_PG0, E_PGD, E_HD0, E_HD1, E_CF0, E_CF1, E_PO0, E_PO1, E_SL, E_H, E_C0, E_PS, E_PQ, E_PK };
struct Tab {
    int e[192][32];
    constexpr Tab() : e() {
        for (int i = 0; i < 192; ++i) {
            const int w4 = i & 3, ty = (i >> 2) & 7, br = (i >> 5) & 1, jc = i >> 6;
            const bool x7 = ty == 7, local = br == 0 && !x7;
            const int ha = (0x4ECA7530u >> (4 * ty)) & 15, hb = (0xBFD98621u >> (4 * ty)) & 15;
            const int ca = (7 * ha) >> 1, cb = (7 * hb) >> 1, cs = ca < cb ? ca : cb;
            const int h = br ? hb : ha, c0 = br ? cb : ca;
            const int nb = x7 ? 15 : (0xF63F30Fu >> (4 * ty)) & 15;
            const int sl = (ty == 2 || ty == 5) ? 3 : 0;
            const int two = (x7 || (br && nb != 15)) ? 0xFF : 0x0F;
            const int rt = (64 + w4) * XT;
            int* v = e[i];
            v[E_HD0] = x7 ? h : ha; v[E_HD1] = x7 ? h : hb; v[E_CF0] = x7 ? c0 : ca; v[E_CF1] = x7 ? c0 : cb;
            v[E_PO0] = x7 ? 0 : (ca - cs) * 512; v[E_PO1] = x7 ? 0 : (cb - cs) * 512;
            v[E_RA0] = w4 * XT + (56 + c0) * 512; v[E_RA1] = jc >= 1 ? v[E_RA0] - 4 * XT : OOB; v[E_RA2] = jc >= 2 ? v[E_RA0] - 8 * XT : OOB;
            v[E_RB0] = rt + (56 + (x7 ? c0 : cb + sl)) * 512;
            v[E_RB1] = (jc == 0 && w4 == 0) ? OOB : ((!x7 && w4 == 0) ? rt + (56 + cs) * 512 : v[E_RB0]) - XT;
            v[E_RQ] = local ? OOB : rt + v[E_CF1] * 512;
            v[E_LB0] = LW + 40960 - w4 * 8192 + (x7 ? 0 : v[E_PO1] + sl * 512); v[E_LB1] = (!x7 && w4 == 0) ? LX : v[E_LB0] + 8192;
            v[E_MA] = two | (jc >= 1 ? two << 8 : 0) | (jc >= 2 ? two << 16 : 0);
            v[E_MB] = local ? 0 : (x7 ? 0xFF : two & 0xF0) | (jc ? (two & 0x88) << 8 : 0);
            v[E_PG0] = (x7 ? 1 + 3 * br : ty) * 32; v[E_PGD] = (x7 ? 1 : (nb == 15 ? 0 : nb - ty)) * 8;
            v[E_SL] = nb == 15 ? 8 : sl;
            v[E_H] = h; v[E_C0] = c0; v[E_PS] = 8 * ty - cs;
            v[E_PQ] = br ? (0x1189188u >> (4 * ty)) & 15 : 15; v[E_PK] = w4 != 3 ? v[E_PQ] : 15;
        }
    }
};
__device__ const Tab g_tab = Tab();

#define STAMP(i)
#define CST(i)
#define STAMP_DECL
#define STAMP_FLUSH()

DI u32x4 srd(const void* p, unsigned bytes) { const u64 a = (u64)p; return (u32x4){(unsigned)a, (unsigned)(a >> 32) & 0xffffu, bytes, 0x00020000u}; }
DI __amdgpu_buffer_rsrc_t rsrc(const void* p, int bytes) { return __builtin_amdgcn_make_buffer_rsrc((void*)p, 0, bytes, 0x00020000); }
DI void dma16(u32x4 r, int lds, int voff, int soff) {
    asm volatile("s_mov_b32 m0, %0\n\ts_nop 0\n\tbuffer_load_dwordx4 %1, %2, %3 offen lds" :: "s"(lds), "v"(voff), "s"(r), "s"(soff) : "memory");
}
DI void dma16_dev(u32x4 r, int lds, int voff, int soff) {
    asm volatile("s_mov_b32 m0, %0\n\ts_nop 0\n\tbuffer_load_dwordx4 %1, %2, %3 offen sc1 lds" :: "s"(lds), "v"(voff), "s"(r), "s"(soff) : "memory");
}
DI void dma64_dev(u32x4 r, int lds, int voff, int soff) {
    asm volatile("s_mov_b32 m0, %0\n\ts_nop 0\n\tbuffer_load_dwordx4 %1, %2, %3 offen sc1 lds\n\tbuffer_load_dwordx4 %1, %2, %3 offen offset:1024 sc1 lds\n\t"
                 "buffer_load_dwordx4 %1, %2, %3 offen offset:2048 sc1 lds\n\tbuffer_load_dwordx4 %1, %2, %3 offen offset:3072 sc1 lds" :: "s"(lds), "v"(voff), "s"(r), "s"(soff) : "memory");
}
DI void dma32_dev(u32x4 r, int lds, int voff, int soff) {
    asm volatile("s_mov_b32 m0, %0\n\ts_nop 0\n\tbuffer_load_dwordx4 %1, %2, %3 offen sc1 lds\n\tbuffer_load_dwordx4 %1, %2, %3 offen offset:1024 sc1 lds"
                 :: "s"(lds), "v"(voff), "s"(r), "s"(soff) : "memory");
}
DI void store_dev(u32x4 v, __amdgpu_buffer_rsrc_t r, int voff, int soff) {
    __builtin_amdgcn_raw_buffer_store_b128(v, r, voff, soff, 16);
    asm volatile("s_nop 1" :: "v"(v) : "memory");
}
template <int N>
DI void dma_rows(u32x4 r, int lds, int voff, int soff) {
    asm volatile("s_mov_b32 m0, %0\n\ts_nop 0\n\tbuffer_load_dwordx4 %2, %3, %1 offen lds\n.rept %4\n\ts_add_u32 m0, m0, 0x1000\n\ts_add_u32 %1, %1, 0x7000\n\tbuffer_load_dwordx4 %2, %3, %1 offen lds\n.endr"
                 : "+s"(lds), "+s"(soff) : "v"(voff), "s"(r), "i"(N - 1) : "scc", "memory");
}
DI bf16x8 tr_frag(const char* a0, const char* a1) {
    s16x4 lo = __builtin_amdgcn_ds_read_tr16_b64_v4i16((LAS s16x4*)a0);
    s16x4 hi = __builtin_amdgcn_ds_read_tr16_b64_v4i16((LAS s16x4*)a1);
    return __builtin_bit_cast(bf16x8, __builtin_shufflevector(lo, hi, 0, 1, 2, 3, 4, 5, 6, 7));
}
DI bf16x8 pack8(const f32x16& v, int s) {
    f32x8 t = {v[8 * s], v[8 * s + 1], v[8 * s + 2], v[8 * s + 3], v[8 * s + 4], v[8 * s + 5], v[8 * s + 6], v[8 * s + 7]};
    return __builtin_convertvector(t, bf16x8);
}
DI float xhalf(float v) {
    const unsigned u = __builtin_bit_cast(unsigned, v);
    const auto r = __builtin_amdgcn_permlane32_swap(u, u, false, false);
    return __builtin_bit_cast(float, (threadIdx.x & 32) ? r[0] : r[1]);
}
#define WAITV(n) asm volatile("s_waitcnt vmcnt(" #n ")" ::: "memory")
#define WAITL0() asm volatile("s_waitcnt lgkmcnt(0)" ::: "memory")
#define BAR() do { asm volatile("" ::: "memory"); __builtin_amdgcn_s_barrier(); asm volatile("" ::: "memory"); } while (0)
#define SCHED() __builtin_amdgcn_sched_barrier(0)

#define SG(m, n) __builtin_amdgcn_sched_group_barrier(m, n, 0)
DI float fmx(float a, float b) { return __builtin_elementwise_maximum(a, b); }
typedef int i32x4 __attribute__((ext_vector_type(4)));
#define PIN(x) asm volatile("" : "+v"(x))
DI void raise(char* p, u64 tag) { (void)__hip_atomic_exchange((gu64*)p, tag, RLX); }
template <bool TWO_BARRIERS>
DI u64 poll(const char* p, u64 all, u64 need, u64 tag) {
    u64 v, zero, sv, left; unsigned cnt = SPIN_MAX;
#define POLL_LOOP ".Lpw%=:\n\ts_waitcnt vmcnt(0)\n\tv_cmp_ne_u64 %4, %6, %0\n\ts_and_b64 vcc, %4, %8\n\ts_cbranch_scc0 .Lpd%=\n\ts_sub_u32 %3, %3, 1\n\ts_cbranch_scc1 .Lpd%=\n\t" \
                  "global_atomic_add_x2 %0, %5, %1, off sc0\n\ts_branch .Lpw%=\n.Lpd%=:\n\ts_mov_b64 exec, %2"
#define POLL_OPS : "=&v"(v), "=&v"(zero), "=&s"(sv), "+s"(cnt), "=&s"(left) : "v"(p), "s"(tag), "s"(all), "s"(need) : "vcc", "scc", "memory"
    if (TWO_BARRIERS)
        asm volatile("s_mov_b64 %2, exec\n\ts_mov_b64 exec, %7\n\tv_mov_b64 %1, 0\n\tglobal_atomic_add_x2 %0, %5, %1, off sc0\n\ts_barrier\n\ts_barrier\n" POLL_LOOP POLL_OPS);
    else
        asm volatile("s_mov_b64 %2, exec\n\ts_mov_b64 exec, %7\n\tv_mov_b64 %1, 0\n\tglobal_atomic_add_x2 %0, %5, %1, off sc0\n" POLL_LOOP POLL_OPS);
    return left;
}

extern "C" __global__ void __launch_bounds__(512, 2)
fused(const char* xa, const char* wa, const char* woa, const char* xb, const char* wb, const char* wob, char* out, char* ws) {
    __shared__ __attribute__((aligned(16))) char smem[LDS_BYTES];
    STAMP_DECL;
    STAMP(0);
    const int b = blockIdx.x, xcd = b & 7, idx = b >> 3;
    const int br = xcd & 1, jj = idx >> 3, ty = idx & 7, j = (xcd >> 1) * 4 + jj;
    unsigned d0 = threadIdx.x * 128;
    {
        u64 pc;
        asm volatile("s_cmp_lg_u32 %2, 7\n\ts_cbranch_scc1 .Lskip%=\n\tv_cmp_gt_u32 vcc, fused_end-.Lhere%=, %1\n\ts_cbranch_vccz .Lskip%=\n\ts_mov_b64 exec, vcc\n\t"
                     "s_getpc_b64 %0\n.Lhere%=:\n\tglobal_load_dword %1, %1, %0\n\ts_mov_b64 exec, -1\n.Lskip%=:"
                     : "=&s"(pc), "+v"(d0) : "s"(ty) : "vcc", "scc", "memory");
    }
    const int wave = __builtin_amdgcn_readfirstlane(threadIdx.x >> 6), lane = threadIdx.x & 63;
    const int r3 = lane >> 3, u = lane & 7;
    const int lbase = (int)(unsigned)(size_t)(LAS char*)smem;
    int vx0 = r3 * 896 + ((u ^ (r3 >> 1)) << 4), vx1 = vx0 ^ 64;
    int vw = r3 * 2688 + ((u ^ (((r3 >> 1) & 1) << 2)) << 4);
    const int rot = ROT == 2 ? ((ty + j) & 1) * 4 : 0;
    const int sx = j * (128 * 896), sw = ty * 128;
    int e0 = sx + wave * 14336 + rot * 128, e1 = sw + (rot * 64 + wave * 8) * 2688, e2 = lbase + RING + wave * 2048, e3 = lbase + RING + 16384 + wave * 1024;
    asm volatile("" : "+s"(e0), "+s"(e1), "+s"(e2), "+s"(e3)); asm volatile("" : "+v"(vx0), "+v"(vx1), "+v"(vw));
    SCHED();
    const u32x4 rx = srd(br ? xb : xa, XB), rw = srd(br ? wb : wa, WB);
    if (ty < 7) {
        dma16(rx, e2, vx0, e0);
        dma16(rw, e3, vw, e1);
        dma16(rx, e2 + 1024, vx1, e0 + 7168);
        dma16(rw, e3 + 8192, vw, e1 + 896);
    } else {
        {
            const int ln = jj * 392 + (threadIdx.x < 392 ? threadIdx.x : 0), row = (ln * 9363) >> 16;
            const int vl = row * 2688 + 1792 + (ln - row * 7) * 128;
            asm volatile("global_load_dword %0, %1, %2" : "+v"(d0) : "v"(vl), "s"(br ? wb : wa) : "memory");
            asm volatile("global_load_dword %0, %1, %2" : "+v"(d0) : "v"(vl + 224 * 2688), "s"(br ? wb : wa) : "memory");
        }
        const int wl = (jj * 784 + (threadIdx.x < 392 ? threadIdx.x : 0)) * 128;
        asm volatile("global_load_dword %0, %1, %2" : "+v"(d0) : "v"(wl), "s"(br ? wob : woa) : "memory");
        asm volatile("global_load_dword %0, %1, %2" : "+v"(d0) : "v"(wl + 392 * 128), "s"(br ? wob : woa) : "memory");
        asm volatile("s_waitcnt vmcnt(0)" : "+v"(d0) :: "memory");
    }
    u64 tag = *(const __attribute__((address_space(4))) u64*)ws;
    auto make_tag = [&]() { asm volatile("" : "+s"(tag)); tag = tag * 0x9E3779B97F4A7C15ull + 0x7F4A7C159E3779B9ull; asm volatile("" : "+s"(tag)); };
    const int w4 = wave & 3;
    const bool aux = wave >= 4;
    const int hh = lane >> 5, t = lane & 31, blk = (lane >> 4) & 1, q = (lane >> 2) & 3, p = lane & 3;
    const int T = 4 * j + w4;
    const bool local = br == 0 && ty < 7;
    typedef int i32x8 __attribute__((ext_vector_type(8)));
    typedef int i32x16 __attribute__((ext_vector_type(16)));
    const __attribute__((address_space(4))) char* te = (const __attribute__((address_space(4))) char*)g_tab.e[(min(j, 2) * 2 + br) * 32 + ty * 4 + w4];
    i32x8 tb8 = *(const __attribute__((address_space(4))) i32x8*)(te + 64);
    int h, c0;
    auto use_tab = [&]() { asm volatile("" : "+s"(tb8)); h = tb8[E_H - 16]; c0 = tb8[E_C0 - 16]; };
    const __amdgpu_buffer_rsrc_t rxs = rsrc(ws + WS_X, X_BYTES);
    const int wb0 = (8 * hh + q) * 128 + ((blk * 2 + (p & 1)) << 4) + (p >> 1) * 8 + ((q >> 1) << 6), wb1 = wb0 ^ 64;

    const unsigned NEGB = 0xF149F2CAu;
    f32x16 cd, co;
    u32x4 pm[2];
    int nd, no, lm;
    auto mask_bits = [&]() {
        const unsigned lo = (2u << t) - 1u;
        nd = (int)(~lo >> (4 * hh)); lm = (int)(lo >> (4 * hh)); no = T < 8 ? -1 : lm;
    };
    auto mask_reg = [&](int r) {
        const int bit = (r & 3) + 8 * (r >> 2);
        float a = __builtin_bit_cast(float, __builtin_amdgcn_sbfe(nd, bit, 1) & NEGB), c = __builtin_bit_cast(float, __builtin_amdgcn_sbfe(no, bit, 1) & NEGB);
        PIN(a); PIN(c); cd[r] = a; co[r] = c;
        if (r & 1) { unsigned m = __builtin_amdgcn_perm(__builtin_amdgcn_sbfe(lm, bit, 1), __builtin_amdgcn_sbfe(lm, bit - 1, 1), 0x05040100u); PIN(m); pm[r >> 3][(r >> 1) & 3] = m; }
    };
    auto koff = [&](int cf, int s) { return (2 * s + hh) * 512 + ((((t >> 2) ^ ((cf + 2 * s + hh) & 3)) << 2) + (t & 3)) * 16; };
    auto voff = [&](int hd, int cf, int tt) {
        const int pc8 = (4 * blk + 2 * (p & 1) + (p >> 1) + (hd & 1)) & 7, cc = pc8 >> 1;
        return cc * 512 + ((((2 * tt + hh) ^ ((cf + cc) & 3)) << 2) + q) * 16 + (pc8 & 1) * 8;
    };
    auto qfix = [&](int hd, u32x4& q0, u32x4& q1) {
        const bool z0 = (hd & 1) && !hh, z1 = !(hd & 1) && hh;
        q0.x = z0 ? 0u : q0.x; q0.y = z0 ? 0u : q0.y; q1.z = z1 ? 0u : q1.z; q1.w = z1 ? 0u : q1.w;
    };
    auto ex = [&](f32x16& s) {
#pragma unroll
        for (int r = 0; r < 16; ++r) s[r] = __builtin_amdgcn_exp2f(s[r]);
    };
    auto rowmax = [&](const f32x16& s) {
        float m = fmx(s[0], s[1]);
#pragma unroll
        for (int r = 2; r < 16; r += 2) m = fmx(fmx(m, s[r]), s[r + 1]);
        const unsigned mu = __builtin_bit_cast(unsigned, m);
        const auto r = __builtin_amdgcn_permlane32_swap(mu, mu, false, false);
        const unsigned r0 = r[0], r1 = r[1];
        return fmx(__builtin_bit_cast(float, r0), __builtin_bit_cast(float, r1));
    };
    const __amdgpu_buffer_rsrc_t ros = rsrc(ws + WS_O, O_BYTES);
    auto put_o = [&](f32x16 o, float l, int rec) {
        o = o * __builtin_amdgcn_rcpf(l);
        store_dev(__builtin_bit_cast(u32x4, pack8(o, 0)), ros, lane * 16, rec);
        store_dev(__builtin_bit_cast(u32x4, pack8(o, 1)), ros, lane * 16 + 1024, rec);
    };
    auto ones = [&](int back, bool two) {
        const int bb = back + (two ? hh : 0);
        if (two || !hh) *(u32x2*)(smem + LR + w4 * 4096 + (2 - bb) * 16384 + 2048 + ((h & 1) ? 0 : 1544) + t * 16) = (u32x2){j >= bb ? 0x3F80u : 0u, 0u};
    };
    LAS char* wd = (LAS char*)(smem + LW + 7 * 512 + 8);

    #define wk ((LAS char*)(smem + LR + 32768 + ((h & 1) ? 0 : 1544)))
#define FTAG ((tag | 1) & 0xBFFFBFFFBFFFBFFFull)
    auto arrive = [&](int n) { if (lane == 0) *(volatile LAS u64*)(wk + (n + w4) * 16) = FTAG; };
    auto await4 = [&](int n) {
#pragma unroll 1
        for (int i = 0; i < SPIN_MAX * 4; ++i) if (__all(*(volatile LAS u64*)(wk + (n + p) * 16) == FTAG)) break;
        asm volatile("" ::: "memory");
    };
    bf16x8 wo[28];
    auto read_wo = [&](int base) {
        int a = base + ((w4 >> 1) ? wb1 : wb0) + hh * (20 * 128);
        PIN(a);
#pragma unroll
        for (int ks = 0; ks < 28; ++ks) {
            const int i = ks % 7, row = (ks / 7) * 112 + (i < 6 ? (i / 3) * 56 + (i % 3) * 8 : 24);
            wo[ks] = tr_frag(smem + a + row * 128, smem + a + (row + (i < 6 ? 4 : 56)) * 128);
        }
    };
    const int vwo = r3 * 896 + ((u ^ (((r3 >> 1) & 1) << 2)) << 4);
    const u32x4 rwo = srd(br ? wob : woa, OB);

    if (__builtin_expect(aux, 1)) {
        const bool x7 = ty == 7;
        i32x16 tb = *(const __attribute__((address_space(4))) i32x16*)te;
        if (ty < 7) {
            int ch = rot, sg = 0;
#pragma unroll 1
            for (int c = 1; c < 7; ++c) {
                ++ch; if (ch == 7) ch = 0;
                ++sg; if (sg == 3) sg = 0;
                const int st = lbase + RING + sg * STAGE;
#pragma unroll
                for (int i = 0; i < 4; ++i) dma16(rx, st + w4 * 4096 + i * 1024, (i & 1) ? vx1 : vx0, sx + (w4 * 4 + i) * 7168 + ch * 128);
#pragma unroll
                for (int cb = 0; cb < 2; ++cb)
#pragma unroll
                    for (int i = 0; i < 2; ++i) dma16(rw, st + 16384 + cb * 8192 + w4 * 2048 + i * 1024, vw, sw + (ch * 64 + w4 * 16 + i * 8) * 2688 + cb * 896);
                WAITV(8); BAR();
            }
#pragma unroll 1
            for (int c = 0; c < 7; ++c) {
                ++ch; if (ch == 7) ch = 0;
#pragma unroll
                for (int i = 0; i < 2; ++i) dma16(rw, lbase + LA + c * 8192 + w4 * 2048 + i * 1024, vw, sw + (ch * 64 + w4 * 16 + i * 8) * 2688 + 1792);
                if (c == 3) { WAITV(8); BAR(); }
            }
            BAR();
        }
        make_tag(); use_tab();
        asm volatile("" : "+s"(tb));
        const int jo = j * (4 * XT), sl = tb8[E_SL - 16];
        int ra0 = tb[E_RA0] + jo, ra1 = tb[E_RA1] + jo, ra2 = tb[E_RA2] + jo, rb0 = tb[E_RB0] + jo, rb1 = tb[E_RB1] + jo, rq = tb[E_RQ] + jo, lh = lbase + LR + w4 * 4096;
        asm volatile("" : "+s"(ra0), "+s"(ra1), "+s"(ra2), "+s"(rb0), "+s"(rb1), "+s"(rq), "+s"(lh));
        const int lb0 = lbase + tb[E_LB0], lb1 = lbase + tb[E_LB1];
        const int hd[2] = {tb[E_HD0], tb[E_HD1]}, cf[2] = {tb[E_CF0], tb[E_CF1]}, po[2] = {tb8[E_PO0 - 16], tb8[E_PO1 - 16]};
        const u64 mb = (u64)(unsigned)tb[E_MB] << 32, mall = mb | (unsigned)tb[E_MA];
        const char* f1 = ws + (WS_F1 + j * 256 + tb[E_PG0]) + (hh * 4096 - (r3 & 3) * 256 + (lane & 4) * tb[E_PGD] + p * 8);
        int l16 = lane * 16;
        PIN(f1); PIN(l16);
        int lz = lane; PIN(lz);
        const int g4 = lz >> 4, c16 = lz & 15;
        int fo[2], ko[2], qo[2], vo[2][2];
        f32x4 md, mo;
        const bool pt = x7 || g4 == sl;
#pragma unroll
        for (int e = 0; e < 2; ++e) fo[e] = g4 * 512 + (((c16 >> 1) ^ ((cf[e] + g4) & 3)) << 6) + (c16 & 1) * 32;
        int qs = pt ? fo[1] : OOB; PIN(qs);
        if (!local) {
#pragma unroll
            for (int e = 0; e < 2; ++e) { ko[e] = LW + 40960 - w4 * 8192 + po[e] + fo[e]; qo[e] = LW + w4 * 4096 + po[e] + fo[e]; PIN(ko[e]); PIN(qo[e]); }
#pragma unroll
            for (int r = 0; r < 4; ++r) {
                const int d = c16 - 4 * g4 - r;
                md[r] = (unsigned)d < 8u ? 0.f : -1.0e30f; mo[r] = (d < -8 && T) ? 0.f : -1.0e30f;
            }
            PIN(md); PIN(mo);
        }
        if (ty < 7) { WAITV(6); BAR(); }
        if (wave == 4 && lane < 2) *(volatile LAS u64*)(wd + lane * 16) = 0;
        asm volatile("s_waitcnt vmcnt(0) lgkmcnt(0)" ::: "memory"); BAR();
        STAMP(4);
        const u32x4 rxx = srd(ws + WS_X, X_BYTES);
        if (TCH && wave != 4) {
            const int k = lane & 7, wv_ = (lane >> 3) & 1;
            int a = (k & 3) == 0 ? (k ? rb1 : ra0 - 56 * 512) : ((k & 3) == 1 ? ra0 : ((k & 3) == 2 ? ra1 : ra2)) + (k >> 2) * (56 * 512);
            a = lane < (wave == 5 ? 16 : 8) ? a - wv_ * XT : OOB;
            asm volatile("buffer_atomic_add %0, %1, %2, 0 offen" :: "v"(0), "v"(a), "s"(rxx) : "memory");
        }
        auto tell = [&](int n) { if (lane == 0) *(volatile LAS u64*)(n < 2 ? wd + n * 16 : wk + 64) = FTAG; };
        auto hear = [&](int n) {
#pragma unroll 1
            for (int i = 0; i < SPIN_MAX * 4; ++i) { if (__all(*(volatile LAS u64*)(n < 2 ? wd + n * 16 : wk + 64) == FTAG)) break; if (HSL) __builtin_amdgcn_s_sleep(HSL); }
            asm volatile("" ::: "memory");
        };
        auto fetch_b = [&](int v) {
            const int go = v * (56 * 512), lo = v * 4096;
            if (x7) { dma32_dev(rxx, lb0 + lo, l16, rb0 + go); dma32_dev(rxx, lb1 + lo, l16, rb1 + go); }
            else {
                if (w4 == 0) dma64_dev(rxx, lb1 + lo, l16, rb1 + go);
                if (sl != 8 && lane < 32) { dma16_dev(rxx, lb0 + lo, l16, rb0 + go); if (w4) dma16_dev(rxx, lb1 + lo, l16, rb1 + go); }
            }
        };
        auto fetch_a = [&](int v) {
            const int go = v * (56 * 512), lo = v * 2048;
            dma32_dev(rxx, lh + 32768 + lo, l16, ra0 + go);
            dma32_dev(rxx, lh + 16384 + lo, l16, ra1 + go);
            dma32_dev(rxx, lh + lo, l16, ra2 + go);
        };
        STAMP(5);
        unsigned left = 1;
        if (!local) { if (wave == 4) { left = __builtin_amdgcn_readfirstlane((unsigned)poll<false>(f1, mall, mb, tag)); tell(0); if (!left) tell(1); } else hear(0); }
        STAMP(2);
        u32x4 qp[2];
        asm volatile("buffer_load_dwordx4 %0, %2, %3, %4 offen sc1\n\tbuffer_load_dwordx4 %1, %2, %3, %4 offen offset:16 sc1"
                     : "=&v"(qp[0]), "=&v"(qp[1]) : "v"(qs), "s"(rxx), "s"(rq) : "memory");
        if (!local) fetch_b(0);
        if (wave == 4) { if (left) { poll<false>(f1, mall ^ mb, mall ^ mb, tag); tell(1); } } else hear(1);
        STAMP(6);
        dma32_dev(rxx, lh + 2048, l16, ra0 - 56 * 512);
        fetch_a(0);
        WAITV(8);
        PIN(qp[0]); PIN(qp[1]);
        STAMP(3);
        bf16x8 pe[2][2];
        float ls[2][2];
        if (!local) {
            f32x4 sd[2][2], so[2][2];
#pragma unroll
            for (int e = 0; e < 2; ++e)
#pragma unroll
                for (int y = 0; y < 2; ++y) {
                    const char* kb = smem + ko[e] + y * 16;
                    u32x4 qv = *(const u32x4*)(smem + qo[e] + y * 16);
                    const bool a = pt && (e || x7), z0 = (hd[e] & 1) && g4 == 0, z1 = !(hd[e] & 1) && g4 == 3;
                    qv = (u32x4){a ? qp[y].x : qv.x, a ? qp[y].y : qv.y, a ? qp[y].z : qv.z, a ? qp[y].w : qv.w};
                    qv = (u32x4){z0 ? 0u : qv.x, z0 ? 0u : qv.y, z1 ? 0u : qv.z, z1 ? 0u : qv.w};
                    sd[e][y] = MFMA16(*(const bf16x8*)kb, __builtin_bit_cast(bf16x8, qv), md);
                    so[e][y] = MFMA16(*(const bf16x8*)(kb + 8192), __builtin_bit_cast(bf16x8, qv), mo);
                }
            const bf16x8 one = __builtin_bit_cast(bf16x8, (u32x4){0x3F803F80u, 0x3F803F80u, 0x3F803F80u, 0x3F803F80u});
            float m[4];
#pragma unroll
            for (int i = 0; i < 4; ++i) {
                const f32x4 a = sd[i >> 1][i & 1], c = so[i >> 1][i & 1];
                m[i] = fmx(fmx(fmx(fmx(a[0], a[1]), a[2]), fmx(fmx(a[3], c[0]), c[1])), fmx(c[2], c[3]));
            }
#pragma unroll
            for (int i = 0; i < 4; ++i) {
                const unsigned mu = __builtin_bit_cast(unsigned, m[i]);
                const auto r = __builtin_amdgcn_permlane32_swap(mu, mu, false, false);
                const unsigned r0 = r[0], r1 = r[1];
                m[i] = fmx(__builtin_bit_cast(float, r0), __builtin_bit_cast(float, r1));
            }
#pragma unroll
            for (int i = 0; i < 4; ++i) {
                const unsigned mu = __builtin_bit_cast(unsigned, m[i]);
                const auto r = __builtin_amdgcn_permlane16_swap(mu, mu, false, false);
                const unsigned r0 = r[0], r1 = r[1];
                m[i] = fmx(__builtin_bit_cast(float, r0), __builtin_bit_cast(float, r1));
            }
            SCHED();
            f32x4 sm[4];
#pragma unroll
            for (int i = 0; i < 4; ++i) {
                const f32x4 a = sd[i >> 1][i & 1], c = so[i >> 1][i & 1];
                f32x8 x = {a[0], a[1], a[2], a[3], c[0], c[1], c[2], c[3]};
                x -= m[i];
#pragma unroll
                for (int r = 0; r < 8; ++r) x[r] = __builtin_amdgcn_exp2f(x[r]);
                pe[i >> 1][i & 1] = __builtin_convertvector(x, bf16x8);
                sm[i] = MFMA16(one, pe[i >> 1][i & 1], (f32x4){});
                SCHED();
            }
#pragma unroll
            for (int i = 0; i < 4; ++i) ls[i >> 1][i & 1] = __builtin_amdgcn_rcpf(sm[i][0]);
#pragma unroll
            for (int i = 0; i < 4; ++i) { PIN(pe[i >> 1][i & 1]); PIN(ls[i >> 1][i & 1]); }
        }
        STAMP(10);
        asm volatile("s_waitcnt vmcnt(0) lgkmcnt(0)" ::: "memory");
        STAMP(7);
        __builtin_amdgcn_s_setprio(2);
        if (wave == 4) { poll<true>(f1 + 8192, mall, mall, tag); tell(2); }
        else {
            BAR(); BAR();
            dma_rows<14>(rwo, lbase + LA + w4 * 1024, vwo, w4 * 7168 + (ty & 6) * 128);
            hear(2);
        }
        STAMP(14);
        if (!local) fetch_b(1);
        fetch_a(1);
        if (!YEARLY) dma_rows<YB0>(rwo, lbase + LA + w4 * 1024, vwo, w4 * 7168 + (ty & 6) * 128);
        if (!local) {
            PIN(lz);
#pragma unroll
            for (int e = 0; e < 2; ++e)
#pragma unroll
                for (int s = 0; s < 2; ++s) {
                    const int pc8 = (2 * (lz & 3) + s + (hd[e] & 1)) & 7, cc = pc8 >> 1;
                    vo[e][s] = LW + 40960 - w4 * 8192 + 4096 + po[e] + cc * 512 + (((lz >> 3) ^ ((cf[e] + cc) & 3)) << 6) + (q & 1) * 32 + (pc8 & 1) * 8; PIN(vo[e][s]);
                }
        }
        if (PPV) { WAITV(2); ones(0, true); WAITL0(); arrive(0); WAITV(0); ones(2, false); WAITL0(); arrive(20); }
        else { WAITV(0); ones(0, true); ones(2, false); WAITL0(); arrive(0); }
        if (!YEARLY) dma_rows<14 - YB0>(rwo, lbase + LA + (w4 + 4 * YB0) * 1024, vwo, (w4 + 4 * YB0) * 7168 + (ty & 6) * 128);
        else if (wave == 4) dma_rows<14>(rwo, lbase + LA, vwo, (ty & 6) * 128);
        STAMP(9);
        if (!local) {
            u32x4 ov[2][2];
#pragma unroll
            for (int e = 0; e < 2; ++e)
#pragma unroll
                for (int y = 0; y < 2; ++y) {
                    const char* v0 = smem + vo[e][0] + y * 16; const char* v1 = smem + vo[e][1] + y * 16;
                    const f32x4 a = MFMA16(tr_frag(v0, v0 + 8192), pe[e][y], (f32x4){}), c = MFMA16(tr_frag(v1, v1 + 8192), pe[e][y], (f32x4){});
                    f32x8 x = {a[0], a[1], a[2], a[3], c[0], c[1], c[2], c[3]};
                    x *= ls[e][y];
                    ov[e][y] = __builtin_bit_cast(u32x4, __builtin_convertvector(x, bf16x8));
                }
#pragma unroll
            for (int e = 0; e < 2; ++e)
#pragma unroll
                for (int y = 0; y < 2; ++y) store_dev(ov[e][y], ros, g4 * 512 + c16 * 16 + y * 256, ((64 + T) * 16 + hd[e]) * 2048);
        }
        STAMP(11);
        __builtin_amdgcn_s_setprio(0);
        if (!local) { arrive(8); WAITV(4); arrive(16); await4(8); } else { WAITV(0); arrive(16); }
        dma_rows<6>(rwo, lbase + LW + w4 * 1024, vwo, ty < 6 ? w4 * 7168 + (ty | 1) * 128 : OOB);
        if (!local) { WAITV(6); if (lane < 2) raise(ws + WS_F2 + (64 + T) * 128 + (lane ? hd[1] : hd[0]) * 8, tag); }
        if (ty < 6) dma_rows<8>(rwo, lbase + LW + (w4 + 24) * 1024, vwo, (w4 + 24) * 7168 + (ty | 1) * 128);
        if (b == 0) {
            poll<false>(ws + WS_F1 + w4 * 2048 + lane * 32, ~0ull, ~0ull, tag);
            arrive(12);
            if (wave == 4) { await4(12); if (lane == 0) __hip_atomic_store((gu64*)ws, tag, RLX); }
        }
        WAITV(0);
    } else {
        mask_bits();
        if (ty < 7) {
            int xo[2][4], wo_[2][2];
#pragma unroll
            for (int g = 0; g < 2; ++g) {
#pragma unroll
                for (int ks = 0; ks < 4; ++ks) { xo[g][ks] = RING + g * 65536 + (w4 * 32 + t) * 128 + (((2 * ks + hh) ^ ((t >> 1) & 7)) << 4); PIN(xo[g][ks]); }
                wo_[g][0] = RING + g * 65536 + wb0; wo_[g][1] = RING + g * 65536 + wb1; PIN(wo_[g][0]); PIN(wo_[g][1]);
            }
            int es[2], el[2][2], g0;
            unsigned pq, pk;
            char* f1;
            auto put = [&](const f32x16& a0, const f32x16& a1, int sec, unsigned pub, u32x4 (&d)[2][2]) {
#pragma unroll
                for (int e = 0; e < 2; ++e)
#pragma unroll
                    for (int s = 0; s < 2; ++s) {
                        d[e][s] = __builtin_bit_cast(u32x4, pack8(e ? a1 : a0, s));
                        if ((pub >> (2 * e + s)) & 1) store_dev(d[e][s], rxs, es[s] + e * 2048, g0 + sec * (56 * 512));
                    }
            };
            auto keep = [&](int lo, const u32x4 (&d)[2][2]) {
#pragma unroll
                for (int e = 0; e < 2; ++e)
#pragma unroll
                    for (int s = 0; s < 2; ++s) if (el[e][s] >= 0) *(u32x4*)(smem + el[e][s] + lo) = d[e][s];
            };
            f32x16 acc[4];
            bf16x8 xf[28], wf[2][4];
            const f32x16 zero = {};
            WAITV(0); BAR();
            STAMP(1); CST(20);
            auto rd = [&](int n) {
                const int sg = (n >> 2) % 3, ks = n & 3;
                const char* st = smem + (sg & 1) * STAGE;
#pragma unroll
                for (int i = 3; i >= 0; --i) {
                    const char* a = st + wo_[sg >> 1][i & 1] + 16384 + (i >> 1) * 8192 + ks * 2048;
                    wf[n & 1][i] = tr_frag(a, a + 512);
                    if (i == 3) xf[n] = *(const bf16x8*)(st + xo[sg >> 1][ks]);
                }
            };
            rd(0);
            SCHED();
#pragma unroll
            for (int n = 0; n < 28; ++n) {
                if (n == 11) {
                    make_tag(); use_tab();
#pragma unroll
            for (int s = 0; s < 2; ++s) {
                const int sw16 = ((((t >> 2) ^ (2 * s + hh)) << 2) + (t & 3)) * 16;
                es[s] = (2 * s + hh) * 512 + sw16; PIN(es[s]);
#pragma unroll
                for (int e = 0; e < 2; ++e) {
                    const int ps = tb8[E_PS - 16] + 4 * e + 2 * s + hh;
                    el[e][s] = (unsigned)ps < 8u ? ps * 512 + sw16 : -1; PIN(el[e][s]);
                }
            }
            pq = tb8[E_PQ - 16]; pk = tb8[E_PK - 16];
            g0 = (br * 64 + T) * XT + ty * 4096;
            f1 = ws + WS_F1 + (br * 16 + j) * 256 + ty * 32 + w4 * 8;
            asm volatile("" : "+s"(g0), "+s"(f1));
                    SCHED();
                }
                if ((n & 3) == 3) { WAITL0(); BAR(); SCHED(); }
                if (n < 27) rd(n + 1);
#pragma unroll
                for (int i = 3; i >= 0; --i) acc[i] = MFMA(wf[n & 1][i], xf[n], n ? acc[i] : zero);
                if (n < 27) { SG(0x8, 1); SG(0x100, 3); SG(0x8, 1); SG(0x100, 3); SG(0x8, 1); SG(0x100, 3); SG(0x8, 1); }
                SCHED();
            }
            STAMP(2);
            const float qs = 0.18898223650461361f * 1.4426950408889634f;
            {
                u32x4 dk[2][2], dq[2][2];
                put(acc[2], acc[3], 1, pk, dk);
                put(acc[0] * qs, acc[1] * qs, 0, pq, dq);
                if (br) { keep(LW + 40960 - w4 * 8192, dk); keep(LW + w4 * 4096, dq); }
            }
            STAMP(3);
            BAR();
            bf16x8 wv[8];
            u32x4 dv[2][2];
            auto rv = [&](int n) { const char* a = smem + LA + (n % 28) * 2048 + (n >= 28 ? wb1 : wb0); wv[n & 7] = tr_frag(a, a + 512); };
            auto putv = [&](int e, int s) {
                dv[e][s] = __builtin_bit_cast(u32x4, pack8(acc[2 + e], s));
                store_dev(dv[e][s], rxs, es[s] + e * 2048, ((pk >> (2 * e + s)) & 1) ? g0 + 112 * 512 : OOB);
            };
#pragma unroll
            for (int n = 0; n < 6; ++n) rv(n);
            PIN(nd); PIN(no); PIN(lm);
            SCHED();
#pragma unroll
            for (int n = 0; n < 56; ++n) {
                if (n == BV1) { BAR(); SCHED(); }
                if (n == RAISE_B || n == RAISE_A) { if ((n == RAISE_B) == (br != 0)) { WAITV(0); if (lane == 0) raise(f1, tag); STAMP(9); } SCHED(); }
                acc[2 + n / 28] = MFMA(wv[n & 7], xf[n % 28], n % 28 ? acc[2 + n / 28] : zero);
                if (n + 6 < 56) rv(n + 6);
                if (n >= 36 && n < 52) mask_reg(n - 36);
                SG(0x8, 1); SG(0x100, 2); SG(0x2, 8);
                SCHED();
                if (n == 30 || n == 32) { putv(0, (n - 30) >> 1); SCHED(); }
            }
            STAMP(4); CST(21);
            putv(1, 0); putv(1, 1);
            if (br) keep(LW + 45056 - w4 * 8192, dv);
            STAMP(5);
            WAITV(0);
            if (lane == 0) raise(f1 + 8192, tag);
        } else {
            make_tag(); use_tab();
            BAR();
            if (threadIdx.x == 0) raise(ws + WS_F1 + (br * 16 + j) * 256 + 7 * 32, tag);
#pragma unroll
            for (int r = 0; r < 16; ++r) mask_reg(r);
        }
        const char* tl = smem + LR + w4 * 4096;
        int fo[2] = {koff(c0, 0), koff(c0, 1)}, vo[2] = {voff(h, c0, 0) + 2048, voff(h, c0, 1) + 2048};
        PIN(fo[0]); PIN(fo[1]); PIN(vo[0]); PIN(vo[1]);
        STAMP(6);
        WAITL0(); BAR();
        STAMP(7);
        __builtin_amdgcn_s_setprio(1);
        u32x4 qf[2];
        qf[0] = *(const u32x4*)(tl + 2048 + fo[0]); qf[1] = *(const u32x4*)(tl + 2048 + fo[1]);
        auto kfrag = [&](int n, int s) { return *(const bf16x8*)(tl + (8 - n) * 4096 + fo[s]); };
        auto vfrag = [&](int n, int s) { const char* vb = tl + (8 - n) * 4096 + s * 256; return tr_frag(vb + vo[0], vb + vo[1]); };
        bf16x8 k0 = kfrag(0, 0), k1 = kfrag(0, 1), k2 = kfrag(8, 0), k3 = kfrag(8, 1);
        WAITL0(); BAR();
        CST(19); STAMP(10);
        qfix(h, qf[0], qf[1]);
        const bf16x8 q0 = __builtin_bit_cast(bf16x8, qf[0]), q1 = __builtin_bit_cast(bf16x8, qf[1]);
        auto qk = [&](int n, const f32x16& c) { f32x16 r = MFMA(kfrag(n, 0), q0, c); return MFMA(kfrag(n, 1), q1, r); };
        f32x16 cm, sa, sb;
        bf16x8 pp[8][2];
        cd = MFMA(k0, q0, cd); co = MFMA(k2, q0, co); cd = MFMA(k1, q1, cd); co = MFMA(k3, q1, co);
        k0 = kfrag(1, 0); k1 = kfrag(1, 1);
#pragma unroll
        for (int r = 0; r < 16; ++r) cd[r] = fmx(cd[r], co[r]);
        {
            const float m = -rowmax(cd);
#pragma unroll
            for (int r = 0; r < 16; ++r) cm[r] = m;
        }
        cd += cm;
        sa = cd;
        SCHED();
#define STG(n, cur, nxt) do { \
            if (n < 7) { nxt = MFMA(k0, q0, cm); nxt = MFMA(k1, q1, nxt); } \
            if (n < 6) { k0 = kfrag(n + 2, 0); k1 = kfrag(n + 2, 1); } \
            ex(cur); pp[n][0] = pack8(cur, 0); pp[n][1] = pack8(cur, 1); \
            if (n < 7) SG(0x8, 1); SG(0x400, 4); if (n < 7) SG(0x8, 1); if (n < 6) SG(0x100, 2); SG(0x400, 4); SG(0x2, 2); SG(0x400, 4); SG(0x2, 2); SG(0x400, 4); SG(0x2, 4); SCHED(); } while (0)
        STG(0, sa, sb); STG(1, sb, sa); STG(2, sa, sb); STG(3, sb, sa); STG(4, sa, sb); STG(5, sb, sa); STG(6, sa, sb); STG(7, sb, sa);
        bf16x8 pz[2];
#pragma unroll
        for (int s = 0; s < 2; ++s) {
            const u32x4 e = __builtin_bit_cast(u32x4, pp[0][s]);
            pp[0][s] = __builtin_bit_cast(bf16x8, e & pm[s]); pz[s] = __builtin_bit_cast(bf16x8, e & ~pm[s]); PIN(pz[s]);
        }
#pragma unroll
        for (int n = 0; n < 8; ++n) { PIN(pp[n][0]); PIN(pp[n][1]); }
        char* f2 = ws + WS_F2 + T * 128 + h * 8;
        int orec = (T * 16 + h) * 2048;
        const char* va = tl + vo[0]; const char* vb = tl + vo[1];
        asm volatile("" : "+s"(f2), "+s"(orec));
        STAMP(8);
        await4(0);
        STAMP(14);
        f32x16 o = {};
        auto pv = [&](int n, bf16x8 p0, bf16x8 p1) { o = MFMA(vfrag(n, 0), p0, o); o = MFMA(vfrag(n, 1), p1, o); };
        {
            bf16x8 vf[18];
            auto rdv = [&](int i) { const int off = (8 - (i >> 1)) * 4096 + (i & 1) * 256; vf[i] = tr_frag(va + off, vb + off); };
#pragma unroll
            for (int i = 0; i < 5; ++i) { rdv(i); SCHED(); }
#pragma unroll
            for (int i = 0; i < 18; ++i) {
                if (PPV && i == 5) { await4(20); SCHED(); }
                o = MFMA(vf[i], i < 16 ? pp[i >> 1][i & 1] : pz[i & 1], o);
                if (i + 5 < 18) rdv(i + 5);
                SCHED();
            }
        }
        auto publish = [&]() {
            const float lf = o[12];
            const unsigned lu = __builtin_bit_cast(unsigned, lf);
            const unsigned lx = __builtin_amdgcn_permlane32_swap(lu, lu, false, false)[1];
            put_o(o, __builtin_bit_cast(float, lx), orec);
        };
        publish();
        STAMP(11); CST(22);
        float big = fmx(__builtin_fabsf(o[0]), __builtin_fabsf(o[1]));
#pragma unroll
        for (int r = 2; r < 16; r += 2) big = fmx(fmx(big, __builtin_fabsf(o[r])), __builtin_fabsf(o[r + 1]));
        if (__builtin_expect(!__all(big < 1.0e36f), 0)) {
            const int nmid = T >= 7 ? 7 : T;
            float m = 0.f;
#pragma unroll 1
            for (int i = 1; i <= nmid; ++i) m = fmx(m, rowmax(qk(i, cm)));
            cd -= m; cm -= m;
            ex(cd);
            o = (f32x16){};
            const u32x4 e0 = __builtin_bit_cast(u32x4, pack8(cd, 0)), e1 = __builtin_bit_cast(u32x4, pack8(cd, 1));
            pv(0, __builtin_bit_cast(bf16x8, e0 & pm[0]), __builtin_bit_cast(bf16x8, e1 & pm[1]));
            pv(8, __builtin_bit_cast(bf16x8, e0 & ~pm[0]), __builtin_bit_cast(bf16x8, e1 & ~pm[1]));
#pragma unroll 1
            for (int i = 1; i <= nmid; ++i) {
                f32x16 s = qk(i, cm);
                ex(s); pv(i, pack8(s, 0), pack8(s, 1));
            }
            WAITV(0);
            publish();
        }
        WAITV(0);
        if (lane == 0) raise(f2, tag);
        __builtin_amdgcn_s_setprio(0);
        STAMP(12);
        await4(16);
        read_wo(LA);
    }
    STAMP(13);
    const int To = br * 64 + 4 * j + 2 * (ty & 1);
    const int tr = br ? 2 * (t & 15) + (t >> 4) : t;
    const int tb = LR + 32768 + ((wave >> 2) * 2 + (w4 & 1)) * 4096;
    int l16 = lane * 16, oa = (w4 & 1) * LR + hh * 2048 + t * 16, tw = tb + tr * 128 + (((4 * (w4 >> 1) + hh) ^ (tr & 7)) << 4), tg = tb + (w4 >> 1) * 2048 + r3 * 128 + ((u ^ r3) << 4);
    int ov = r3 * 1792 + u * 16;
    int oo = ((4 * j + 2 * (ty & 1) + (w4 & 1)) * 32 + (w4 >> 1) * 16) * 1792 + br * 896 + ((ty & 6) + (wave >> 2)) * 128;
    const int os = (To * 16 + w4) * 2048, ol = lbase + w4 * 2048;
    if (F64) { tg = tb + (lane >> 2) * 128 + (((4 * (w4 >> 1) + p) ^ ((lane >> 2) & 7)) << 4); ov = (lane >> 2) * 1792 + p * 16; oo += (w4 >> 1) * (64 - 16 * 1792); }
    asm volatile("" : "+v"(l16), "+v"(oa), "+v"(tw), "+v"(tg), "+v"(ov)); asm volatile("" : "+s"(oo));
    const __amdgpu_buffer_rsrc_t rout = rsrc(out, 2048 * 1792);
    const u32x4 roo = srd(ws + WS_O, O_BYTES);
    const bool idle = aux && ty >= 6;
    if (wave == 0) poll<false>(ws + WS_F2 + To * 128 + lane * 8, 0xFFFFFFFFull, 0xFFFFFFFFull, tag);
    WAITL0(); BAR();
    STAMP(15);
    if (idle) { STAMP_FLUSH(); return; }
    auto geto = [&](int qd) { dma32_dev(roo, ol + qd * 8192, l16, os + qd * 8192); dma32_dev(roo, ol + qd * 8192 + LR, l16, os + qd * 8192 + 32768); };
    f32x16 acc = {};
    {
        bf16x8 of[2][7];
        auto rdo = [&](int qd) {
            const char* a = smem + oa + qd * 8192;
#pragma unroll
            for (int i = 0; i < 6; ++i) of[qd & 1][i] = *(const bf16x8*)(a + (i / 3) * 4096 + (i % 3) * 512);
            const u32x2 lo = *(const u32x2*)(a + 1536), hi = *(const u32x2*)(a + 4096 + 1536);
            of[qd & 1][6] = __builtin_bit_cast(bf16x8, (u32x4){lo.x, lo.y, hi.x, hi.y});
        };
        auto mmo = [&](int qd, int i0, int i1) {
#pragma unroll
            for (int i = i0; i < i1; ++i) acc = MFMA(wo[qd * 7 + i], of[qd & 1][i], acc);
            SCHED();
        };
        if (aux) read_wo(LW); else { geto(0); geto(1); geto(2); WAITV(8); }
        BAR(); rdo(0); SCHED();
        if (!aux) geto(3);
        mmo(0, 0, 3);
        WAITV(8);
        BAR(); rdo(1); SCHED();
        mmo(0, 3, 7); mmo(1, 0, 3);
        WAITV(4);
        BAR(); rdo(2); SCHED();
        mmo(1, 3, 7); mmo(2, 0, 3);
        WAITV(0);
        BAR(); rdo(3); SCHED();
        mmo(2, 3, 7); mmo(3, 0, 7);
    }
    STAMP(16);
    {
#pragma unroll
        for (int s = 0; s < 2; ++s) *(bf16x8*)(smem + (tw ^ (s << 5))) = pack8(acc, s);
        WAITL0(); if (!F64) BAR();
        const u32x4 v0 = *(const u32x4*)(smem + tg), v1 = *(const u32x4*)(smem + tg + (F64 ? 2048 : 1024));
        SCHED();
        store_dev(v0, rout, ov, oo); store_dev(v1, rout, ov, oo + (F64 ? 16 : 8) * 1792);
    }
    STAMP(17);
    STAMP_FLUSH();
}

extern "C" __device__ __attribute__((noinline, used)) void fused_end() {}

extern "C" void kernel_launch(void* const* d_in, const int* in_sizes, int n_in, void* d_out, int out_size, void* d_ws, size_t ws_size, hipStream_t stream) {
    fused<<<256, 512, 0, stream>>>((const char*)d_in[0], (const char*)d_in[1], (const char*)d_in[2], (const char*)d_in[3], (const char*)d_in[4], (const char*)d_in[5], (char*)d_out, (char*)d_ws);
}
```

```cpp
#define SMASK 2u
#define DIAG 1
#include <hip/hip_runtime.h>
#include <hip/hip_bf16.h>

typedef __bf16 bf16x8 __attribute__((ext_vector_type(8)));
typedef short s16x4 __attribute__((ext_vector_type(4)));
typedef float f32x16 __attribute__((ext_vector_type(16)));
typedef float f32x8 __attribute__((ext_vector_type(8)));
typedef float f32x4 __attribute__((ext_vector_type(4)));
typedef unsigned u32x4 __attribute__((ext_vector_type(4)));
typedef unsigned u32x2 __attribute__((ext_vector_type(2)));
typedef unsigned long long u64;
typedef __attribute__((address_space(1))) u64 gu64;
#define LAS __attribute__((address_space(3)))
#define DI __device__ __forceinline__
#define MFMA(a, b, c) __builtin_amdgcn_mfma_f32_32x32x16_bf16((a), (b), (c), 0, 0, 0)
#define MFMA16(a, b, c) __builtin_amdgcn_mfma_f32_16x16x32_bf16((a), (b), (c), 0, 0, 0)
#define RLX __ATOMIC_RELAXED, __HIP_MEMORY_SCOPE_AGENT

constexpr int XB = 2048 * 896, WB = 448 * 2688, OB = 448 * 896;
constexpr int STAGE = 32768, RING = 65536, LA = 0, LW = 57344, LR = 114688, LX = 106496, LDS_BYTES = 163840;
constexpr int WS_F1 = 4096, WS_F2 = 65536, WS_X = 1 << 20, XT = 168 * 512, X_BYTES = 2 * 64 * XT, WS_O = 16 << 20, O_BYTES = 2 * 64 * 16 * 2048;
constexpr int OOB = 0x40000000;
#define ROT 2
#define PF0 2
#define RAISE_A 27
#define RAISE_B 4
#define BV1 10
#define KH_DELAY 8
#define OPRE 10
#define OROT 0
#define XB0 5
#define YB0 3
#define YEARLY 1
constexpr int SPIN_MAX = 1 << 18;

enum { E_RA0, E_RA1, E_RA2, E_RB0, E_RB1, E_RQ, E_LB0, E_LB1, E_MA, E_MB, E_PG0, E_PGD, E_HD0, E_HD1, E_CF0, E_CF1, E_PO0, E_PO1, E_SL, E_H, E_C0, E_PS, E_PQ, E_PK };
struct Tab {
    int e[192][32];
    constexpr Tab() : e() {
        for (int i = 0; i < 192; ++i) {
            const int w4 = i & 3, ty = (i >> 2) & 7, br = (i >> 5) & 1, jc = i >> 6;
            const bool x7 = ty == 7, local = br == 0 && !x7;
            const int ha = (0x4ECA7530u >> (4 * ty)) & 15, hb = (0xBFD98621u >> (4 * ty)) & 15;
            const int ca = (7 * ha) >> 1, cb = (7 * hb) >> 1, cs = ca < cb ? ca : cb;
            const int h = br ? hb : ha, c0 = br ? cb : ca;
            const int nb = x7 ? 15 : (0xF63F30Fu >> (4 * ty)) & 15;
            const int sl = (ty == 2 || ty == 5) ? 3 : 0;
            const int two = (x7 || (br && nb != 15)) ? 0xFF : 0x0F;
            const int rt = (64 + w4) * XT;
            int* v = e[i];
            v[E_HD0] = x7 ? h : ha; v[E_HD1] = x7 ? h : hb; v[E_CF0] = x7 ? c0 : ca; v[E_CF1] = x7 ? c0 : cb;
            v[E_PO0] = x7 ? 0 : (ca - cs) * 512; v[E_PO1] = x7 ? 0 : (cb - cs) * 512;
            v[E_RA0] = w4 * XT + (56 + c0) * 512; v[E_RA1] = jc >= 1 ? v[E_RA0] - 4 * XT : OOB; v[E_RA2] = jc >= 2 ? v[E_RA0] - 8 * XT : OOB;
            v[E_RB0] = rt + (56 + (x7 ? c0 : cb + sl)) * 512;
            v[E_RB1] = (jc == 0 && w4 == 0) ? OOB : ((!x7 && w4 == 0) ? rt + (56 + cs) * 512 : v[E_RB0]) - XT;
            v[E_RQ] = local ? OOB : rt + v[E_CF1] * 512;
            v[E_LB0] = LW + 40960 - w4 * 8192 + (x7 ? 0 : v[E_PO1] + sl * 512); v[E_LB1] = (!x7 && w4 == 0) ? LX : v[E_LB0] + 8192;
            v[E_MA] = two | (jc >= 1 ? two << 8 : 0) | (jc >= 2 ? two << 16 : 0);
            v[E_MB] = local ? 0 : (x7 ? 0xFF : two & 0xF0) | (jc ? (two & 0x88) << 8 : 0);
            v[E_PG0] = (x7 ? 1 + 3 * br : ty) * 32; v[E_PGD] = (x7 ? 1 : (nb == 15 ? 0 : nb - ty)) * 8;
            v[E_SL] = nb == 15 ? 8 : sl;
            v[E_H] = h; v[E_C0] = c0; v[E_PS] = 8 * ty - cs;
            v[E_PQ] = br ? (0x1189188u >> (4 * ty)) & 15 : 15; v[E_PK] = w4 != 3 ? v[E_PQ] : 15;
        }
    }
};
__device__ const Tab g_tab = Tab();

__device__ u64 g_diag[256 * 8 * 32];
#define STAMP(i) do { if (!((SMASK >> (i)) & 1)) break; u64 t_; asm volatile("s_memrealtime %0\n\ts_waitcnt lgkmcnt(0)" : "=s"(t_) :: "memory"); stamps[i] = t_; } while (0)
#define CST(i) do { if (!((SMASK >> (i)) & 1)) break; u64 t_; asm volatile("s_memtime %0\n\ts_waitcnt lgkmcnt(0)" : "=s"(t_) :: "memory"); stamps[i] = t_; } while (0)
#define STAMP_DECL u64 stamps[24]; _Pragma("unroll") for (int i_ = 0; i_ < 24; ++i_) stamps[i_] = 0
#define STAMP_FLUSH() do { if ((threadIdx.x & 63) == 0) { _Pragma("unroll") for (int i_ = 0; i_ < 24; ++i_) g_diag[(blockIdx.x * 8 + (threadIdx.x >> 6)) * 32 + i_] = stamps[i_]; } } while (0)

DI u32x4 srd(const void* p, unsigned bytes) { const u64 a = (u64)p; return (u32x4){(unsigned)a, (unsigned)(a >> 32) & 0xffffu, bytes, 0x00020000u}; }
DI __amdgpu_buffer_rsrc_t rsrc(const void* p, int bytes) { return __builtin_amdgcn_make_buffer_rsrc((void*)p, 0, bytes, 0x00020000); }
DI void dma16(u32x4 r, int lds, int voff, int soff) {
    asm volatile("s_mov_b32 m0, %0\n\ts_nop 0\n\tbuffer_load_dwordx4 %1, %2, %3 offen lds" :: "s"(lds), "v"(voff), "s"(r), "s"(soff) : "memory");
}
DI void dma16_dev(u32x4 r, int lds, int voff, int soff) {
    asm volatile("s_mov_b32 m0, %0\n\ts_nop 0\n\tbuffer_load_dwordx4 %1, %2, %3 offen sc1 lds" :: "s"(lds), "v"(voff), "s"(r), "s"(soff) : "memory");
}
DI void dma64_dev(u32x4 r, int lds, int voff, int soff) {
    asm volatile("s_mov_b32 m0, %0\n\ts_nop 0\n\tbuffer_load_dwordx4 %1, %2, %3 offen sc1 lds\n\tbuffer_load_dwordx4 %1, %2, %3 offen offset:1024 sc1 lds\n\t"
                 "buffer_load_dwordx4 %1, %2, %3 offen offset:2048 sc1 lds\n\tbuffer_load_dwordx4 %1, %2, %3 offen offset:3072 sc1 lds" :: "s"(lds), "v"(voff), "s"(r), "s"(soff) : "memory");
}
DI void dma32_dev(u32x4 r, int lds, int voff, int soff) {
    asm volatile("s_mov_b32 m0, %0\n\ts_nop 0\n\tbuffer_load_dwordx4 %1, %2, %3 offen sc1 lds\n\tbuffer_load_dwordx4 %1, %2, %3 offen offset:1024 sc1 lds"
                 :: "s"(lds), "v"(voff), "s"(r), "s"(soff) : "memory");
}
DI void store_dev(u32x4 v, __amdgpu_buffer_rsrc_t r, int voff, int soff) {
    __builtin_amdgcn_raw_buffer_store_b128(v, r, voff, soff, 16);
    asm volatile("s_nop 1" :: "v"(v) : "memory");
}
template <int N>
DI void dma_rows(u32x4 r, int lds, int voff, int soff) {
    asm volatile("s_mov_b32 m0, %0\n\ts_nop 0\n\tbuffer_load_dwordx4 %2, %3, %1 offen lds\n.rept %4\n\ts_add_u32 m0, m0, 0x1000\n\ts_add_u32 %1, %1, 0x7000\n\tbuffer_load_dwordx4 %2, %3, %1 offen lds\n.endr"
                 : "+s"(lds), "+s"(soff) : "v"(voff), "s"(r), "i"(N - 1) : "scc", "memory");
}
DI bf16x8 tr_frag(const char* a0, const char* a1) {
    s16x4 lo = __builtin_amdgcn_ds_read_tr16_b64_v4i16((LAS s16x4*)a0);
    s16x4 hi = __builtin_amdgcn_ds_read_tr16_b64_v4i16((LAS s16x4*)a1);
    return __builtin_bit_cast(bf16x8, __builtin_shufflevector(lo, hi, 0, 1, 2, 3, 4, 5, 6, 7));
}
DI bf16x8 pack8(const f32x16& v, int s) {
    f32x8 t = {v[8 * s], v[8 * s + 1], v[8 * s + 2], v[8 * s + 3], v[8 * s + 4], v[8 * s + 5], v[8 * s + 6], v[8 * s + 7]};
    return __builtin_convertvector(t, bf16x8);
}
DI float xhalf(float v) {
    const unsigned u = __builtin_bit_cast(unsigned, v);
    const auto r = __builtin_amdgcn_permlane32_swap(u, u, false, false);
    return __builtin_bit_cast(float, (threadIdx.x & 32) ? r[0] : r[1]);
}
#define WAITV(n) asm volatile("s_waitcnt vmcnt(" #n ")" ::: "memory")
#define WAITL0() asm volatile("s_waitcnt lgkmcnt(0)" ::: "memory")
#define BAR() do { asm volatile("" ::: "memory"); __builtin_amdgcn_s_barrier(); asm volatile("" ::: "memory"); } while (0)
#define SCHED() __builtin_amdgcn_sched_barrier(0)

#define SG(m, n) __builtin_amdgcn_sched_group_barrier(m, n, 0)
DI float fmx(float a, float b) { return __builtin_elementwise_maximum(a, b); }
typedef int i32x4 __attribute__((ext_vector_type(4)));
#define PIN(x) asm volatile("" : "+v"(x))
DI void raise(char* p, u64 tag) { (void)__hip_atomic_exchange((gu64*)p, tag, RLX); }
template <bool TWO_BARRIERS>
DI u64 poll(const char* p, u64 all, u64 need, u64 tag) {
    u64 v, zero, sv, left; unsigned cnt = SPIN_MAX;
#define POLL_LOOP ".Lpw%=:\n\ts_waitcnt vmcnt(0)\n\tv_cmp_ne_u64 %4, %6, %0\n\ts_and_b64 vcc, %4, %8\n\ts_cbranch_scc0 .Lpd%=\n\ts_sub_u32 %3, %3, 1\n\ts_cbranch_scc1 .Lpd%=\n\t" \
                  "global_atomic_add_x2 %0, %5, %1, off sc0\n\ts_branch .Lpw%=\n.Lpd%=:\n\ts_mov_b64 exec, %2"
#define POLL_OPS : "=&v"(v), "=&v"(zero), "=&s"(sv), "+s"(cnt), "=&s"(left) : "v"(p), "s"(tag), "s"(all), "s"(need) : "vcc", "scc", "memory"
    if (TWO_BARRIERS)
        asm volatile("s_mov_b64 %2, exec\n\ts_mov_b64 exec, %7\n\tv_mov_b64 %1, 0\n\tglobal_atomic_add_x2 %0, %5, %1, off sc0\n\ts_barrier\n\ts_barrier\n" POLL_LOOP POLL_OPS);
    else
        asm volatile("s_mov_b64 %2, exec\n\ts_mov_b64 exec, %7\n\tv_mov_b64 %1, 0\n\tglobal_atomic_add_x2 %0, %5, %1, off sc0\n" POLL_LOOP POLL_OPS);
    return left;
}

extern "C" __global__ void __launch_bounds__(512, 2)
fused(const char* xa, const char* wa, const char* woa, const char* xb, const char* wb, const char* wob, char* out, char* ws) {
    __shared__ __attribute__((aligned(16))) char smem[LDS_BYTES];
    STAMP_DECL;
    STAMP(0);
    const int b = blockIdx.x, xcd = b & 7, idx = b >> 3;
    const int br = xcd & 1, jj = idx >> 3, ty = idx & 7, j = (xcd >> 1) * 4 + jj;
    unsigned d0 = threadIdx.x * 128;
    {
        u64 pc;
        asm volatile("s_cmp_lg_u32 %2, 7\n\ts_cbranch_scc1 .Lskip%=\n\tv_cmp_gt_u32 vcc, fused_end-.Lhere%=, %1\n\ts_cbranch_vccz .Lskip%=\n\ts_mov_b64 exec, vcc\n\t"
                     "s_getpc_b64 %0\n.Lhere%=:\n\tglobal_load_dword %1, %1, %0\n\ts_mov_b64 exec, -1\n.Lskip%=:"
                     : "=&s"(pc), "+v"(d0) : "s"(ty) : "vcc", "scc", "memory");
    }
    const int wave = __builtin_amdgcn_readfirstlane(threadIdx.x >> 6), lane = threadIdx.x & 63;
    const int r3 = lane >> 3, u = lane & 7;
    const int lbase = (int)(unsigned)(size_t)(LAS char*)smem;
    int vx0 = r3 * 896 + ((u ^ (r3 >> 1)) << 4), vx1 = vx0 ^ 64;
    int vw = r3 * 2688 + ((u ^ (((r3 >> 1) & 1) << 2)) << 4);
    const int rot = ROT == 2 ? ((ty + j) & 1) * 4 : 0;
    const int sx = j * (128 * 896), sw = ty * 128;
    int e0 = sx + wave * 14336 + rot * 128, e1 = sw + (rot * 64 + wave * 8) * 2688, e2 = lbase + RING + wave * 2048, e3 = lbase + RING + 16384 + wave * 1024;
    asm volatile("" : "+s"(e0), "+s"(e1), "+s"(e2), "+s"(e3)); asm volatile("" : "+v"(vx0), "+v"(vx1), "+v"(vw));
    SCHED();
    const u32x4 rx = srd(br ? xb : xa, XB), rw = srd(br ? wb : wa, WB);
    if (ty < 7) {
        dma16(rx, e2, vx0, e0);
        dma16(rw, e3, vw, e1);
        dma16(rx, e2 + 1024, vx1, e0 + 7168);
        dma16(rw, e3 + 8192, vw, e1 + 896);
    } else {
        {
            const int ln = jj * 392 + (threadIdx.x < 392 ? threadIdx.x : 0), row = (ln * 9363) >> 16;
            const int vl = row * 2688 + 1792 + (ln - row * 7) * 128;
            asm volatile("global_load_dword %0, %1, %2" : "+v"(d0) : "v"(vl), "s"(br ? wb : wa) : "memory");
            asm volatile("global_load_dword %0, %1, %2" : "+v"(d0) : "v"(vl + 224 * 2688), "s"(br ? wb : wa) : "memory");
        }
        const int wl = (jj * 784 + (threadIdx.x < 392 ? threadIdx.x : 0)) * 128;
        asm volatile("global_load_dword %0, %1, %2" : "+v"(d0) : "v"(wl), "s"(br ? wob : woa) : "memory");
        asm volatile("global_load_dword %0, %1, %2" : "+v"(d0) : "v"(wl + 392 * 128), "s"(br ? wob : woa) : "memory");
        asm volatile("s_waitcnt vmcnt(0)" : "+v"(d0) :: "memory");
    }
    u64 tag = *(const __attribute__((address_space(4))) u64*)ws;
    auto make_tag = [&]() { asm volatile("" : "+s"(tag)); tag = tag * 0x9E3779B97F4A7C15ull + 0x7F4A7C159E3779B9ull; asm volatile("" : "+s"(tag)); };
    const int w4 = wave & 3;
    const bool aux = wave >= 4;
    const int hh = lane >> 5, t = lane & 31, blk = (lane >> 4) & 1, q = (lane >> 2) & 3, p = lane & 3;
    const int T = 4 * j + w4;
    const bool local = br == 0 && ty < 7;
    typedef int i32x8 __attribute__((ext_vector_type(8)));
    typedef int i32x16 __attribute__((ext_vector_type(16)));
    const __attribute__((address_space(4))) char* te = (const __attribute__((address_space(4))) char*)g_tab.e[(min(j, 2) * 2 + br) * 32 + ty * 4 + w4];
    i32x8 tb8 = *(const __attribute__((address_space(4))) i32x8*)(te + 64);
    int h, c0;
    auto use_tab = [&]() { asm volatile("" : "+s"(tb8)); h = tb8[E_H - 16]; c0 = tb8[E_C0 - 16]; };
    const __amdgpu_buffer_rsrc_t rxs = rsrc(ws + WS_X, X_BYTES);
    const int wb0 = (8 * hh + q) * 128 + ((blk * 2 + (p & 1)) << 4) + (p >> 1) * 8 + ((q >> 1) << 6), wb1 = wb0 ^ 64;

    const unsigned NEGB = 0xF149F2CAu;
    f32x16 cd, co;
    u32x4 pm[2];
    int nd, no, lm;
    auto mask_bits = [&]() {
        const unsigned lo = (2u << t) - 1u;
        nd = (int)(~lo >> (4 * hh)); lm = (int)(lo >> (4 * hh)); no = T < 8 ? -1 : lm;
    };
    auto mask_reg = [&](int r) {
        const int bit = (r & 3) + 8 * (r >> 2);
        float a = __builtin_bit_cast(float, __builtin_amdgcn_sbfe(nd, bit, 1) & NEGB), c = __builtin_bit_cast(float, __builtin_amdgcn_sbfe(no, bit, 1) & NEGB);
        PIN(a); PIN(c); cd[r] = a; co[r] = c;
        if (r & 1) { unsigned m = __builtin_amdgcn_perm(__builtin_amdgcn_sbfe(lm, bit, 1), __builtin_amdgcn_sbfe(lm, bit - 1, 1), 0x05040100u); PIN(m); pm[r >> 3][(r >> 1) & 3] = m; }
    };
    auto koff = [&](int cf, int s) { return (2 * s + hh) * 512 + ((((t >> 2) ^ ((cf + 2 * s + hh) & 3)) << 2) + (t & 3)) * 16; };
    auto voff = [&](int hd, int cf, int tt) {
        const int pc8 = (4 * blk + 2 * (p & 1) + (p >> 1) + (hd & 1)) & 7, cc = pc8 >> 1;
        return cc * 512 + ((((2 * tt + hh) ^ ((cf + cc) & 3)) << 2) + q) * 16 + (pc8 & 1) * 8;
    };
    auto qfix = [&](int hd, u32x4& q0, u32x4& q1) {
        const bool z0 = (hd & 1) && !hh, z1 = !(hd & 1) && hh;
        q0.x = z0 ? 0u : q0.x; q0.y = z0 ? 0u : q0.y; q1.z = z1 ? 0u : q1.z; q1.w = z1 ? 0u : q1.w;
    };
    auto ex = [&](f32x16& s) {
#pragma unroll
        for (int r = 0; r < 16; ++r) s[r] = __builtin_amdgcn_exp2f(s[r]);
    };
    auto rowmax = [&](const f32x16& s) {
        float m = fmx(s[0], s[1]);
#pragma unroll
        for (int r = 2; r < 16; r += 2) m = fmx(fmx(m, s[r]), s[r + 1]);
        const unsigned mu = __builtin_bit_cast(unsigned, m);
        const auto r = __builtin_amdgcn_permlane32_swap(mu, mu, false, false);
        const unsigned r0 = r[0], r1 = r[1];
        return fmx(__builtin_bit_cast(float, r0), __builtin_bit_cast(float, r1));
    };
    const __amdgpu_buffer_rsrc_t ros = rsrc(ws + WS_O, O_BYTES);
    auto put_o = [&](f32x16 o, float l, int rec) {
        o = o * __builtin_amdgcn_rcpf(l);
        store_dev(__builtin_bit_cast(u32x4, pack8(o, 0)), ros, lane * 16, rec);
        store_dev(__builtin_bit_cast(u32x4, pack8(o, 1)), ros, lane * 16 + 1024, rec);
    };
    auto ones = [&](int back, bool two) {
        const int bb = back + (two ? hh : 0);
        if (two || !hh) *(u32x2*)(smem + LR + w4 * 4096 + (2 - bb) * 16384 + 2048 + ((h & 1) ? 0 : 1544) + t * 16) = (u32x2){j >= bb ? 0x3F80u : 0u, 0u};
    };
    LAS char* wd = (LAS char*)(smem + LW + 7 * 512 + 8);

    #define wk ((LAS char*)(smem + LR + 32768 + ((h & 1) ? 0 : 1544)))
#define FTAG ((tag | 1) & 0xBFFFBFFFBFFFBFFFull)
    auto arrive = [&](int n) { if (lane == 0) *(volatile LAS u64*)(wk + (n + w4) * 16) = FTAG; };
    auto await4 = [&](int n) {
#pragma unroll 1
        for (int i = 0; i < SPIN_MAX * 4; ++i) if (__all(*(volatile LAS u64*)(wk + (n + p) * 16) == FTAG)) break;
        asm volatile("" ::: "memory");
    };
    bf16x8 wo[28];
    auto read_wo = [&](int base) {
        int a = base + ((w4 >> 1) ? wb1 : wb0) + hh * (20 * 128);
        PIN(a);
#pragma unroll
        for (int ks = 0; ks < 28; ++ks) {
            const int i = ks % 7, row = (ks / 7) * 112 + (i < 6 ? (i / 3) * 56 + (i % 3) * 8 : 24);
            wo[ks] = tr_frag(smem + a + row * 128, smem + a + (row + (i < 6 ? 4 : 56)) * 128);
        }
    };
    const int vwo = r3 * 896 + ((u ^ (((r3 >> 1) & 1) << 2)) << 4);
    const u32x4 rwo = srd(br ? wob : woa, OB);

    if (__builtin_expect(aux, 1)) {
        const bool x7 = ty == 7;
        i32x16 tb = *(const __attribute__((address_space(4))) i32x16*)te;
        if (ty < 7) {
            int ch = rot, sg = 0;
#pragma unroll 1
            for (int c = 1; c < 7; ++c) {
                ++ch; if (ch == 7) ch = 0;
                ++sg; if (sg == 3) sg = 0;
                const int st = lbase + RING + sg * STAGE;
#pragma unroll
                for (int i = 0; i < 4; ++i) dma16(rx, st + w4 * 4096 + i * 1024, (i & 1) ? vx1 : vx0, sx + (w4 * 4 + i) * 7168 + ch * 128);
#pragma unroll
                for (int cb = 0; cb < 2; ++cb)
#pragma unroll
                    for (int i = 0; i < 2; ++i) dma16(rw, st + 16384 + cb * 8192 + w4 * 2048 + i * 1024, vw, sw + (ch * 64 + w4 * 16 + i * 8) * 2688 + cb * 896);
                WAITV(8); BAR();
            }
#pragma unroll 1
            for (int c = 0; c < 7; ++c) {
                ++ch; if (ch == 7) ch = 0;
#pragma unroll
                for (int i = 0; i < 2; ++i) dma16(rw, lbase + LA + c * 8192 + w4 * 2048 + i * 1024, vw, sw + (ch * 64 + w4 * 16 + i * 8) * 2688 + 1792);
                if (c == 3) { WAITV(8); BAR(); }
            }
            BAR();
        }
        make_tag(); use_tab();
        asm volatile("" : "+s"(tb));
        const int jo = j * (4 * XT), sl = tb8[E_SL - 16];
        int ra0 = tb[E_RA0] + jo, ra1 = tb[E_RA1] + jo, ra2 = tb[E_RA2] + jo, rb0 = tb[E_RB0] + jo, rb1 = tb[E_RB1] + jo, rq = tb[E_RQ] + jo, lh = lbase + LR + w4 * 4096;
        asm volatile("" : "+s"(ra0), "+s"(ra1), "+s"(ra2), "+s"(rb0), "+s"(rb1), "+s"(rq), "+s"(lh));
        const int lb0 = lbase + tb[E_LB0], lb1 = lbase + tb[E_LB1];
        const int hd[2] = {tb[E_HD0], tb[E_HD1]}, cf[2] = {tb[E_CF0], tb[E_CF1]}, po[2] = {tb8[E_PO0 - 16], tb8[E_PO1 - 16]};
        const u64 mb = (u64)(unsigned)tb[E_MB] << 32, mall = mb | (unsigned)tb[E_MA];
        const char* f1 = ws + (WS_F1 + j * 256 + tb[E_PG0]) + (hh * 4096 - (r3 & 3) * 256 + (lane & 4) * tb[E_PGD] + p * 8);
        int l16 = lane * 16;
        PIN(f1); PIN(l16);
        int lz = lane; PIN(lz);
        const int g4 = lz >> 4, c16 = lz & 15;
        int fo[2], ko[2], qo[2], vo[2][2];
        f32x4 md, mo;
        const bool pt = x7 || g4 == sl;
#pragma unroll
        for (int e = 0; e < 2; ++e) fo[e] = g4 * 512 + (((c16 >> 1) ^ ((cf[e] + g4) & 3)) << 6) + (c16 & 1) * 32;
        int qs = pt ? fo[1] : OOB; PIN(qs);
        if (!local) {
#pragma unroll
            for (int e = 0; e < 2; ++e) { ko[e] = LW + 40960 - w4 * 8192 + po[e] + fo[e]; qo[e] = LW + w4 * 4096 + po[e] + fo[e]; PIN(ko[e]); PIN(qo[e]); }
#pragma unroll
            for (int r = 0; r < 4; ++r) {
                const int d = c16 - 4 * g4 - r;
                md[r] = (unsigned)d < 8u ? 0.f : -1.0e30f; mo[r] = (d < -8 && T) ? 0.f : -1.0e30f;
            }
            PIN(md); PIN(mo);
        }
        if (ty < 7) { WAITV(6); BAR(); }
        if (wave == 4 && lane < 2) *(volatile LAS u64*)(wd + lane * 16) = 0;
        asm volatile("s_waitcnt vmcnt(0) lgkmcnt(0)" ::: "memory"); BAR();
        STAMP(4);
        const u32x4 rxx = srd(ws + WS_X, X_BYTES);
        auto tell = [&](int n) { if (lane == 0) *(volatile LAS u64*)(n < 2 ? wd + n * 16 : wk + 64) = FTAG; };
        auto hear = [&](int n) {
#pragma unroll 1
            for (int i = 0; i < SPIN_MAX * 4; ++i) { if (__all(*(volatile LAS u64*)(n < 2 ? wd + n * 16 : wk + 64) == FTAG)) break; __builtin_amdgcn_s_sleep(1); }
            asm volatile("" ::: "memory");
        };
        auto fetch_b = [&](int v) {
            const int go = v * (56 * 512), lo = v * 4096;
            if (x7) { dma32_dev(rxx, lb0 + lo, l16, rb0 + go); dma32_dev(rxx, lb1 + lo, l16, rb1 + go); }
            else {
                if (w4 == 0) dma64_dev(rxx, lb1 + lo, l16, rb1 + go);
                if (sl != 8 && lane < 32) { dma16_dev(rxx, lb0 + lo, l16, rb0 + go); if (w4) dma16_dev(rxx, lb1 + lo, l16, rb1 + go); }
            }
        };
        auto fetch_a = [&](int v) {
            const int go = v * (56 * 512), lo = v * 2048;
            dma32_dev(rxx, lh + 32768 + lo, l16, ra0 + go);
            dma32_dev(rxx, lh + 16384 + lo, l16, ra1 + go);
            dma32_dev(rxx, lh + lo, l16, ra2 + go);
        };
        STAMP(5);
        unsigned left = 1;
        if (!local) { if (wave == 4) { left = __builtin_amdgcn_readfirstlane((unsigned)poll<false>(f1, mall, mb, tag)); tell(0); if (!left) tell(1); } else hear(0); }
        STAMP(2);
        u32x4 qp[2];
        asm volatile("buffer_load_dwordx4 %0, %2, %3, %4 offen sc1\n\tbuffer_load_dwordx4 %1, %2, %3, %4 offen offset:16 sc1"
                     : "=&v"(qp[0]), "=&v"(qp[1]) : "v"(qs), "s"(rxx), "s"(rq) : "memory");
        if (!local) fetch_b(0);
        if (wave == 4) { if (left) { poll<false>(f1, mall ^ mb, mall ^ mb, tag); tell(1); } } else hear(1);
        STAMP(6);
        dma32_dev(rxx, lh + 2048, l16, ra0 - 56 * 512);
        fetch_a(0);
        WAITV(8);
        PIN(qp[0]); PIN(qp[1]);
        STAMP(3);
        bf16x8 pe[2][2];
        float ls[2][2];
        if (!local) {
            f32x4 sd[2][2], so[2][2];
#pragma unroll
            for (int e = 0; e < 2; ++e)
#pragma unroll
                for (int y = 0; y < 2; ++y) {
                    const char* kb = smem + ko[e] + y * 16;
                    u32x4 qv = *(const u32x4*)(smem + qo[e] + y * 16);
                    const bool a = pt && (e || x7), z0 = (hd[e] & 1) && g4 == 0, z1 = !(hd[e] & 1) && g4 == 3;
                    qv = (u32x4){a ? qp[y].x : qv.x, a ? qp[y].y : qv.y, a ? qp[y].z : qv.z, a ? qp[y].w : qv.w};
                    qv = (u32x4){z0 ? 0u : qv.x, z0 ? 0u : qv.y, z1 ? 0u : qv.z, z1 ? 0u : qv.w};
                    sd[e][y] = MFMA16(*(const bf16x8*)kb, __builtin_bit_cast(bf16x8, qv), md);
                    so[e][y] = MFMA16(*(const bf16x8*)(kb + 8192), __builtin_bit_cast(bf16x8, qv), mo);
                }
            const bf16x8 one = __builtin_bit_cast(bf16x8, (u32x4){0x3F803F80u, 0x3F803F80u, 0x3F803F80u, 0x3F803F80u});
            float m[4];
#pragma unroll
            for (int i = 0; i < 4; ++i) {
                const f32x4 a = sd[i >> 1][i & 1], c = so[i >> 1][i & 1];
                m[i] = fmx(fmx(fmx(fmx(a[0], a[1]), a[2]), fmx(fmx(a[3], c[0]), c[1])), fmx(c[2], c[3]));
            }
#pragma unroll
            for (int i = 0; i < 4; ++i) {
                const unsigned mu = __builtin_bit_cast(unsigned, m[i]);
                const auto r = __builtin_amdgcn_permlane32_swap(mu, mu, false, false);
                const unsigned r0 = r[0], r1 = r[1];
                m[i] = fmx(__builtin_bit_cast(float, r0), __builtin_bit_cast(float, r1));
            }
#pragma unroll
            for (int i = 0; i < 4; ++i) {
                const unsigned mu = __builtin_bit_cast(unsigned, m[i]);
                const auto r = __builtin_amdgcn_permlane16_swap(mu, mu, false, false);
                const unsigned r0 = r[0], r1 = r[1];
                m[i] = fmx(__builtin_bit_cast(float, r0), __builtin_bit_cast(float, r1));
            }
            SCHED();
            f32x4 sm[4];
#pragma unroll
            for (int i = 0; i < 4; ++i) {
                const f32x4 a = sd[i >> 1][i & 1], c = so[i >> 1][i & 1];
                f32x8 x = {a[0], a[1], a[2], a[3], c[0], c[1], c[2], c[3]};
                x -= m[i];
#pragma unroll
                for (int r = 0; r < 8; ++r) x[r] = __builtin_amdgcn_exp2f(x[r]);
                pe[i >> 1][i & 1] = __builtin_convertvector(x, bf16x8);
                sm[i] = MFMA16(one, pe[i >> 1][i & 1], (f32x4){});
                SCHED();
            }
#pragma unroll
            for (int i = 0; i < 4; ++i) ls[i >> 1][i & 1] = __builtin_amdgcn_rcpf(sm[i][0]);
#pragma unroll
            for (int i = 0; i < 4; ++i) { PIN(pe[i >> 1][i & 1]); PIN(ls[i >> 1][i & 1]); }
        }
        STAMP(10);
        asm volatile("s_waitcnt vmcnt(0) lgkmcnt(0)" ::: "memory");
        STAMP(7);
        __builtin_amdgcn_s_setprio(2);
        if (wave == 4) { poll<true>(f1 + 8192, mall, mall, tag); tell(2); }
        else {
            BAR(); BAR();
            dma_rows<14>(rwo, lbase + LA + w4 * 1024, vwo, w4 * 7168 + (ty & 6) * 128);
            hear(2);
        }
        STAMP(14);
        if (!local) fetch_b(1);
        fetch_a(1);
        if (!YEARLY) dma_rows<YB0>(rwo, lbase + LA + w4 * 1024, vwo, w4 * 7168 + (ty & 6) * 128);
        if (!local) {
            PIN(lz);
#pragma unroll
            for (int e = 0; e < 2; ++e)
#pragma unroll
                for (int s = 0; s < 2; ++s) {
                    const int pc8 = (2 * (lz & 3) + s + (hd[e] & 1)) & 7, cc = pc8 >> 1;
                    vo[e][s] = LW + 40960 - w4 * 8192 + 4096 + po[e] + cc * 512 + (((lz >> 3) ^ ((cf[e] + cc) & 3)) << 6) + (q & 1) * 32 + (pc8 & 1) * 8; PIN(vo[e][s]);
                }
        }
        asm volatile("s_waitcnt vmcnt(%0)" :: "i"(YEARLY ? 0 : YB0) : "memory");
        ones(0, true); ones(2, false);
        WAITL0();
        arrive(0);
        if (!YEARLY) dma_rows<14 - YB0>(rwo, lbase + LA + (w4 + 4 * YB0) * 1024, vwo, (w4 + 4 * YB0) * 7168 + (ty & 6) * 128);
        else if (wave == 4) dma_rows<14>(rwo, lbase + LA, vwo, (ty & 6) * 128);
        STAMP(9);
        if (!local) {
            u32x4 ov[2][2];
#pragma unroll
            for (int e = 0; e < 2; ++e)
#pragma unroll
                for (int y = 0; y < 2; ++y) {
                    const char* v0 = smem + vo[e][0] + y * 16; const char* v1 = smem + vo[e][1] + y * 16;
                    const f32x4 a = MFMA16(tr_frag(v0, v0 + 8192), pe[e][y], (f32x4){}), c = MFMA16(tr_frag(v1, v1 + 8192), pe[e][y], (f32x4){});
                    f32x8 x = {a[0], a[1], a[2], a[3], c[0], c[1], c[2], c[3]};
                    x *= ls[e][y];
                    ov[e][y] = __builtin_bit_cast(u32x4, __builtin_convertvector(x, bf16x8));
                }
#pragma unroll
            for (int e = 0; e < 2; ++e)
#pragma unroll
                for (int y = 0; y < 2; ++y) store_dev(ov[e][y], ros, g4 * 512 + c16 * 16 + y * 256, ((64 + T) * 16 + hd[e]) * 2048);
        }
        STAMP(11);
        __builtin_amdgcn_s_setprio(0);
        if (!local) { arrive(8); WAITV(4); arrive(16); await4(8); } else { WAITV(0); arrive(16); }
        dma_rows<6>(rwo, lbase + LW + w4 * 1024, vwo, ty < 6 ? w4 * 7168 + (ty | 1) * 128 : OOB);
        if (!local) { WAITV(6); if (lane < 2) raise(ws + WS_F2 + (64 + T) * 128 + (lane ? hd[1] : hd[0]) * 8, tag); }
        if (ty < 6) dma_rows<8>(rwo, lbase + LW + (w4 + 24) * 1024, vwo, (w4 + 24) * 7168 + (ty | 1) * 128);
        if (b == 0) {
            poll<false>(ws + WS_F1 + w4 * 2048 + lane * 32, ~0ull, ~0ull, tag);
            arrive(12);
            if (wave == 4) { await4(12); if (lane == 0) __hip_atomic_store((gu64*)ws, tag, RLX); }
        }
        WAITV(0);
    } else {
        mask_bits();
        if (ty < 7) {
            int xo[2][4], wo_[2][2];
#pragma unroll
            for (int g = 0; g < 2; ++g) {
#pragma unroll
                for (int ks = 0; ks < 4; ++ks) { xo[g][ks] = RING + g * 65536 + (w4 * 32 + t) * 128 + (((2 * ks + hh) ^ ((t >> 1) & 7)) << 4); PIN(xo[g][ks]); }
                wo_[g][0] = RING + g * 65536 + wb0; wo_[g][1] = RING + g * 65536 + wb1; PIN(wo_[g][0]); PIN(wo_[g][1]);
            }
            int es[2], el[2][2], g0;
            unsigned pq, pk;
            char* f1;
            auto put = [&](const f32x16& a0, const f32x16& a1, int sec, unsigned pub, u32x4 (&d)[2][2]) {
#pragma unroll
                for (int e = 0; e < 2; ++e)
#pragma unroll
                    for (int s = 0; s < 2; ++s) {
                        d[e][s] = __builtin_bit_cast(u32x4, pack8(e ? a1 : a0, s));
                        if ((pub >> (2 * e + s)) & 1) store_dev(d[e][s], rxs, es[s] + e * 2048, g0 + sec * (56 * 512));
                    }
            };
            auto keep = [&](int lo, const u32x4 (&d)[2][2]) {
#pragma unroll
                for (int e = 0; e < 2; ++e)
#pragma unroll
                    for (int s = 0; s < 2; ++s) if (el[e][s] >= 0) *(u32x4*)(smem + el[e][s] + lo) = d[e][s];
            };
            f32x16 acc[4];
            bf16x8 xf[28], wf[2][4];
            const f32x16 zero = {};
            WAITV(0); BAR();
            STAMP(1); CST(20);
            auto rd = [&](int n) {
                const int sg = (n >> 2) % 3, ks = n & 3;
                const char* st = smem + (sg & 1) * STAGE;
#pragma unroll
                for (int i = 3; i >= 0; --i) {
                    const char* a = st + wo_[sg >> 1][i & 1] + 16384 + (i >> 1) * 8192 + ks * 2048;
                    wf[n & 1][i] = tr_frag(a, a + 512);
                    if (i == 3) xf[n] = *(const bf16x8*)(st + xo[sg >> 1][ks]);
                }
            };
            rd(0);
            SCHED();
#pragma unroll
            for (int n = 0; n < 28; ++n) {
                if (n == 11) {
                    make_tag(); use_tab();
#pragma unroll
            for (int s = 0; s < 2; ++s) {
                const int sw16 = ((((t >> 2) ^ (2 * s + hh)) << 2) + (t & 3)) * 16;
                es[s] = (2 * s + hh) * 512 + sw16; PIN(es[s]);
#pragma unroll
                for (int e = 0; e < 2; ++e) {
                    const int ps = tb8[E_PS - 16] + 4 * e + 2 * s + hh;
                    el[e][s] = (unsigned)ps < 8u ? ps * 512 + sw16 : -1; PIN(el[e][s]);
                }
            }
            pq = tb8[E_PQ - 16]; pk = tb8[E_PK - 16];
            g0 = (br * 64 + T) * XT + ty * 4096;
            f1 = ws + WS_F1 + (br * 16 + j) * 256 + ty * 32 + w4 * 8;
            asm volatile("" : "+s"(g0), "+s"(f1));
                    SCHED();
                }
                if ((n & 3) == 3) { WAITL0(); BAR(); SCHED(); }
                if (n < 27) rd(n + 1);
#pragma unroll
                for (int i = 3; i >= 0; --i) acc[i] = MFMA(wf[n & 1][i], xf[n], n ? acc[i] : zero);
                if (n < 27) { SG(0x8, 1); SG(0x100, 3); SG(0x8, 1); SG(0x100, 3); SG(0x8, 1); SG(0x100, 3); SG(0x8, 1); }
                SCHED();
            }
            STAMP(2);
            const float qs = 0.18898223650461361f * 1.4426950408889634f;
            {
                u32x4 dk[2][2], dq[2][2];
                put(acc[2], acc[3], 1, pk, dk);
                put(acc[0] * qs, acc[1] * qs, 0, pq, dq);
                if (br) { keep(LW + 40960 - w4 * 8192, dk); keep(LW + w4 * 4096, dq); }
            }
            STAMP(3);
            BAR();
            bf16x8 wv[8];
            u32x4 dv[2][2];
            auto rv = [&](int n) { const char* a = smem + LA + (n % 28) * 2048 + (n >= 28 ? wb1 : wb0); wv[n & 7] = tr_frag(a, a + 512); };
            auto putv = [&](int e, int s) {
                dv[e][s] = __builtin_bit_cast(u32x4, pack8(acc[2 + e], s));
                store_dev(dv[e][s], rxs, es[s] + e * 2048, ((pk >> (2 * e + s)) & 1) ? g0 + 112 * 512 : OOB);
            };
#pragma unroll
            for (int n = 0; n < 6; ++n) rv(n);
            PIN(nd); PIN(no); PIN(lm);
            SCHED();
#pragma unroll
            for (int n = 0; n < 56; ++n) {
                if (n == BV1) { BAR(); SCHED(); }
                if (n == RAISE_B || n == RAISE_A) { if ((n == RAISE_B) == (br != 0)) { WAITV(0); if (lane == 0) raise(f1, tag); STAMP(9); } SCHED(); }
                acc[2 + n / 28] = MFMA(wv[n & 7], xf[n % 28], n % 28 ? acc[2 + n / 28] : zero);
                if (n + 6 < 56) rv(n + 6);
                if (n >= 36 && n < 52) mask_reg(n - 36);
                SG(0x8, 1); SG(0x100, 2); SG(0x2, 8);
                SCHED();
                if (n == 30 || n == 32) { putv(0, (n - 30) >> 1); SCHED(); }
            }
            STAMP(4); CST(21);
            putv(1, 0); putv(1, 1);
            if (br) keep(LW + 45056 - w4 * 8192, dv);
            STAMP(5);
            WAITV(0);
            if (lane == 0) raise(f1 + 8192, tag);
        } else {
            make_tag(); use_tab();
            BAR();
            if (threadIdx.x == 0) raise(ws + WS_F1 + (br * 16 + j) * 256 + 7 * 32, tag);
#pragma unroll
            for (int r = 0; r < 16; ++r) mask_reg(r);
        }
        const char* tl = smem + LR + w4 * 4096;
        int fo[2] = {koff(c0, 0), koff(c0, 1)}, vo[2] = {voff(h, c0, 0) + 2048, voff(h, c0, 1) + 2048};
        PIN(fo[0]); PIN(fo[1]); PIN(vo[0]); PIN(vo[1]);
        STAMP(6);
        WAITL0(); BAR();
        STAMP(7);
        __builtin_amdgcn_s_setprio(1);
        u32x4 qf[2];
        qf[0] = *(const u32x4*)(tl + 2048 + fo[0]); qf[1] = *(const u32x4*)(tl + 2048 + fo[1]);
        auto kfrag = [&](int n, int s) { return *(const bf16x8*)(tl + (8 - n) * 4096 + fo[s]); };
        auto vfrag = [&](int n, int s) { const char* vb = tl + (8 - n) * 4096 + s * 256; return tr_frag(vb + vo[0], vb + vo[1]); };
        bf16x8 k0 = kfrag(0, 0), k1 = kfrag(0, 1), k2 = kfrag(8, 0), k3 = kfrag(8, 1);
        WAITL0(); BAR();
        CST(19); STAMP(10);
        qfix(h, qf[0], qf[1]);
        const bf16x8 q0 = __builtin_bit_cast(bf16x8, qf[0]), q1 = __builtin_bit_cast(bf16x8, qf[1]);
        auto qk = [&](int n, const f32x16& c) { f32x16 r = MFMA(kfrag(n, 0), q0, c); return MFMA(kfrag(n, 1), q1, r); };
        f32x16 cm, sa, sb;
        bf16x8 pp[8][2];
        cd = MFMA(k0, q0, cd); co = MFMA(k2, q0, co); cd = MFMA(k1, q1, cd); co = MFMA(k3, q1, co);
        k0 = kfrag(1, 0); k1 = kfrag(1, 1);
#pragma unroll
        for (int r = 0; r < 16; ++r) cd[r] = fmx(cd[r], co[r]);
        {
            const float m = -rowmax(cd);
#pragma unroll
            for (int r = 0; r < 16; ++r) cm[r] = m;
        }
        cd += cm;
        sa = cd;
        SCHED();
#define STG(n, cur, nxt) do { \
            if (n < 7) { nxt = MFMA(k0, q0, cm); nxt = MFMA(k1, q1, nxt); } \
            if (n < 6) { k0 = kfrag(n + 2, 0); k1 = kfrag(n + 2, 1); } \
            ex(cur); pp[n][0] = pack8(cur, 0); pp[n][1] = pack8(cur, 1); \
            if (n < 7) SG(0x8, 1); SG(0x400, 4); if (n < 7) SG(0x8, 1); if (n < 6) SG(0x100, 2); SG(0x400, 4); SG(0x2, 2); SG(0x400, 4); SG(0x2, 2); SG(0x400, 4); SG(0x2, 4); SCHED(); } while (0)
        STG(0, sa, sb); STG(1, sb, sa); STG(2, sa, sb); STG(3, sb, sa); STG(4, sa, sb); STG(5, sb, sa); STG(6, sa, sb); STG(7, sb, sa);
        bf16x8 pz[2];
#pragma unroll
        for (int s = 0; s < 2; ++s) {
            const u32x4 e = __builtin_bit_cast(u32x4, pp[0][s]);
            pp[0][s] = __builtin_bit_cast(bf16x8, e & pm[s]); pz[s] = __builtin_bit_cast(bf16x8, e & ~pm[s]); PIN(pz[s]);
        }
#pragma unroll
        for (int n = 0; n < 8; ++n) { PIN(pp[n][0]); PIN(pp[n][1]); }
        char* f2 = ws + WS_F2 + T * 128 + h * 8;
        int orec = (T * 16 + h) * 2048;
        const char* va = tl + vo[0]; const char* vb = tl + vo[1];
        asm volatile("" : "+s"(f2), "+s"(orec));
        STAMP(8);
        await4(0);
        STAMP(14);
        f32x16 o = {};
        auto pv = [&](int n, bf16x8 p0, bf16x8 p1) { o = MFMA(vfrag(n, 0), p0, o); o = MFMA(vfrag(n, 1), p1, o); };
        {
            bf16x8 vf[18];
            auto rdv = [&](int i) { const int off = (8 - (i >> 1)) * 4096 + (i & 1) * 256; vf[i] = tr_frag(va + off, vb + off); };
#pragma unroll
            for (int i = 0; i < 5; ++i) { rdv(i); SCHED(); }
#pragma unroll
            for (int i = 0; i < 18; ++i) {
                o = MFMA(vf[i], i < 16 ? pp[i >> 1][i & 1] : pz[i & 1], o);
                if (i + 5 < 18) rdv(i + 5);
                SCHED();
            }
        }
        auto publish = [&]() {
            const float lf = o[12];
            const unsigned lu = __builtin_bit_cast(unsigned, lf);
            const unsigned lx = __builtin_amdgcn_permlane32_swap(lu, lu, false, false)[1];
            put_o(o, __builtin_bit_cast(float, lx), orec);
        };
        publish();
        STAMP(11); CST(22);
        float big = fmx(__builtin_fabsf(o[0]), __builtin_fabsf(o[1]));
#pragma unroll
        for (int r = 2; r < 16; r += 2) big = fmx(fmx(big, __builtin_fabsf(o[r])), __builtin_fabsf(o[r + 1]));
        if (__builtin_expect(!__all(big < 1.0e36f), 0)) {
            const int nmid = T >= 7 ? 7 : T;
            float m = 0.f;
#pragma unroll 1
            for (int i = 1; i <= nmid; ++i) m = fmx(m, rowmax(qk(i, cm)));
            cd -= m; cm -= m;
            ex(cd);
            o = (f32x16){};
            const u32x4 e0 = __builtin_bit_cast(u32x4, pack8(cd, 0)), e1 = __builtin_bit_cast(u32x4, pack8(cd, 1));
            pv(0, __builtin_bit_cast(bf16x8, e0 & pm[0]), __builtin_bit_cast(bf16x8, e1 & pm[1]));
            pv(8, __builtin_bit_cast(bf16x8, e0 & ~pm[0]), __builtin_bit_cast(bf16x8, e1 & ~pm[1]));
#pragma unroll 1
            for (int i = 1; i <= nmid; ++i) {
                f32x16 s = qk(i, cm);
                ex(s); pv(i, pack8(s, 0), pack8(s, 1));
            }
            WAITV(0);
            publish();
        }
        WAITV(0);
        if (lane == 0) raise(f2, tag);
        __builtin_amdgcn_s_setprio(0);
        STAMP(12);
        await4(16);
        read_wo(LA);
    }
    STAMP(13);
    const int To = br * 64 + 4 * j + 2 * (ty & 1);
    const int tr = br ? 2 * (t & 15) + (t >> 4) : t;
    const int tb = LR + 32768 + ((wave >> 2) * 2 + (w4 & 1)) * 4096;
    int l16 = lane * 16, oa = (w4 & 1) * LR + hh * 2048 + t * 16, tw = tb + tr * 128 + (((4 * (w4 >> 1) + hh) ^ (tr & 7)) << 4), tg = tb + (w4 >> 1) * 2048 + r3 * 128 + ((u ^ r3) << 4);
    int ov = r3 * 1792 + u * 16;
    int oo = ((4 * j + 2 * (ty & 1) + (w4 & 1)) * 32 + (w4 >> 1) * 16) * 1792 + br * 896 + ((ty & 6) + (wave >> 2)) * 128;
    const int os = (To * 16 + w4) * 2048, ol = lbase + w4 * 2048;
    asm volatile("" : "+v"(l16), "+v"(oa), "+v"(tw), "+v"(tg), "+v"(ov)); asm volatile("" : "+s"(oo));
    const __amdgpu_buffer_rsrc_t rout = rsrc(out, 2048 * 1792);
    const u32x4 roo = srd(ws + WS_O, O_BYTES);
    const bool idle = aux && ty >= 6;
    if (wave == 0) poll<false>(ws + WS_F2 + To * 128 + lane * 8, 0xFFFFFFFFull, 0xFFFFFFFFull, tag);
    WAITL0(); BAR();
    STAMP(15);
    if (idle) { STAMP_FLUSH(); return; }
    auto geto = [&](int qd) { dma32_dev(roo, ol + qd * 8192, l16, os + qd * 8192); dma32_dev(roo, ol + qd * 8192 + LR, l16, os + qd * 8192 + 32768); };
    f32x16 acc = {};
    {
        bf16x8 of[2][7];
        auto rdo = [&](int qd) {
            const char* a = smem + oa + qd * 8192;
#pragma unroll
            for (int i = 0; i < 6; ++i) of[qd & 1][i] = *(const bf16x8*)(a + (i / 3) * 4096 + (i % 3) * 512);
            const u32x2 lo = *(const u32x2*)(a + 1536), hi = *(const u32x2*)(a + 4096 + 1536);
            of[qd & 1][6] = __builtin_bit_cast(bf16x8, (u32x4){lo.x, lo.y, hi.x, hi.y});
        };
        auto mmo = [&](int qd, int i0, int i1) {
#pragma unroll
            for (int i = i0; i < i1; ++i) acc = MFMA(wo[qd * 7 + i], of[qd & 1][i], acc);
            SCHED();
        };
        if (aux) read_wo(LW); else { geto(0); geto(1); geto(2); WAITV(8); }
        BAR(); rdo(0); SCHED();
        if (!aux) geto(3);
        mmo(0, 0, 3);
        WAITV(8);
        BAR(); rdo(1); SCHED();
        mmo(0, 3, 7); mmo(1, 0, 3);
        WAITV(4);
        BAR(); rdo(2); SCHED();
        mmo(1, 3, 7); mmo(2, 0, 3);
        WAITV(0);
        BAR(); rdo(3); SCHED();
        mmo(2, 3, 7); mmo(3, 0, 7);
    }
    STAMP(16);
    {
#pragma unroll
        for (int s = 0; s < 2; ++s) *(bf16x8*)(smem + (tw ^ (s << 5))) = pack8(acc, s);
        WAITL0(); BAR();
        const u32x4 v0 = *(const u32x4*)(smem + tg), v1 = *(const u32x4*)(smem + tg + 1024);
        SCHED();
        store_dev(v0, rout, ov, oo); store_dev(v1, rout, ov, oo + 8 * 1792);
    }
    STAMP(17);
    WAITV(0);
    STAMP(18);
    STAMP_FLUSH();
}

extern "C" __device__ __attribute__((noinline, used)) void fused_end() {}

__global__ void k_probe(int ia, int maxa) {
    __shared__ unsigned long long sa[1024];
    const int b = threadIdx.x;
    unsigned long long x0 = g_diag[(b * 2) * 32 + ia], x1 = g_diag[(b * 2 + 1) * 32 + ia];
    if (!maxa) { if (x0 == 0) x0 = ~0ull; if (x1 == 0) x1 = ~0ull; }
    sa[b] = maxa ? (x0 > x1 ? x0 : x1) : (x0 < x1 ? x0 : x1); __syncthreads();
    for (int o = 512; o > 0; o >>= 1) { if (b < o) { unsigned long long x = sa[b], y = sa[b + o]; sa[b] = maxa ? (x > y ? x : y) : (x < y ? x : y); } __syncthreads(); }
    if (b == 0) {
        const unsigned long long A = sa[0];
        unsigned long long t; int n = 0; do { asm volatile("s_memrealtime %0\n\ts_waitcnt lgkmcnt(0)" : "=s"(t) :: "memory"); } while (t - A < 6000 && ++n < 100000);
    }
}
extern "C" void kernel_launch(void* const* d_in, const int* in_sizes, int n_in, void* d_out, int out_size, void* d_ws, size_t ws_size, hipStream_t stream) {
    fused<<<256, 512, 0, stream>>>((const char*)d_in[0], (const char*)d_in[1], (const char*)d_in[2], (const char*)d_in[3], (const char*)d_in[4], (const char*)d_in[5], (char*)d_out, (char*)d_ws);
    k_probe<<<1, 1024, 0, stream>>>(1,1);
}
```

```cpp
#include <hip/hip_runtime.h>
#include <hip/hip_bf16.h>

typedef __bf16 bf16x8 __attribute__((ext_vector_type(8)));
typedef short s16x4 __attribute__((ext_vector_type(4)));
typedef float f32x16 __attribute__((ext_vector_type(16)));
typedef float f32x8 __attribute__((ext_vector_type(8)));
typedef float f32x4 __attribute__((ext_vector_type(4)));
typedef unsigned u32x4 __attribute__((ext_vector_type(4)));
typedef unsigned u32x2 __attribute__((ext_vector_type(2)));
typedef unsigned long long u64;
typedef __attribute__((address_space(1))) u64 gu64;
#define LAS __attribute__((address_space(3)))
#define DI __device__ __forceinline__
#define MFMA(a, b, c) __builtin_amdgcn_mfma_f32_32x32x16_bf16((a), (b), (c), 0, 0, 0)
#define MFMA16(a, b, c) __builtin_amdgcn_mfma_f32_16x16x32_bf16((a), (b), (c), 0, 0, 0)
#define RLX __ATOMIC_RELAXED, __HIP_MEMORY_SCOPE_AGENT

constexpr int XB = 2048 * 896, WB = 448 * 2688, OB = 448 * 896;
constexpr int STAGE = 32768, RING = 65536, LA = 0, LW = 57344, LR = 114688, LX = 106496, LDS_BYTES = 163840;
constexpr int WS_F1 = 4096, WS_F2 = 65536, WS_X = 1 << 20, XT = 168 * 512, X_BYTES = 2 * 64 * XT, WS_O = 16 << 20, O_BYTES = 2 * 64 * 16 * 2048;
constexpr int OOB = 0x40000000;
#define ROT 2
#define PF0 2
#define RAISE_A 27
#define RAISE_B 4
#define BV1 10
#define KH_DELAY 8
#define OPRE 10
#define OROT 0
#define XB0 5
#define YB0 3
#define YEARLY 1
#define HSL 0
#define F64 0
#define PPV 0
#define RDFIRST 1
constexpr int SPIN_MAX = 1 << 18;

enum { E_RA0, E_RA1, E_RA2, E_RB0, E_RB1, E_RQ, E_LB0, E_LB1, E_MA, E_MB, E_PG0, E_PGD, E_HD0, E_HD1, E_CF0, E_CF1, E_PO0, E_PO1, E_SL, E_H, E_C0, E_PS, E_PQ, E_PK };
struct Tab {
    int e[192][32];
    constexpr Tab() : e() {
        for (int i = 0; i < 192; ++i) {
            const int w4 = i & 3, ty = (i >> 2) & 7, br = (i >> 5) & 1, jc = i >> 6;
            const bool x7 = ty == 7, local = br == 0 && !x7;
            const int ha = (0x4ECA7530u >> (4 * ty)) & 15, hb = (0xBFD98621u >> (4 * ty)) & 15;
            const int ca = (7 * ha) >> 1, cb = (7 * hb) >> 1, cs = ca < cb ? ca : cb;
            const int h = br ? hb : ha, c0 = br ? cb : ca;
            const int nb = x7 ? 15 : (0xF63F30Fu >> (4 * ty)) & 15;
            const int sl = (ty == 2 || ty == 5) ? 3 : 0;
            const int two = (x7 || (br && nb != 15)) ? 0xFF : 0x0F;
            const int rt = (64 + w4) * XT;
            int* v = e[i];
            v[E_HD0] = x7 ? h : ha; v[E_HD1] = x7 ? h : hb; v[E_CF0] = x7 ? c0 : ca; v[E_CF1] = x7 ? c0 : cb;
            v[E_PO0] = x7 ? 0 : (ca - cs) * 512; v[E_PO1] = x7 ? 0 : (cb - cs) * 512;
            v[E_RA0] = w4 * XT + (56 + c0) * 512; v[E_RA1] = jc >= 1 ? v[E_RA0] - 4 * XT : OOB; v[E_RA2] = jc >= 2 ? v[E_RA0] - 8 * XT : OOB;
            v[E_RB0] = rt + (56 + (x7 ? c0 : cb + sl)) * 512;
            v[E_RB1] = (jc == 0 && w4 == 0) ? OOB : ((!x7 && w4 == 0) ? rt + (56 + cs) * 512 : v[E_RB0]) - XT;
            v[E_RQ] = local ? OOB : rt + v[E_CF1] * 512;
            v[E_LB0] = LW + 40960 - w4 * 8192 + (x7 ? 0 : v[E_PO1] + sl * 512); v[E_LB1] = (!x7 && w4 == 0) ? LX : v[E_LB0] + 8192;
            v[E_MA] = two | (jc >= 1 ? two << 8 : 0) | (jc >= 2 ? two << 16 : 0);
            v[E_MB] = local ? 0 : (x7 ? 0xFF : two & 0xF0) | (jc ? (two & 0x88) << 8 : 0);
            v[E_PG0] = (x7 ? 1 + 3 * br : ty) * 32; v[E_PGD] = (x7 ? 1 : (nb == 15 ? 0 : nb - ty)) * 8;
            v[E_SL] = nb == 15 ? 8 : sl;
            v[E_H] = h; v[E_C0] = c0; v[E_PS] = 8 * ty - cs;
            v[E_PQ] = br ? (0x1189188u >> (4 * ty)) & 15 : 15; v[E_PK] = w4 != 3 ? v[E_PQ] : 15;
        }
    }
};
__device__ const Tab g_tab = Tab();

#define STAMP(i)
#define CST(i)
#define STAMP_DECL
#define STAMP_FLUSH()

DI u32x4 srd(const void* p, unsigned bytes) { const u64 a = (u64)p; return (u32x4){(unsigned)a, (unsigned)(a >> 32) & 0xffffu, bytes, 0x00020000u}; }
DI __amdgpu_buffer_rsrc_t rsrc(const void* p, int bytes) { return __builtin_amdgcn_make_buffer_rsrc((void*)p, 0, bytes, 0x00020000); }
DI void dma16(u32x4 r, int lds, int voff, int soff) {
    asm volatile("s_mov_b32 m0, %0\n\ts_nop 0\n\tbuffer_load_dwordx4 %1, %2, %3 offen lds" :: "s"(lds), "v"(voff), "s"(r), "s"(soff) : "memory");
}
DI void dma16_dev(u32x4 r, int lds, int voff, int soff) {
    asm volatile("s_mov_b32 m0, %0\n\ts_nop 0\n\tbuffer_load_dwordx4 %1, %2, %3 offen sc1 lds" :: "s"(lds), "v"(voff), "s"(r), "s"(soff) : "memory");
}
DI void dma64_dev(u32x4 r, int lds, int voff, int soff) {
    asm volatile("s_mov_b32 m0, %0\n\ts_nop 0\n\tbuffer_load_dwordx4 %1, %2, %3 offen sc1 lds\n\tbuffer_load_dwordx4 %1, %2, %3 offen offset:1024 sc1 lds\n\t"
                 "buffer_load_dwordx4 %1, %2, %3 offen offset:2048 sc1 lds\n\tbuffer_load_dwordx4 %1, %2, %3 offen offset:3072 sc1 lds" :: "s"(lds), "v"(voff), "s"(r), "s"(soff) : "memory");
}
DI void dma32_dev(u32x4 r, int lds, int voff, int soff) {
    asm volatile("s_mov_b32 m0, %0\n\ts_nop 0\n\tbuffer_load_dwordx4 %1, %2, %3 offen sc1 lds\n\tbuffer_load_dwordx4 %1, %2, %3 offen offset:1024 sc1 lds"
                 :: "s"(lds), "v"(voff), "s"(r), "s"(soff) : "memory");
}
DI void store_dev(u32x4 v, __amdgpu_buffer_rsrc_t r, int voff, int soff) {
    __builtin_amdgcn_raw_buffer_store_b128(v, r, voff, soff, 16);
    asm volatile("s_nop 1" :: "v"(v) : "memory");
}
template <int N>
DI void dma_rows(u32x4 r, int lds, int voff, int soff) {
    asm volatile("s_mov_b32 m0, %0\n\ts_nop 0\n\tbuffer_load_dwordx4 %2, %3, %1 offen lds\n.rept %4\n\ts_add_u32 m0, m0, 0x1000\n\ts_add_u32 %1, %1, 0x7000\n\tbuffer_load_dwordx4 %2, %3, %1 offen lds\n.endr"
                 : "+s"(lds), "+s"(soff) : "v"(voff), "s"(r), "i"(N - 1) : "scc", "memory");
}
DI bf16x8 tr_frag(const char* a0, const char* a1) {
    s16x4 lo = __builtin_amdgcn_ds_read_tr16_b64_v4i16((LAS s16x4*)a0);
    s16x4 hi = __builtin_amdgcn_ds_read_tr16_b64_v4i16((LAS s16x4*)a1);
    return __builtin_bit_cast(bf16x8, __builtin_shufflevector(lo, hi, 0, 1, 2, 3, 4, 5, 6, 7));
}
DI bf16x8 pack8(const f32x16& v, int s) {
    f32x8 t = {v[8 * s], v[8 * s + 1], v[8 * s + 2], v[8 * s + 3], v[8 * s + 4], v[8 * s + 5], v[8 * s + 6], v[8 * s + 7]};
    return __builtin_convertvector(t, bf16x8);
}
DI float xhalf(float v) {
    const unsigned u = __builtin_bit_cast(unsigned, v);
    const auto r = __builtin_amdgcn_permlane32_swap(u, u, false, false);
    return __builtin_bit_cast(float, (threadIdx.x & 32) ? r[0] : r[1]);
}
#define WAITV(n) asm volatile("s_waitcnt vmcnt(" #n ")" ::: "memory")
#define WAITL0() asm volatile("s_waitcnt lgkmcnt(0)" ::: "memory")
#define BAR() do { asm volatile("" ::: "memory"); __builtin_amdgcn_s_barrier(); asm volatile("" ::: "memory"); } while (0)
#define SCHED() __builtin_amdgcn_sched_barrier(0)

#define SG(m, n) __builtin_amdgcn_sched_group_barrier(m, n, 0)
DI float fmx(float a, float b) { return __builtin_elementwise_maximum(a, b); }
typedef int i32x4 __attribute__((ext_vector_type(4)));
#define PIN(x) asm volatile("" : "+v"(x))
DI void raise(char* p, u64 tag) { (void)__hip_atomic_exchange((gu64*)p, tag, RLX); }
template <bool TWO_BARRIERS>
DI u64 poll(const char* p, u64 all, u64 need, u64 tag) {
    u64 v, zero, sv, left; unsigned cnt = SPIN_MAX;
#define POLL_LOOP ".Lpw%=:\n\ts_waitcnt vmcnt(0)\n\tv_cmp_ne_u64 %4, %6, %0\n\ts_and_b64 vcc, %4, %8\n\ts_cbranch_scc0 .Lpd%=\n\ts_sub_u32 %3, %3, 1\n\ts_cbranch_scc1 .Lpd%=\n\t" \
                  "global_atomic_add_x2 %0, %5, %1, off sc0\n\ts_branch .Lpw%=\n.Lpd%=:\n\ts_mov_b64 exec, %2"
#define POLL_OPS : "=&v"(v), "=&v"(zero), "=&s"(sv), "+s"(cnt), "=&s"(left) : "v"(p), "s"(tag), "s"(all), "s"(need) : "vcc", "scc", "memory"
    if (TWO_BARRIERS)
        asm volatile("s_mov_b64 %2, exec\n\ts_mov_b64 exec, %7\n\tv_mov_b64 %1, 0\n\tglobal_atomic_add_x2 %0, %5, %1, off sc0\n\ts_barrier\n\ts_barrier\n" POLL_LOOP POLL_OPS);
    else
        asm volatile("s_mov_b64 %2, exec\n\ts_mov_b64 exec, %7\n\tv_mov_b64 %1, 0\n\tglobal_atomic_add_x2 %0, %5, %1, off sc0\n" POLL_LOOP POLL_OPS);
    return left;
}

extern "C" __global__ void __launch_bounds__(512, 2)
fused(const char* xa, const char* wa, const char* woa, const char* xb, const char* wb, const char* wob, char* out, char* ws) {
    __shared__ __attribute__((aligned(16))) char smem[LDS_BYTES];
    STAMP_DECL;
    STAMP(0);
    const int b = blockIdx.x, xcd = b & 7, idx = b >> 3;
    const int br = xcd & 1, jj = idx >> 3, ty = idx & 7, j = (xcd >> 1) * 4 + jj;
    unsigned d0 = threadIdx.x * 128;
    {
        u64 pc;
        asm volatile("s_cmp_lg_u32 %2, 7\n\ts_cbranch_scc1 .Lskip%=\n\tv_cmp_gt_u32 vcc, fused_end-.Lhere%=, %1\n\ts_cbranch_vccz .Lskip%=\n\ts_mov_b64 exec, vcc\n\t"
                     "s_getpc_b64 %0\n.Lhere%=:\n\tglobal_load_dword %1, %1, %0\n\ts_mov_b64 exec, -1\n.Lskip%=:"
                     : "=&s"(pc), "+v"(d0) : "s"(ty) : "vcc", "scc", "memory");
    }
    const int wave = __builtin_amdgcn_readfirstlane(threadIdx.x >> 6), lane = threadIdx.x & 63;
    const int r3 = lane >> 3, u = lane & 7;
    const int lbase = (int)(unsigned)(size_t)(LAS char*)smem;
    int vx0 = r3 * 896 + ((u ^ (r3 >> 1)) << 4), vx1 = vx0 ^ 64;
    int vw = r3 * 2688 + ((u ^ (((r3 >> 1) & 1) << 2)) << 4);
    const int rot = ROT == 2 ? ((ty + j) & 1) * 4 : 0;
    const int sx = j * (128 * 896), sw = ty * 128;
    int e0 = sx + wave * 14336 + rot * 128, e1 = sw + (rot * 64 + wave * 8) * 2688, e2 = lbase + RING + wave * 2048, e3 = lbase + RING + 16384 + wave * 1024;
    asm volatile("" : "+s"(e0), "+s"(e1), "+s"(e2), "+s"(e3)); asm volatile("" : "+v"(vx0), "+v"(vx1), "+v"(vw));
    SCHED();
    const u32x4 rx = srd(br ? xb : xa, XB), rw = srd(br ? wb : wa, WB);
    if (ty < 7) {
        dma16(rx, e2, vx0, e0);
        dma16(rw, e3, vw, e1);
        dma16(rx, e2 + 1024, vx1, e0 + 7168);
        dma16(rw, e3 + 8192, vw, e1 + 896);
    } else {
        {
            const int ln = jj * 392 + (threadIdx.x < 392 ? threadIdx.x : 0), row = (ln * 9363) >> 16;
            const int vl = row * 2688 + 1792 + (ln - row * 7) * 128;
            asm volatile("global_load_dword %0, %1, %2" : "+v"(d0) : "v"(vl), "s"(br ? wb : wa) : "memory");
            asm volatile("global_load_dword %0, %1, %2" : "+v"(d0) : "v"(vl + 224 * 2688), "s"(br ? wb : wa) : "memory");
        }
        const int wl = (jj * 784 + (threadIdx.x < 392 ? threadIdx.x : 0)) * 128;
        asm volatile("global_load_dword %0, %1, %2" : "+v"(d0) : "v"(wl), "s"(br ? wob : woa) : "memory");
        asm volatile("global_load_dword %0, %1, %2" : "+v"(d0) : "v"(wl + 392 * 128), "s"(br ? wob : woa) : "memory");
        asm volatile("s_waitcnt vmcnt(0)" : "+v"(d0) :: "memory");
    }
    u64 tag = *(const __attribute__((address_space(4))) u64*)ws;
    auto make_tag = [&]() { asm volatile("" : "+s"(tag)); tag = tag * 0x9E3779B97F4A7C15ull + 0x7F4A7C159E3779B9ull; asm volatile("" : "+s"(tag)); };
    const int w4 = wave & 3;
    const bool aux = wave >= 4;
    const int hh = lane >> 5, t = lane & 31, blk = (lane >> 4) & 1, q = (lane >> 2) & 3, p = lane & 3;
    const int T = 4 * j + w4;
    const bool local = br == 0 && ty < 7;
    typedef int i32x8 __attribute__((ext_vector_type(8)));
    typedef int i32x16 __attribute__((ext_vector_type(16)));
    const __attribute__((address_space(4))) char* te = (const __attribute__((address_space(4))) char*)g_tab.e[(min(j, 2) * 2 + br) * 32 + ty * 4 + w4];
    i32x8 tb8 = *(const __attribute__((address_space(4))) i32x8*)(te + 64);
    int h, c0;
    auto use_tab = [&]() { asm volatile("" : "+s"(tb8)); h = tb8[E_H - 16]; c0 = tb8[E_C0 - 16]; };
    const __amdgpu_buffer_rsrc_t rxs = rsrc(ws + WS_X, X_BYTES);
    const int wb0 = (8 * hh + q) * 128 + ((blk * 2 + (p & 1)) << 4) + (p >> 1) * 8 + ((q >> 1) << 6), wb1 = wb0 ^ 64;

    const unsigned NEGB = 0xF149F2CAu;
    f32x16 cd, co;
    u32x4 pm[2];
    int nd, no, lm;
    auto mask_bits = [&]() {
        const unsigned lo = (2u << t) - 1u;
        nd = (int)(~lo >> (4 * hh)); lm = (int)(lo >> (4 * hh)); no = T < 8 ? -1 : lm;
    };
    auto mask_reg = [&](int r) {
        const int bit = (r & 3) + 8 * (r >> 2);
        float a = __builtin_bit_cast(float, __builtin_amdgcn_sbfe(nd, bit, 1) & NEGB), c = __builtin_bit_cast(float, __builtin_amdgcn_sbfe(no, bit, 1) & NEGB);
        PIN(a); PIN(c); cd[r] = a; co[r] = c;
        if (r & 1) { unsigned m = __builtin_amdgcn_perm(__builtin_amdgcn_sbfe(lm, bit, 1), __builtin_amdgcn_sbfe(lm, bit - 1, 1), 0x05040100u); PIN(m); pm[r >> 3][(r >> 1) & 3] = m; }
    };
    auto koff = [&](int cf, int s) { return (2 * s + hh) * 512 + ((((t >> 2) ^ ((cf + 2 * s + hh) & 3)) << 2) + (t & 3)) * 16; };
    auto voff = [&](int hd, int cf, int tt) {
        const int pc8 = (4 * blk + 2 * (p & 1) + (p >> 1) + (hd & 1)) & 7, cc = pc8 >> 1;
        return cc * 512 + ((((2 * tt + hh) ^ ((cf + cc) & 3)) << 2) + q) * 16 + (pc8 & 1) * 8;
    };
    auto qfix = [&](int hd, u32x4& q0, u32x4& q1) {
        const bool z0 = (hd & 1) && !hh, z1 = !(hd & 1) && hh;
        q0.x = z0 ? 0u : q0.x; q0.y = z0 ? 0u : q0.y; q1.z = z1 ? 0u : q1.z; q1.w = z1 ? 0u : q1.w;
    };
    auto ex = [&](f32x16& s) {
#pragma unroll
        for (int r = 0; r < 16; ++r) s[r] = __builtin_amdgcn_exp2f(s[r]);
    };
    auto rowmax = [&](const f32x16& s) {
        float m = fmx(s[0], s[1]);
#pragma unroll
        for (int r = 2; r < 16; r += 2) m = fmx(fmx(m, s[r]), s[r + 1]);
        const unsigned mu = __builtin_bit_cast(unsigned, m);
        const auto r = __builtin_amdgcn_permlane32_swap(mu, mu, false, false);
        const unsigned r0 = r[0], r1 = r[1];
        return fmx(__builtin_bit_cast(float, r0), __builtin_bit_cast(float, r1));
    };
    const __amdgpu_buffer_rsrc_t ros = rsrc(ws + WS_O, O_BYTES);
    auto put_o = [&](f32x16 o, float l, int rec) {
        o = o * __builtin_amdgcn_rcpf(l);
        store_dev(__builtin_bit_cast(u32x4, pack8(o, 0)), ros, lane * 16, rec);
        store_dev(__builtin_bit_cast(u32x4, pack8(o, 1)), ros, lane * 16 + 1024, rec);
    };
    auto ones = [&](int back, bool two) {
        const int bb = back + (two ? hh : 0);
        if (two || !hh) *(u32x2*)(smem + LR + w4 * 4096 + (2 - bb) * 16384 + 2048 + ((h & 1) ? 0 : 1544) + t * 16) = (u32x2){j >= bb ? 0x3F80u : 0u, 0u};
    };
    LAS char* wd = (LAS char*)(smem + LW + 7 * 512 + 8);

    #define wk ((LAS char*)(smem + LR + 32768 + ((h & 1) ? 0 : 1544)))
#define FTAG ((tag | 1) & 0xBFFFBFFFBFFFBFFFull)
    auto arrive = [&](int n) { if (lane == 0) *(volatile LAS u64*)(wk + (n + w4) * 16) = FTAG; };
    auto await4 = [&](int n) {
#pragma unroll 1
        for (int i = 0; i < SPIN_MAX * 4; ++i) if (__all(*(volatile LAS u64*)(wk + (n + p) * 16) == FTAG)) break;
        asm volatile("" ::: "memory");
    };
    bf16x8 wo[28];
    auto read_wo = [&](int base) {
        int a = base + ((w4 >> 1) ? wb1 : wb0) + hh * (20 * 128);
        PIN(a);
#pragma unroll
        for (int ks = 0; ks < 28; ++ks) {
            const int i = ks % 7, row = (ks / 7) * 112 + (i < 6 ? (i / 3) * 56 + (i % 3) * 8 : 24);
            wo[ks] = tr_frag(smem + a + row * 128, smem + a + (row + (i < 6 ? 4 : 56)) * 128);
        }
    };
    const int vwo = r3 * 896 + ((u ^ (((r3 >> 1) & 1) << 2)) << 4);
    const u32x4 rwo = srd(br ? wob : woa, OB);

    if (__builtin_expect(aux, 1)) {
        const bool x7 = ty == 7;
        i32x16 tb = *(const __attribute__((address_space(4))) i32x16*)te;
        if (ty < 7) {
            int ch = rot, sg = 0;
#pragma unroll 1
            for (int c = 1; c < 7; ++c) {
                ++ch; if (ch == 7) ch = 0;
                ++sg; if (sg == 3) sg = 0;
                const int st = lbase + RING + sg * STAGE;
#pragma unroll
                for (int i = 0; i < 4; ++i) dma16(rx, st + w4 * 4096 + i * 1024, (i & 1) ? vx1 : vx0, sx + (w4 * 4 + i) * 7168 + ch * 128);
#pragma unroll
                for (int cb = 0; cb < 2; ++cb)
#pragma unroll
                    for (int i = 0; i < 2; ++i) dma16(rw, st + 16384 + cb * 8192 + w4 * 2048 + i * 1024, vw, sw + (ch * 64 + w4 * 16 + i * 8) * 2688 + cb * 896);
                WAITV(8); BAR();
            }
#pragma unroll 1
            for (int c = 0; c < 7; ++c) {
                ++ch; if (ch == 7) ch = 0;
#pragma unroll
                for (int i = 0; i < 2; ++i) dma16(rw, lbase + LA + c * 8192 + w4 * 2048 + i * 1024, vw, sw + (ch * 64 + w4 * 16 + i * 8) * 2688 + 1792);
                if (c == 3) { WAITV(8); BAR(); }
            }
            BAR();
        }
        make_tag(); use_tab();
        asm volatile("" : "+s"(tb));
        const int jo = j * (4 * XT), sl = tb8[E_SL - 16];
        int ra0 = tb[E_RA0] + jo, ra1 = tb[E_RA1] + jo, ra2 = tb[E_RA2] + jo, rb0 = tb[E_RB0] + jo, rb1 = tb[E_RB1] + jo, rq = tb[E_RQ] + jo, lh = lbase + LR + w4 * 4096;
        asm volatile("" : "+s"(ra0), "+s"(ra1), "+s"(ra2), "+s"(rb0), "+s"(rb1), "+s"(rq), "+s"(lh));
        const int lb0 = lbase + tb[E_LB0], lb1 = lbase + tb[E_LB1];
        const int hd[2] = {tb[E_HD0], tb[E_HD1]}, cf[2] = {tb[E_CF0], tb[E_CF1]}, po[2] = {tb8[E_PO0 - 16], tb8[E_PO1 - 16]};
        const u64 mb = (u64)(unsigned)tb[E_MB] << 32, mall = mb | (unsigned)tb[E_MA];
        const char* f1 = ws + (WS_F1 + j * 256 + tb[E_PG0]) + (hh * 4096 - (r3 & 3) * 256 + (lane & 4) * tb[E_PGD] + p * 8);
        int l16 = lane * 16;
        PIN(f1); PIN(l16);
        int lz = lane; PIN(lz);
        const int g4 = lz >> 4, c16 = lz & 15;
        int fo[2], ko[2], qo[2], vo[2][2];
        f32x4 md, mo;
        const bool pt = x7 || g4 == sl;
#pragma unroll
        for (int e = 0; e < 2; ++e) fo[e] = g4 * 512 + (((c16 >> 1) ^ ((cf[e] + g4) & 3)) << 6) + (c16 & 1) * 32;
        int qs = pt ? fo[1] : OOB; PIN(qs);
        if (!local) {
#pragma unroll
            for (int e = 0; e < 2; ++e) { ko[e] = LW + 40960 - w4 * 8192 + po[e] + fo[e]; qo[e] = LW + w4 * 4096 + po[e] + fo[e]; PIN(ko[e]); PIN(qo[e]); }
#pragma unroll
            for (int r = 0; r < 4; ++r) {
                const int d = c16 - 4 * g4 - r;
                md[r] = (unsigned)d < 8u ? 0.f : -1.0e30f; mo[r] = (d < -8 && T) ? 0.f : -1.0e30f;
            }
            PIN(md); PIN(mo);
        }
        if (ty < 7) { WAITV(6); BAR(); }
        if (wave == 4 && lane < 2) *(volatile LAS u64*)(wd + lane * 16) = 0;
        asm volatile("s_waitcnt vmcnt(0) lgkmcnt(0)" ::: "memory"); BAR();
        STAMP(4);
        const u32x4 rxx = srd(ws + WS_X, X_BYTES);
        auto tell = [&](int n) { if (lane == 0) *(volatile LAS u64*)(n < 2 ? wd + n * 16 : wk + 64) = FTAG; };
        auto hear = [&](int n) {
#pragma unroll 1
            for (int i = 0; i < SPIN_MAX * 4; ++i) { if (__all(*(volatile LAS u64*)(n < 2 ? wd + n * 16 : wk + 64) == FTAG)) break; if (HSL) __builtin_amdgcn_s_sleep(HSL); }
            asm volatile("" ::: "memory");
        };
        auto fetch_b = [&](int v) {
            const int go = v * (56 * 512), lo = v * 4096;
            if (x7) { dma32_dev(rxx, lb0 + lo, l16, rb0 + go); dma32_dev(rxx, lb1 + lo, l16, rb1 + go); }
            else {
                if (w4 == 0) dma64_dev(rxx, lb1 + lo, l16, rb1 + go);
                if (sl != 8 && lane < 32) { dma16_dev(rxx, lb0 + lo, l16, rb0 + go); if (w4) dma16_dev(rxx, lb1 + lo, l16, rb1 + go); }
            }
        };
        auto fetch_a = [&](int v) {
            const int go = v * (56 * 512), lo = v * 2048;
            dma32_dev(rxx, lh + 32768 + lo, l16, ra0 + go);
            dma32_dev(rxx, lh + 16384 + lo, l16, ra1 + go);
            dma32_dev(rxx, lh + lo, l16, ra2 + go);
        };
        STAMP(5);
        unsigned left = 1;
        if (!local) { if (wave == 4) { left = __builtin_amdgcn_readfirstlane((unsigned)poll<false>(f1, mall, mb, tag)); tell(0); if (!left) tell(1); } else hear(0); }
        STAMP(2);
        u32x4 qp[2];
        asm volatile("buffer_load_dwordx4 %0, %2, %3, %4 offen sc1\n\tbuffer_load_dwordx4 %1, %2, %3, %4 offen offset:16 sc1"
                     : "=&v"(qp[0]), "=&v"(qp[1]) : "v"(qs), "s"(rxx), "s"(rq) : "memory");
        if (!local) fetch_b(0);
        if (wave == 4) { if (left) { poll<false>(f1, mall ^ mb, mall ^ mb, tag); tell(1); } } else hear(1);
        STAMP(6);
        dma32_dev(rxx, lh + 2048, l16, ra0 - 56 * 512);
        fetch_a(0);
        WAITV(8);
        PIN(qp[0]); PIN(qp[1]);
        STAMP(3);
        bf16x8 pe[2][2];
        float ls[2][2];
        if (!local) {
            f32x4 sd[2][2], so[2][2];
#pragma unroll
            for (int e = 0; e < 2; ++e)
#pragma unroll
                for (int y = 0; y < 2; ++y) {
                    const char* kb = smem + ko[e] + y * 16;
                    u32x4 qv = *(const u32x4*)(smem + qo[e] + y * 16);
                    const bool a = pt && (e || x7), z0 = (hd[e] & 1) && g4 == 0, z1 = !(hd[e] & 1) && g4 == 3;
                    qv = (u32x4){a ? qp[y].x : qv.x, a ? qp[y].y : qv.y, a ? qp[y].z : qv.z, a ? qp[y].w : qv.w};
                    qv = (u32x4){z0 ? 0u : qv.x, z0 ? 0u : qv.y, z1 ? 0u : qv.z, z1 ? 0u : qv.w};
                    sd[e][y] = MFMA16(*(const bf16x8*)kb, __builtin_bit_cast(bf16x8, qv), md);
                    so[e][y] = MFMA16(*(const bf16x8*)(kb + 8192), __builtin_bit_cast(bf16x8, qv), mo);
                }
            const bf16x8 one = __builtin_bit_cast(bf16x8, (u32x4){0x3F803F80u, 0x3F803F80u, 0x3F803F80u, 0x3F803F80u});
            float m[4];
#pragma unroll
            for (int i = 0; i < 4; ++i) {
                const f32x4 a = sd[i >> 1][i & 1], c = so[i >> 1][i & 1];
                m[i] = fmx(fmx(fmx(fmx(a[0], a[1]), a[2]), fmx(fmx(a[3], c[0]), c[1])), fmx(c[2], c[3]));
            }
#pragma unroll
            for (int i = 0; i < 4; ++i) {
                const unsigned mu = __builtin_bit_cast(unsigned, m[i]);
                const auto r = __builtin_amdgcn_permlane32_swap(mu, mu, false, false);
                const unsigned r0 = r[0], r1 = r[1];
                m[i] = fmx(__builtin_bit_cast(float, r0), __builtin_bit_cast(float, r1));
            }
#pragma unroll
            for (int i = 0; i < 4; ++i) {
                const unsigned mu = __builtin_bit_cast(unsigned, m[i]);
                const auto r = __builtin_amdgcn_permlane16_swap(mu, mu, false, false);
                const unsigned r0 = r[0], r1 = r[1];
                m[i] = fmx(__builtin_bit_cast(float, r0), __builtin_bit_cast(float, r1));
            }
            SCHED();
            f32x4 sm[4];
#pragma unroll
            for (int i = 0; i < 4; ++i) {
                const f32x4 a = sd[i >> 1][i & 1], c = so[i >> 1][i & 1];
                f32x8 x = {a[0], a[1], a[2], a[3], c[0], c[1], c[2], c[3]};
                x -= m[i];
#pragma unroll
                for (int r = 0; r < 8; ++r) x[r] = __builtin_amdgcn_exp2f(x[r]);
                pe[i >> 1][i & 1] = __builtin_convertvector(x, bf16x8);
                sm[i] = MFMA16(one, pe[i >> 1][i & 1], (f32x4){});
                SCHED();
            }
#pragma unroll
            for (int i = 0; i < 4; ++i) ls[i >> 1][i & 1] = __builtin_amdgcn_rcpf(sm[i][0]);
#pragma unroll
            for (int i = 0; i < 4; ++i) { PIN(pe[i >> 1][i & 1]); PIN(ls[i >> 1][i & 1]); }
        }
        STAMP(10);
        asm volatile("s_waitcnt vmcnt(0) lgkmcnt(0)" ::: "memory");
        STAMP(7);
        __builtin_amdgcn_s_setprio(2);
        if (wave == 4) { poll<true>(f1 + 8192, mall, mall, tag); tell(2); }
        else {
            BAR(); BAR();
            dma_rows<14>(rwo, lbase + LA + w4 * 1024, vwo, w4 * 7168 + (ty & 6) * 128);
            hear(2);
        }
        STAMP(14);
        if (!local) fetch_b(1);
        fetch_a(1);
        if (!YEARLY) dma_rows<YB0>(rwo, lbase + LA + w4 * 1024, vwo, w4 * 7168 + (ty & 6) * 128);
        if (!local) {
            PIN(lz);
#pragma unroll
            for (int e = 0; e < 2; ++e)
#pragma unroll
                for (int s = 0; s < 2; ++s) {
                    const int pc8 = (2 * (lz & 3) + s + (hd[e] & 1)) & 7, cc = pc8 >> 1;
                    vo[e][s] = LW + 40960 - w4 * 8192 + 4096 + po[e] + cc * 512 + (((lz >> 3) ^ ((cf[e] + cc) & 3)) << 6) + (q & 1) * 32 + (pc8 & 1) * 8; PIN(vo[e][s]);
                }
        }
        if (!local) {
            WAITV(6);
            u32x4 ov[2][2];
#pragma unroll
            for (int e = 0; e < 2; ++e)
#pragma unroll
                for (int y = 0; y < 2; ++y) {
                    const char* v0 = smem + vo[e][0] + y * 16; const char* v1 = smem + vo[e][1] + y * 16;
                    const f32x4 a = MFMA16(tr_frag(v0, v0 + 8192), pe[e][y], (f32x4){}), c = MFMA16(tr_frag(v1, v1 + 8192), pe[e][y], (f32x4){});
                    f32x8 x = {a[0], a[1], a[2], a[3], c[0], c[1], c[2], c[3]};
                    x *= ls[e][y];
                    ov[e][y] = __builtin_bit_cast(u32x4, __builtin_convertvector(x, bf16x8));
                }
#pragma unroll
            for (int e = 0; e < 2; ++e)
#pragma unroll
                for (int y = 0; y < 2; ++y) store_dev(ov[e][y], ros, g4 * 512 + c16 * 16 + y * 256, ((64 + T) * 16 + hd[e]) * 2048);
            arrive(8);
            WAITV(4);
        } else WAITV(0);
        STAMP(11);
        ones(0, true); ones(2, false); WAITL0(); arrive(0);
        STAMP(9);
        __builtin_amdgcn_s_setprio(0);
        if (wave == 4) dma_rows<14>(rwo, lbase + LA, vwo, (ty & 6) * 128);
        if (!local) {
            if (wave == 4) WAITV(14); else WAITV(0);
            if (lane < 2) raise(ws + WS_F2 + (64 + T) * 128 + (lane ? hd[1] : hd[0]) * 8, tag);
            await4(8);
        }
        dma_rows<6>(rwo, lbase + LW + w4 * 1024, vwo, ty < 6 ? w4 * 7168 + (ty | 1) * 128 : OOB);
        WAITV(6); arrive(16);
        if (ty < 6) dma_rows<8>(rwo, lbase + LW + (w4 + 24) * 1024, vwo, (w4 + 24) * 7168 + (ty | 1) * 128);
        if (b == 0) {
            poll<false>(ws + WS_F1 + w4 * 2048 + lane * 32, ~0ull, ~0ull, tag);
            arrive(12);
            if (wave == 4) { await4(12); if (lane == 0) __hip_atomic_store((gu64*)ws, tag, RLX); }
        }
        WAITV(0);
    } else {
        mask_bits();
        if (ty < 7) {
            int xo[2][4], wo_[2][2];
#pragma unroll
            for (int g = 0; g < 2; ++g) {
#pragma unroll
                for (int ks = 0; ks < 4; ++ks) { xo[g][ks] = RING + g * 65536 + (w4 * 32 + t) * 128 + (((2 * ks + hh) ^ ((t >> 1) & 7)) << 4); PIN(xo[g][ks]); }
                wo_[g][0] = RING + g * 65536 + wb0; wo_[g][1] = RING + g * 65536 + wb1; PIN(wo_[g][0]); PIN(wo_[g][1]);
            }
            int es[2], el[2][2], g0;
            unsigned pq, pk;
            char* f1;
            auto put = [&](const f32x16& a0, const f32x16& a1, int sec, unsigned pub, u32x4 (&d)[2][2]) {
#pragma unroll
                for (int e = 0; e < 2; ++e)
#pragma unroll
                    for (int s = 0; s < 2; ++s) {
                        d[e][s] = __builtin_bit_cast(u32x4, pack8(e ? a1 : a0, s));
                        if ((pub >> (2 * e + s)) & 1) store_dev(d[e][s], rxs, es[s] + e * 2048, g0 + sec * (56 * 512));
                    }
            };
            auto keep = [&](int lo, const u32x4 (&d)[2][2]) {
#pragma unroll
                for (int e = 0; e < 2; ++e)
#pragma unroll
                    for (int s = 0; s < 2; ++s) if (el[e][s] >= 0) *(u32x4*)(smem + el[e][s] + lo) = d[e][s];
            };
            f32x16 acc[4];
            bf16x8 xf[28], wf[2][4];
            const f32x16 zero = {};
            WAITV(0); BAR();
            STAMP(1); CST(20);
            auto rd = [&](int n) {
                const int sg = (n >> 2) % 3, ks = n & 3;
                const char* st = smem + (sg & 1) * STAGE;
#pragma unroll
                for (int i = 3; i >= 0; --i) {
                    const char* a = st + wo_[sg >> 1][i & 1] + 16384 + (i >> 1) * 8192 + ks * 2048;
                    wf[n & 1][i] = tr_frag(a, a + 512);
                    if (i == 3) xf[n] = *(const bf16x8*)(st + xo[sg >> 1][ks]);
                }
            };
            rd(0);
            SCHED();
#pragma unroll
            for (int n = 0; n < 28; ++n) {
                if (n == 11) {
                    make_tag(); use_tab();
#pragma unroll
            for (int s = 0; s < 2; ++s) {
                const int sw16 = ((((t >> 2) ^ (2 * s + hh)) << 2) + (t & 3)) * 16;
                es[s] = (2 * s + hh) * 512 + sw16; PIN(es[s]);
#pragma unroll
                for (int e = 0; e < 2; ++e) {
                    const int ps = tb8[E_PS - 16] + 4 * e + 2 * s + hh;
                    el[e][s] = (unsigned)ps < 8u ? ps * 512 + sw16 : -1; PIN(el[e][s]);
                }
            }
            pq = tb8[E_PQ - 16]; pk = tb8[E_PK - 16];
            g0 = (br * 64 + T) * XT + ty * 4096;
            f1 = ws + WS_F1 + (br * 16 + j) * 256 + ty * 32 + w4 * 8;
            asm volatile("" : "+s"(g0), "+s"(f1));
                    SCHED();
                }
                if ((n & 3) == 3) { WAITL0(); BAR(); SCHED(); }
                if (n < 27) rd(n + 1);
#pragma unroll
                for (int i = 3; i >= 0; --i) acc[i] = MFMA(wf[n & 1][i], xf[n], n ? acc[i] : zero);
                if (n < 27) { SG(0x8, 1); SG(0x100, 3); SG(0x8, 1); SG(0x100, 3); SG(0x8, 1); SG(0x100, 3); SG(0x8, 1); }
                SCHED();
            }
            STAMP(2);
            const float qs = 0.18898223650461361f * 1.4426950408889634f;
            {
                u32x4 dk[2][2], dq[2][2];
                put(acc[2], acc[3], 1, pk, dk);
                put(acc[0] * qs, acc[1] * qs, 0, pq, dq);
                if (br) { keep(LW + 40960 - w4 * 8192, dk); keep(LW + w4 * 4096, dq); }
            }
            STAMP(3);
            BAR();
            bf16x8 wv[8];
            u32x4 dv[2][2];
            auto rv = [&](int n) { const char* a = smem + LA + (n % 28) * 2048 + (n >= 28 ? wb1 : wb0); wv[n & 7] = tr_frag(a, a + 512); };
            auto putv = [&](int e, int s) {
                dv[e][s] = __builtin_bit_cast(u32x4, pack8(acc[2 + e], s));
                store_dev(dv[e][s], rxs, es[s] + e * 2048, ((pk >> (2 * e + s)) & 1) ? g0 + 112 * 512 : OOB);
            };
#pragma unroll
            for (int n = 0; n < 6; ++n) rv(n);
            PIN(nd); PIN(no); PIN(lm);
            SCHED();
#pragma unroll
            for (int n = 0; n < 56; ++n) {
                if (n == BV1) { BAR(); SCHED(); }
                if (n == RAISE_B || n == RAISE_A) { if ((n == RAISE_B) == (br != 0)) { WAITV(0); if (lane == 0) raise(f1, tag); STAMP(9); } SCHED(); }
                acc[2 + n / 28] = MFMA(wv[n & 7], xf[n % 28], n % 28 ? acc[2 + n / 28] : zero);
                if (n + 6 < 56) rv(n + 6);
                if (n >= 36 && n < 52) mask_reg(n - 36);
                SG(0x8, 1); SG(0x100, 2); SG(0x2, 8);
                SCHED();
                if (n == 30 || n == 32) { putv(0, (n - 30) >> 1); SCHED(); }
            }
            STAMP(4); CST(21);
            putv(1, 0); putv(1, 1);
            if (br) keep(LW + 45056 - w4 * 8192, dv);
            STAMP(5);
            WAITV(0);
            if (lane == 0) raise(f1 + 8192, tag);
        } else {
            make_tag(); use_tab();
            BAR();
            if (threadIdx.x == 0) raise(ws + WS_F1 + (br * 16 + j) * 256 + 7 * 32, tag);
#pragma unroll
            for (int r = 0; r < 16; ++r) mask_reg(r);
        }
        const char* tl = smem + LR + w4 * 4096;
        int fo[2] = {koff(c0, 0), koff(c0, 1)}, vo[2] = {voff(h, c0, 0) + 2048, voff(h, c0, 1) + 2048};
        PIN(fo[0]); PIN(fo[1]); PIN(vo[0]); PIN(vo[1]);
        STAMP(6);
        WAITL0(); BAR();
        STAMP(7);
        __builtin_amdgcn_s_setprio(1);
        u32x4 qf[2];
        qf[0] = *(const u32x4*)(tl + 2048 + fo[0]); qf[1] = *(const u32x4*)(tl + 2048 + fo[1]);
        auto kfrag = [&](int n, int s) { return *(const bf16x8*)(tl + (8 - n) * 4096 + fo[s]); };
        auto vfrag = [&](int n, int s) { const char* vb = tl + (8 - n) * 4096 + s * 256; return tr_frag(vb + vo[0], vb + vo[1]); };
        bf16x8 k0 = kfrag(0, 0), k1 = kfrag(0, 1), k2 = kfrag(8, 0), k3 = kfrag(8, 1);
        WAITL0(); BAR();
        CST(19); STAMP(10);
        qfix(h, qf[0], qf[1]);
        const bf16x8 q0 = __builtin_bit_cast(bf16x8, qf[0]), q1 = __builtin_bit_cast(bf16x8, qf[1]);
        auto qk = [&](int n, const f32x16& c) { f32x16 r = MFMA(kfrag(n, 0), q0, c); return MFMA(kfrag(n, 1), q1, r); };
        f32x16 cm, sa, sb;
        bf16x8 pp[8][2];
        cd = MFMA(k0, q0, cd); co = MFMA(k2, q0, co); cd = MFMA(k1, q1, cd); co = MFMA(k3, q1, co);
        k0 = kfrag(1, 0); k1 = kfrag(1, 1);
#pragma unroll
        for (int r = 0; r < 16; ++r) cd[r] = fmx(cd[r], co[r]);
        {
            const float m = -rowmax(cd);
#pragma unroll
            for (int r = 0; r < 16; ++r) cm[r] = m;
        }
        cd += cm;
        sa = cd;
        SCHED();
#define STG(n, cur, nxt) do { \
            if (n < 7) { nxt = MFMA(k0, q0, cm); nxt = MFMA(k1, q1, nxt); } \
            if (n < 6) { k0 = kfrag(n + 2, 0); k1 = kfrag(n + 2, 1); } \
            ex(cur); pp[n][0] = pack8(cur, 0); pp[n][1] = pack8(cur, 1); \
            if (n < 7) SG(0x8, 1); SG(0x400, 4); if (n < 7) SG(0x8, 1); if (n < 6) SG(0x100, 2); SG(0x400, 4); SG(0x2, 2); SG(0x400, 4); SG(0x2, 2); SG(0x400, 4); SG(0x2, 4); SCHED(); } while (0)
        STG(0, sa, sb); STG(1, sb, sa); STG(2, sa, sb); STG(3, sb, sa); STG(4, sa, sb); STG(5, sb, sa); STG(6, sa, sb); STG(7, sb, sa);
        bf16x8 pz[2];
#pragma unroll
        for (int s = 0; s < 2; ++s) {
            const u32x4 e = __builtin_bit_cast(u32x4, pp[0][s]);
            pp[0][s] = __builtin_bit_cast(bf16x8, e & pm[s]); pz[s] = __builtin_bit_cast(bf16x8, e & ~pm[s]); PIN(pz[s]);
        }
#pragma unroll
        for (int n = 0; n < 8; ++n) { PIN(pp[n][0]); PIN(pp[n][1]); }
        char* f2 = ws + WS_F2 + T * 128 + h * 8;
        int orec = (T * 16 + h) * 2048;
        const char* va = tl + vo[0]; const char* vb = tl + vo[1];
        asm volatile("" : "+s"(f2), "+s"(orec));
        STAMP(8);
        await4(0);
        STAMP(14);
        f32x16 o = {};
        auto pv = [&](int n, bf16x8 p0, bf16x8 p1) { o = MFMA(vfrag(n, 0), p0, o); o = MFMA(vfrag(n, 1), p1, o); };
        {
            bf16x8 vf[18];
            auto rdv = [&](int i) { const int off = (8 - (i >> 1)) * 4096 + (i & 1) * 256; vf[i] = tr_frag(va + off, vb + off); };
#pragma unroll
            for (int i = 0; i < 5; ++i) { rdv(i); SCHED(); }
#pragma unroll
            for (int i = 0; i < 18; ++i) {
                if (PPV && i == 5) { await4(20); SCHED(); }
                o = MFMA(vf[i], i < 16 ? pp[i >> 1][i & 1] : pz[i & 1], o);
                if (i + 5 < 18) rdv(i + 5);
                SCHED();
            }
        }
        auto publish = [&]() {
            const float lf = o[12];
            const unsigned lu = __builtin_bit_cast(unsigned, lf);
            const unsigned lx = __builtin_amdgcn_permlane32_swap(lu, lu, false, false)[1];
            put_o(o, __builtin_bit_cast(float, lx), orec);
        };
        publish();
        STAMP(11); CST(22);
        float big = fmx(__builtin_fabsf(o[0]), __builtin_fabsf(o[1]));
#pragma unroll
        for (int r = 2; r < 16; r += 2) big = fmx(fmx(big, __builtin_fabsf(o[r])), __builtin_fabsf(o[r + 1]));
        if (__builtin_expect(!__all(big < 1.0e36f), 0)) {
            const int nmid = T >= 7 ? 7 : T;
            float m = 0.f;
#pragma unroll 1
            for (int i = 1; i <= nmid; ++i) m = fmx(m, rowmax(qk(i, cm)));
            cd -= m; cm -= m;
            ex(cd);
            o = (f32x16){};
            const u32x4 e0 = __builtin_bit_cast(u32x4, pack8(cd, 0)), e1 = __builtin_bit_cast(u32x4, pack8(cd, 1));
            pv(0, __builtin_bit_cast(bf16x8, e0 & pm[0]), __builtin_bit_cast(bf16x8, e1 & pm[1]));
            pv(8, __builtin_bit_cast(bf16x8, e0 & ~pm[0]), __builtin_bit_cast(bf16x8, e1 & ~pm[1]));
#pragma unroll 1
            for (int i = 1; i <= nmid; ++i) {
                f32x16 s = qk(i, cm);
                ex(s); pv(i, pack8(s, 0), pack8(s, 1));
            }
            WAITV(0);
            publish();
        }
        if (RDFIRST) { await4(16); read_wo(LA); SCHED(); }
        WAITV(0);
        if (lane == 0) raise(f2, tag);
        __builtin_amdgcn_s_setprio(0);
        STAMP(12);
        if (!RDFIRST) { await4(16); read_wo(LA); }
    }
    STAMP(13);
    const int To = br * 64 + 4 * j + 2 * (ty & 1);
    const int tr = br ? 2 * (t & 15) + (t >> 4) : t;
    const int tb = LR + 32768 + ((wave >> 2) * 2 + (w4 & 1)) * 4096;
    int l16 = lane * 16, oa = (w4 & 1) * LR + hh * 2048 + t * 16, tw = tb + tr * 128 + (((4 * (w4 >> 1) + hh) ^ (tr & 7)) << 4), tg = tb + (w4 >> 1) * 2048 + r3 * 128 + ((u ^ r3) << 4);
    int ov = r3 * 1792 + u * 16;
    int oo = ((4 * j + 2 * (ty & 1) + (w4 & 1)) * 32 + (w4 >> 1) * 16) * 1792 + br * 896 + ((ty & 6) + (wave >> 2)) * 128;
    const int os = (To * 16 + w4) * 2048, ol = lbase + w4 * 2048;
    if (F64) { tg = tb + (lane >> 2) * 128 + (((4 * (w4 >> 1) + p) ^ ((lane >> 2) & 7)) << 4); ov = (lane >> 2) * 1792 + p * 16; oo += (w4 >> 1) * (64 - 16 * 1792); }
    asm volatile("" : "+v"(l16), "+v"(oa), "+v"(tw), "+v"(tg), "+v"(ov)); asm volatile("" : "+s"(oo));
    const __amdgpu_buffer_rsrc_t rout = rsrc(out, 2048 * 1792);
    const u32x4 roo = srd(ws + WS_O, O_BYTES);
    const bool idle = aux && ty >= 6;
    if (wave == 0) poll<false>(ws + WS_F2 + To * 128 + lane * 8, 0xFFFFFFFFull, 0xFFFFFFFFull, tag);
    WAITL0(); BAR();
    STAMP(15);
    if (idle) { STAMP_FLUSH(); return; }
    auto geto = [&](int qd) { dma32_dev(roo, ol + qd * 8192, l16, os + qd * 8192); dma32_dev(roo, ol + qd * 8192 + LR, l16, os + qd * 8192 + 32768); };
    f32x16 acc = {};
    {
        bf16x8 of[2][7];
        auto rdo = [&](int qd) {
            const char* a = smem + oa + qd * 8192;
#pragma unroll
            for (int i = 0; i < 6; ++i) of[qd & 1][i] = *(const bf16x8*)(a + (i / 3) * 4096 + (i % 3) * 512);
            const u32x2 lo = *(const u32x2*)(a + 1536), hi = *(const u32x2*)(a + 4096 + 1536);
            of[qd & 1][6] = __builtin_bit_cast(bf16x8, (u32x4){lo.x, lo.y, hi.x, hi.y});
        };
        auto mmo = [&](int qd, int i0, int i1) {
#pragma unroll
            for (int i = i0; i < i1; ++i) acc = MFMA(wo[qd * 7 + i], of[qd & 1][i], acc);
            SCHED();
        };
        if (aux) read_wo(LW); else { geto(0); geto(1); geto(2); WAITV(8); }
        BAR(); rdo(0); SCHED();
        if (!aux) geto(3);
        mmo(0, 0, 3);
        WAITV(8);
        BAR(); rdo(1); SCHED();
        mmo(0, 3, 7); mmo(1, 0, 3);
        WAITV(4);
        BAR(); rdo(2); SCHED();
        mmo(1, 3, 7); mmo(2, 0, 3);
        WAITV(0);
        BAR(); rdo(3); SCHED();
        mmo(2, 3, 7); mmo(3, 0, 7);
    }
    STAMP(16);
    {
#pragma unroll
        for (int s = 0; s < 2; ++s) *(bf16x8*)(smem + (tw ^ (s << 5))) = pack8(acc, s);
        WAITL0(); if (!F64) BAR();
        const u32x4 v0 = *(const u32x4*)(smem + tg), v1 = *(const u32x4*)(smem + tg + (F64 ? 2048 : 1024));
        SCHED();
        store_dev(v0, rout, ov, oo); store_dev(v1, rout, ov, oo + (F64 ? 16 : 8) * 1792);
    }
    STAMP(17);
    STAMP_FLUSH();
}

extern "C" __device__ __attribute__((noinline, used)) void fused_end() {}

extern "C" void kernel_launch(void* const* d_in, const int* in_sizes, int n_in, void* d_out, int out_size, void* d_ws, size_t ws_size, hipStream_t stream) {
    fused<<<256, 512, 0, stream>>>((const char*)d_in[0], (const char*)d_in[1], (const char*)d_in[2], (const char*)d_in[3], (const char*)d_in[4], (const char*)d_in[5], (char*)d_out, (char*)d_ws);
}
```

```cpp
#include <hip/hip_runtime.h>
#include <hip/hip_bf16.h>

typedef __bf16 bf16x8 __attribute__((ext_vector_type(8)));
typedef short s16x4 __attribute__((ext_vector_type(4)));
typedef float f32x16 __attribute__((ext_vector_type(16)));
typedef float f32x8 __attribute__((ext_vector_type(8)));
typedef float f32x4 __attribute__((ext_vector_type(4)));
typedef unsigned u32x4 __attribute__((ext_vector_type(4)));
typedef unsigned u32x2 __attribute__((ext_vector_type(2)));
typedef unsigned long long u64;
typedef __attribute__((address_space(1))) u64 gu64;
#define LAS __attribute__((address_space(3)))
#define DI __device__ __forceinline__
#define MFMA(a, b, c) __builtin_amdgcn_mfma_f32_32x32x16_bf16((a), (b), (c), 0, 0, 0)
#define MFMA16(a, b, c) __builtin_amdgcn_mfma_f32_16x16x32_bf16((a), (b), (c), 0, 0, 0)
#define RLX __ATOMIC_RELAXED, __HIP_MEMORY_SCOPE_AGENT

constexpr int XB = 2048 * 896, WB = 448 * 2688, OB = 448 * 896;
constexpr int STAGE = 32768, RING = 65536, LA = 0, LW = 57344, LR = 114688, LX = 106496, LDS_BYTES = 163840;
constexpr int WS_F1 = 4096, WS_F2 = 65536, WS_X = 1 << 20, XT = 168 * 512, X_BYTES = 2 * 64 * XT, WS_O = 16 << 20, O_BYTES = 2 * 64 * 16 * 2048;
constexpr int OOB = 0x40000000;
#define ROT 2
#define PF0 2
#define RAISE_A 27
#define RAISE_B 4
#define BV1 10
#define KH_DELAY 8
#define OPRE 10
#define OROT 0
#define XB0 5
#define YB0 3
#define YEARLY 1
#define HSL 0
#define F64 0
#define PPV 0
#define RDFIRST 1
constexpr int SPIN_MAX = 1 << 18;

enum { E_RA0, E_RA1, E_RA2, E_RB0, E_RB1, E_RQ, E_LB0, E_LB1, E_MA, E_MB, E_PG0, E_PGD, E_HD0, E_HD1, E_CF0, E_CF1, E_PO0, E_PO1, E_SL, E_H, E_C0, E_PS, E_PQ, E_PK };
struct Tab {
    int e[192][32];
    constexpr Tab() : e() {
        for (int i = 0; i < 192; ++i) {
            const int w4 = i & 3, ty = (i >> 2) & 7, br = (i >> 5) & 1, jc = i >> 6;
            const bool x7 = ty == 7, local = br == 0 && !x7;
            const int ha = (0x4ECA7530u >> (4 * ty)) & 15, hb = (0xBFD98621u >> (4 * ty)) & 15;
            const int ca = (7 * ha) >> 1, cb = (7 * hb) >> 1, cs = ca < cb ? ca : cb;
            const int h = br ? hb : ha, c0 = br ? cb : ca;
            const int nb = x7 ? 15 : (0xF63F30Fu >> (4 * ty)) & 15;
            const int sl = (ty == 2 || ty == 5) ? 3 : 0;
            const int two = (x7 || (br && nb != 15)) ? 0xFF : 0x0F;
            const int rt = (64 + w4) * XT;
            int* v = e[i];
            v[E_HD0] = x7 ? h : ha; v[E_HD1] = x7 ? h : hb; v[E_CF0] = x7 ? c0 : ca; v[E_CF1] = x7 ? c0 : cb;
            v[E_PO0] = x7 ? 0 : (ca - cs) * 512; v[E_PO1] = x7 ? 0 : (cb - cs) * 512;
            v[E_RA0] = w4 * XT + (56 + c0) * 512; v[E_RA1] = jc >= 1 ? v[E_RA0] - 4 * XT : OOB; v[E_RA2] = jc >= 2 ? v[E_RA0] - 8 * XT : OOB;
            v[E_RB0] = rt + (56 + (x7 ? c0 : cb + sl)) * 512;
            v[E_RB1] = (jc == 0 && w4 == 0) ? OOB : ((!x7 && w4 == 0) ? rt + (56 + cs) * 512 : v[E_RB0]) - XT;
            v[E_RQ] = local ? OOB : rt + v[E_CF1] * 512;
            v[E_LB0] = LW + 40960 - w4 * 8192 + (x7 ? 0 : v[E_PO1] + sl * 512); v[E_LB1] = (!x7 && w4 == 0) ? LX : v[E_LB0] + 8192;
            v[E_MA] = two | (jc >= 1 ? two << 8 : 0) | (jc >= 2 ? two << 16 : 0);
            v[E_MB] = local ? 0 : (x7 ? 0xFF : two & 0xF0) | (jc ? (two & 0x88) << 8 : 0);
            v[E_PG0] = (x7 ? 1 + 3 * br : ty) * 32; v[E_PGD] = (x7 ? 1 : (nb == 15 ? 0 : nb - ty)) * 8;
            v[E_SL] = nb == 15 ? 8 : sl;
            v[E_H] = h; v[E_C0] = c0; v[E_PS] = 8 * ty - cs;
            v[E_PQ] = br ? (0x1189188u >> (4 * ty)) & 15 : 15; v[E_PK] = w4 != 3 ? v[E_PQ] : 15;
        }
    }
};
__device__ const Tab g_tab = Tab();

#define STAMP(i)
#define CST(i)
#define STAMP_DECL
#define STAMP_FLUSH()

DI u32x4 srd(const void* p, unsigned bytes) { const u64 a = (u64)p; return (u32x4){(unsigned)a, (unsigned)(a >> 32) & 0xffffu, bytes, 0x00020000u}; }
DI __amdgpu_buffer_rsrc_t rsrc(const void* p, int bytes) { return __builtin_amdgcn_make_buffer_rsrc((void*)p, 0, bytes, 0x00020000); }
DI void dma16(u32x4 r, int lds, int voff, int soff) {
    asm volatile("s_mov_b32 m0, %0\n\ts_nop 0\n\tbuffer_load_dwordx4 %1, %2, %3 offen lds" :: "s"(lds), "v"(voff), "s"(r), "s"(soff) : "memory");
}
DI void dma16_dev(u32x4 r, int lds, int voff, int soff) {
    asm volatile("s_mov_b32 m0, %0\n\ts_nop 0\n\tbuffer_load_dwordx4 %1, %2, %3 offen sc1 lds" :: "s"(lds), "v"(voff), "s"(r), "s"(soff) : "memory");
}
DI void dma64_dev(u32x4 r, int lds, int voff, int soff) {
    asm volatile("s_mov_b32 m0, %0\n\ts_nop 0\n\tbuffer_load_dwordx4 %1, %2, %3 offen sc1 lds\n\tbuffer_load_dwordx4 %1, %2, %3 offen offset:1024 sc1 lds\n\t"
                 "buffer_load_dwordx4 %1, %2, %3 offen offset:2048 sc1 lds\n\tbuffer_load_dwordx4 %1, %2, %3 offen offset:3072 sc1 lds" :: "s"(lds), "v"(voff), "s"(r), "s"(soff) : "memory");
}
DI void dma32_dev(u32x4 r, int lds, int voff, int soff) {
    asm volatile("s_mov_b32 m0, %0\n\ts_nop 0\n\tbuffer_load_dwordx4 %1, %2, %3 offen sc1 lds\n\tbuffer_load_dwordx4 %1, %2, %3 offen offset:1024 sc1 lds"
                 :: "s"(lds), "v"(voff), "s"(r), "s"(soff) : "memory");
}
DI void store_dev(u32x4 v, __amdgpu_buffer_rsrc_t r, int voff, int soff) {
    __builtin_amdgcn_raw_buffer_store_b128(v, r, voff, soff, 16);
    asm volatile("s_nop 1" :: "v"(v) : "memory");
}
template <int N>
DI void dma_rows(u32x4 r, int lds, int voff, int soff) {
    asm volatile("s_mov_b32 m0, %0\n\ts_nop 0\n\tbuffer_load_dwordx4 %2, %3, %1 offen lds\n.rept %4\n\ts_add_u32 m0, m0, 0x1000\n\ts_add_u32 %1, %1, 0x7000\n\tbuffer_load_dwordx4 %2, %3, %1 offen lds\n.endr"
                 : "+s"(lds), "+s"(soff) : "v"(voff), "s"(r), "i"(N - 1) : "scc", "memory");
}
DI bf16x8 tr_frag(const char* a0, const char* a1) {
    s16x4 lo = __builtin_amdgcn_ds_read_tr16_b64_v4i16((LAS s16x4*)a0);
    s16x4 hi = __builtin_amdgcn_ds_read_tr16_b64_v4i16((LAS s16x4*)a1);
    return __builtin_bit_cast(bf16x8, __builtin_shufflevector(lo, hi, 0, 1, 2, 3, 4, 5, 6, 7));
}
DI bf16x8 pack8(const f32x16& v, int s) {
    f32x8 t = {v[8 * s], v[8 * s + 1], v[8 * s + 2], v[8 * s + 3], v[8 * s + 4], v[8 * s + 5], v[8 * s + 6], v[8 * s + 7]};
    return __builtin_convertvector(t, bf16x8);
}
DI float xhalf(float v) {
    const unsigned u = __builtin_bit_cast(unsigned, v);
    const auto r = __builtin_amdgcn_permlane32_swap(u, u, false, false);
    return __builtin_bit_cast(float, (threadIdx.x & 32) ? r[0] : r[1]);
}
#define WAITV(n) asm volatile("s_waitcnt vmcnt(" #n ")" ::: "memory")
#define WAITL0() asm volatile("s_waitcnt lgkmcnt(0)" ::: "memory")
#define BAR() do { asm volatile("" ::: "memory"); __builtin_amdgcn_s_barrier(); asm volatile("" ::: "memory"); } while (0)
#define SCHED() __builtin_amdgcn_sched_barrier(0)

#define SG(m, n) __builtin_amdgcn_sched_group_barrier(m, n, 0)
DI float fmx(float a, float b) { return __builtin_elementwise_maximum(a, b); }
typedef int i32x4 __attribute__((ext_vector_type(4)));
#define PIN(x) asm volatile("" : "+v"(x))
DI void raise(char* p, u64 tag) { (void)__hip_atomic_exchange((gu64*)p, tag, RLX); }
template <bool TWO_BARRIERS>
DI u64 poll(const char* p, u64 all, u64 need, u64 tag) {
    u64 v, zero, sv, left; unsigned cnt = SPIN_MAX;
#define POLL_LOOP ".Lpw%=:\n\ts_waitcnt vmcnt(0)\n\tv_cmp_ne_u64 %4, %6, %0\n\ts_and_b64 vcc, %4, %8\n\ts_cbranch_scc0 .Lpd%=\n\ts_sub_u32 %3, %3, 1\n\ts_cbranch_scc1 .Lpd%=\n\t" \
                  "global_atomic_add_x2 %0, %5, %1, off sc0\n\ts_branch .Lpw%=\n.Lpd%=:\n\ts_mov_b64 exec, %2"
#define POLL_OPS : "=&v"(v), "=&v"(zero), "=&s"(sv), "+s"(cnt), "=&s"(left) : "v"(p), "s"(tag), "s"(all), "s"(need) : "vcc", "scc", "memory"
    if (TWO_BARRIERS)
        asm volatile("s_mov_b64 %2, exec\n\ts_mov_b64 exec, %7\n\tv_mov_b64 %1, 0\n\tglobal_atomic_add_x2 %0, %5, %1, off sc0\n\ts_barrier\n\ts_barrier\n" POLL_LOOP POLL_OPS);
    else
        asm volatile("s_mov_b64 %2, exec\n\ts_mov_b64 exec, %7\n\tv_mov_b64 %1, 0\n\tglobal_atomic_add_x2 %0, %5, %1, off sc0\n" POLL_LOOP POLL_OPS);
    return left;
}

extern "C" __global__ void __launch_bounds__(512, 2)
fused(const char* xa, const char* wa, const char* woa, const char* xb, const char* wb, const char* wob, char* out, char* ws) {
    __shared__ __attribute__((aligned(16))) char smem[LDS_BYTES];
    STAMP_DECL;
    STAMP(0);
    const int b = blockIdx.x, xcd = b & 7, idx = b >> 3;
    const int br = xcd & 1, jj = idx >> 3, ty = idx & 7, j = (xcd >> 1) * 4 + jj;
    unsigned d0 = threadIdx.x * 128;
    {
        u64 pc;
        asm volatile("s_cmp_lg_u32 %2, 7\n\ts_cbranch_scc1 .Lskip%=\n\tv_cmp_gt_u32 vcc, fused_end-.Lhere%=, %1\n\ts_cbranch_vccz .Lskip%=\n\ts_mov_b64 exec, vcc\n\t"
                     "s_getpc_b64 %0\n.Lhere%=:\n\tglobal_load_dword %1, %1, %0\n\ts_mov_b64 exec, -1\n.Lskip%=:"
                     : "=&s"(pc), "+v"(d0) : "s"(ty) : "vcc", "scc", "memory");
    }
    const int wave = __builtin_amdgcn_readfirstlane(threadIdx.x >> 6), lane = threadIdx.x & 63;
    const int r3 = lane >> 3, u = lane & 7;
    const int lbase = (int)(unsigned)(size_t)(LAS char*)smem;
    int vx0 = r3 * 896 + ((u ^ (r3 >> 1)) << 4), vx1 = vx0 ^ 64;
    int vw = r3 * 2688 + ((u ^ (((r3 >> 1) & 1) << 2)) << 4);
    const int rot = ROT == 2 ? ((ty + j) & 1) * 4 : 0;
    const int sx = j * (128 * 896), sw = ty * 128;
    int e0 = sx + wave * 14336 + rot * 128, e1 = sw + (rot * 64 + wave * 8) * 2688, e2 = lbase + RING + wave * 2048, e3 = lbase + RING + 16384 + wave * 1024;
    asm volatile("" : "+s"(e0), "+s"(e1), "+s"(e2), "+s"(e3)); asm volatile("" : "+v"(vx0), "+v"(vx1), "+v"(vw));
    SCHED();
    const u32x4 rx = srd(br ? xb : xa, XB), rw = srd(br ? wb : wa, WB);
    if (ty < 7) {
        dma16(rx, e2, vx0, e0);
        dma16(rw, e3, vw, e1);
        dma16(rx, e2 + 1024, vx1, e0 + 7168);
        dma16(rw, e3 + 8192, vw, e1 + 896);
    } else {
        {
            const int ln = jj * 392 + (threadIdx.x < 392 ? threadIdx.x : 0), row = (ln * 9363) >> 16;
            const int vl = row * 2688 + 1792 + (ln - row * 7) * 128;
            asm volatile("global_load_dword %0, %1, %2" : "+v"(d0) : "v"(vl), "s"(br ? wb : wa) : "memory");
            asm volatile("global_load_dword %0, %1, %2" : "+v"(d0) : "v"(vl + 224 * 2688), "s"(br ? wb : wa) : "memory");
        }
        const int wl = (jj * 784 + (threadIdx.x < 392 ? threadIdx.x : 0)) * 128;
        asm volatile("global_load_dword %0, %1, %2" : "+v"(d0) : "v"(wl), "s"(br ? wob : woa) : "memory");
        asm volatile("global_load_dword %0, %1, %2" : "+v"(d0) : "v"(wl + 392 * 128), "s"(br ? wob : woa) : "memory");
        asm volatile("s_waitcnt vmcnt(0)" : "+v"(d0) :: "memory");
    }
    u64 tag = *(const __attribute__((address_space(4))) u64*)ws;
    auto make_tag = [&]() { asm volatile("" : "+s"(tag)); tag = tag * 0x9E3779B97F4A7C15ull + 0x7F4A7C159E3779B9ull; asm volatile("" : "+s"(tag)); };
    const int w4 = wave & 3;
    const bool aux = wave >= 4;
    const int hh = lane >> 5, t = lane & 31, blk = (lane >> 4) & 1, q = (lane >> 2) & 3, p = lane & 3;
    const int T = 4 * j + w4;
    const bool local = br == 0 && ty < 7;
    typedef int i32x8 __attribute__((ext_vector_type(8)));
    typedef int i32x16 __attribute__((ext_vector_type(16)));
    const __attribute__((address_space(4))) char* te = (const __attribute__((address_space(4))) char*)g_tab.e[(min(j, 2) * 2 + br) * 32 + ty * 4 + w4];
    i32x8 tb8 = *(const __attribute__((address_space(4))) i32x8*)(te + 64);
    int h, c0;
    auto use_tab = [&]() { asm volatile("" : "+s"(tb8)); h = tb8[E_H - 16]; c0 = tb8[E_C0 - 16]; };
    const __amdgpu_buffer_rsrc_t rxs = rsrc(ws + WS_X, X_BYTES);
    const int wb0 = (8 * hh + q) * 128 + ((blk * 2 + (p & 1)) << 4) + (p >> 1) * 8 + ((q >> 1) << 6), wb1 = wb0 ^ 64;

    const unsigned NEGB = 0xF149F2CAu;
    f32x16 cd, co;
    u32x4 pm[2];
    int nd, no, lm;
    auto mask_bits = [&]() {
        const unsigned lo = (2u << t) - 1u;
        nd = (int)(~lo >> (4 * hh)); lm = (int)(lo >> (4 * hh)); no = T < 8 ? -1 : lm;
    };
    auto mask_reg = [&](int r) {
        const int bit = (r & 3) + 8 * (r >> 2);
        float a = __builtin_bit_cast(float, __builtin_amdgcn_sbfe(nd, bit, 1) & NEGB), c = __builtin_bit_cast(float, __builtin_amdgcn_sbfe(no, bit, 1) & NEGB);
        PIN(a); PIN(c); cd[r] = a; co[r] = c;
        if (r & 1) { unsigned m = __builtin_amdgcn_perm(__builtin_amdgcn_sbfe(lm, bit, 1), __builtin_amdgcn_sbfe(lm, bit - 1, 1), 0x05040100u); PIN(m); pm[r >> 3][(r >> 1) & 3] = m; }
    };
    auto koff = [&](int cf, int s) { return (2 * s + hh) * 512 + ((((t >> 2) ^ ((cf + 2 * s + hh) & 3)) << 2) + (t & 3)) * 16; };
    auto voff = [&](int hd, int cf, int tt) {
        const int pc8 = (4 * blk + 2 * (p & 1) + (p >> 1) + (hd & 1)) & 7, cc = pc8 >> 1;
        return cc * 512 + ((((2 * tt + hh) ^ ((cf + cc) & 3)) << 2) + q) * 16 + (pc8 & 1) * 8;
    };
    auto qfix = [&](int hd, u32x4& q0, u32x4& q1) {
        const bool z0 = (hd & 1) && !hh, z1 = !(hd & 1) && hh;
        q0.x = z0 ? 0u : q0.x; q0.y = z0 ? 0u : q0.y; q1.z = z1 ? 0u : q1.z; q1.w = z1 ? 0u : q1.w;
    };
    auto ex = [&](f32x16& s) {
#pragma unroll
        for (int r = 0; r < 16; ++r) s[r] = __builtin_amdgcn_exp2f(s[r]);
    };
    auto rowmax = [&](const f32x16& s) {
        float m = fmx(s[0], s[1]);
#pragma unroll
        for (int r = 2; r < 16; r += 2) m = fmx(fmx(m, s[r]), s[r + 1]);
        const unsigned mu = __builtin_bit_cast(unsigned, m);
        const auto r = __builtin_amdgcn_permlane32_swap(mu, mu, false, false);
        const unsigned r0 = r[0], r1 = r[1];
        return fmx(__builtin_bit_cast(float, r0), __builtin_bit_cast(float, r1));
    };
    const __amdgpu_buffer_rsrc_t ros = rsrc(ws + WS_O, O_BYTES);
    auto put_o = [&](f32x16 o, float l, int rec) {
        o = o * __builtin_amdgcn_rcpf(l);
        store_dev(__builtin_bit_cast(u32x4, pack8(o, 0)), ros, lane * 16, rec);
        store_dev(__builtin_bit_cast(u32x4, pack8(o, 1)), ros, lane * 16 + 1024, rec);
    };
    auto ones = [&](int back, bool two) {
        const int bb = back + (two ? hh : 0);
        if (two || !hh) *(u32x2*)(smem + LR + w4 * 4096 + (2 - bb) * 16384 + 2048 + ((h & 1) ? 0 : 1544) + t * 16) = (u32x2){j >= bb ? 0x3F80u : 0u, 0u};
    };
    LAS char* wd = (LAS char*)(smem + LW + 7 * 512 + 8);

    #define wk ((LAS char*)(smem + LR + 32768 + ((h & 1) ? 0 : 1544)))
#define FTAG ((tag | 1) & 0xBFFFBFFFBFFFBFFFull)
    auto arrive = [&](int n) { if (lane == 0) *(volatile LAS u64*)(wk + (n + w4) * 16) = FTAG; };
    auto await4 = [&](int n) {
#pragma unroll 1
        for (int i = 0; i < SPIN_MAX * 4; ++i) if (__all(*(volatile LAS u64*)(wk + (n + p) * 16) == FTAG)) break;
        asm volatile("" ::: "memory");
    };
    bf16x8 wo[28];
    auto read_wo = [&](int base) {
        int a = base + ((w4 >> 1) ? wb1 : wb0) + hh * (20 * 128);
        PIN(a);
#pragma unroll
        for (int ks = 0; ks < 28; ++ks) {
            const int i = ks % 7, row = (ks / 7) * 112 + (i < 6 ? (i / 3) * 56 + (i % 3) * 8 : 24);
            wo[ks] = tr_frag(smem + a + row * 128, smem + a + (row + (i < 6 ? 4 : 56)) * 128);
        }
    };
    const int vwo = r3 * 896 + ((u ^ (((r3 >> 1) & 1) << 2)) << 4);
    const u32x4 rwo = srd(br ? wob : woa, OB);

    if (__builtin_expect(aux, 1)) {
        const bool x7 = ty == 7;
        i32x16 tb = *(const __attribute__((address_space(4))) i32x16*)te;
        if (ty < 7) {
            int ch = rot, sg = 0;
#pragma unroll 1
            for (int c = 1; c < 7; ++c) {
                ++ch; if (ch == 7) ch = 0;
                ++sg; if (sg == 3) sg = 0;
                const int st = lbase + RING + sg * STAGE;
#pragma unroll
                for (int i = 0; i < 4; ++i) dma16(rx, st + w4 * 4096 + i * 1024, (i & 1) ? vx1 : vx0, sx + (w4 * 4 + i) * 7168 + ch * 128);
#pragma unroll
                for (int cb = 0; cb < 2; ++cb)
#pragma unroll
                    for (int i = 0; i < 2; ++i) dma16(rw, st + 16384 + cb * 8192 + w4 * 2048 + i * 1024, vw, sw + (ch * 64 + w4 * 16 + i * 8) * 2688 + cb * 896);
                WAITV(8); BAR();
            }
#pragma unroll 1
            for (int c = 0; c < 7; ++c) {
                ++ch; if (ch == 7) ch = 0;
#pragma unroll
                for (int i = 0; i < 2; ++i) dma16(rw, lbase + LA + c * 8192 + w4 * 2048 + i * 1024, vw, sw + (ch * 64 + w4 * 16 + i * 8) * 2688 + 1792);
                if (c == 3) { WAITV(8); BAR(); }
            }
            BAR();
        }
        make_tag(); use_tab();
        asm volatile("" : "+s"(tb));
        const int jo = j * (4 * XT), sl = tb8[E_SL - 16];
        int ra0 = tb[E_RA0] + jo, ra1 = tb[E_RA1] + jo, ra2 = tb[E_RA2] + jo, rb0 = tb[E_RB0] + jo, rb1 = tb[E_RB1] + jo, rq = tb[E_RQ] + jo, lh = lbase + LR + w4 * 4096;
        asm volatile("" : "+s"(ra0), "+s"(ra1), "+s"(ra2), "+s"(rb0), "+s"(rb1), "+s"(rq), "+s"(lh));
        const int lb0 = lbase + tb[E_LB0], lb1 = lbase + tb[E_LB1];
        const int hd[2] = {tb[E_HD0], tb[E_HD1]}, cf[2] = {tb[E_CF0], tb[E_CF1]}, po[2] = {tb8[E_PO0 - 16], tb8[E_PO1 - 16]};
        const u64 mb = (u64)(unsigned)tb[E_MB] << 32, mall = mb | (unsigned)tb[E_MA];
        const char* f1 = ws + (WS_F1 + j * 256 + tb[E_PG0]) + (hh * 4096 - (r3 & 3) * 256 + (lane & 4) * tb[E_PGD] + p * 8);
        int l16 = lane * 16;
        PIN(f1); PIN(l16);
        int lz = lane; PIN(lz);
        const int g4 = lz >> 4, c16 = lz & 15;
        int fo[2], ko[2], qo[2], vo[2][2];
        f32x4 md, mo;
        const bool pt = x7 || g4 == sl;
#pragma unroll
        for (int e = 0; e < 2; ++e) fo[e] = g4 * 512 + (((c16 >> 1) ^ ((cf[e] + g4) & 3)) << 6) + (c16 & 1) * 32;
        int qs = pt ? fo[1] : OOB; PIN(qs);
        if (!local) {
#pragma unroll
            for (int e = 0; e < 2; ++e) { ko[e] = LW + 40960 - w4 * 8192 + po[e] + fo[e]; qo[e] = LW + w4 * 4096 + po[e] + fo[e]; PIN(ko[e]); PIN(qo[e]); }
#pragma unroll
            for (int r = 0; r < 4; ++r) {
                const int d = c16 - 4 * g4 - r;
                md[r] = (unsigned)d < 8u ? 0.f : -1.0e30f; mo[r] = (d < -8 && T) ? 0.f : -1.0e30f;
            }
            PIN(md); PIN(mo);
        }
        if (ty < 7) { WAITV(6); BAR(); }
        if (!local) {
            PIN(lz);
#pragma unroll
            for (int e = 0; e < 2; ++e)
#pragma unroll
                for (int s = 0; s < 2; ++s) {
                    const int pc8 = (2 * (lz & 3) + s + (hd[e] & 1)) & 7, cc = pc8 >> 1;
                    vo[e][s] = LW + 40960 - w4 * 8192 + 4096 + po[e] + cc * 512 + (((lz >> 3) ^ ((cf[e] + cc) & 3)) << 6) + (q & 1) * 32 + (pc8 & 1) * 8; PIN(vo[e][s]);
                }
        }

        if (wave == 4 && lane < 2) *(volatile LAS u64*)(wd + lane * 16) = 0;
        asm volatile("s_waitcnt vmcnt(0) lgkmcnt(0)" ::: "memory"); BAR();
        STAMP(4);
        const u32x4 rxx = srd(ws + WS_X, X_BYTES);
        auto tell = [&](int n) { if (lane == 0) *(volatile LAS u64*)(n < 2 ? wd + n * 16 : wk + 64) = FTAG; };
        auto hear = [&](int n) {
#pragma unroll 1
            for (int i = 0; i < SPIN_MAX * 4; ++i) { if (__all(*(volatile LAS u64*)(n < 2 ? wd + n * 16 : wk + 64) == FTAG)) break; if (HSL) __builtin_amdgcn_s_sleep(HSL); }
            asm volatile("" ::: "memory");
        };
        auto fetch_b = [&](int v) {
            const int go = v * (56 * 512), lo = v * 4096;
            if (x7) { dma32_dev(rxx, lb0 + lo, l16, rb0 + go); dma32_dev(rxx, lb1 + lo, l16, rb1 + go); }
            else {
                if (w4 == 0) dma64_dev(rxx, lb1 + lo, l16, rb1 + go);
                if (sl != 8 && lane < 32) { dma16_dev(rxx, lb0 + lo, l16, rb0 + go); if (w4) dma16_dev(rxx, lb1 + lo, l16, rb1 + go); }
            }
        };
        auto fetch_a = [&](int v) {
            const int go = v * (56 * 512), lo = v * 2048;
            dma32_dev(rxx, lh + 32768 + lo, l16, ra0 + go);
            dma32_dev(rxx, lh + 16384 + lo, l16, ra1 + go);
            dma32_dev(rxx, lh + lo, l16, ra2 + go);
        };
        STAMP(5);
        unsigned left = 1;
        if (!local) { if (wave == 4) { left = __builtin_amdgcn_readfirstlane((unsigned)poll<false>(f1, mall, mb, tag)); tell(0); if (!left) tell(1); } else hear(0); }
        STAMP(2);
        u32x4 qp[2];
        asm volatile("buffer_load_dwordx4 %0, %2, %3, %4 offen sc1\n\tbuffer_load_dwordx4 %1, %2, %3, %4 offen offset:16 sc1"
                     : "=&v"(qp[0]), "=&v"(qp[1]) : "v"(qs), "s"(rxx), "s"(rq) : "memory");
        if (!local) fetch_b(0);
        if (wave == 4) { if (left) { poll<false>(f1, mall ^ mb, mall ^ mb, tag); tell(1); } } else hear(1);
        STAMP(6);
        dma32_dev(rxx, lh + 2048, l16, ra0 - 56 * 512);
        fetch_a(0);
        WAITV(8);
        PIN(qp[0]); PIN(qp[1]);
        STAMP(3);
        bf16x8 pe[2][2];
        float ls[2][2];
        if (!local) {
            f32x4 sd[2][2], so[2][2];
#pragma unroll
            for (int e = 0; e < 2; ++e)
#pragma unroll
                for (int y = 0; y < 2; ++y) {
                    const char* kb = smem + ko[e] + y * 16;
                    u32x4 qv = *(const u32x4*)(smem + qo[e] + y * 16);
                    const bool a = pt && (e || x7), z0 = (hd[e] & 1) && g4 == 0, z1 = !(hd[e] & 1) && g4 == 3;
                    qv = (u32x4){a ? qp[y].x : qv.x, a ? qp[y].y : qv.y, a ? qp[y].z : qv.z, a ? qp[y].w : qv.w};
                    qv = (u32x4){z0 ? 0u : qv.x, z0 ? 0u : qv.y, z1 ? 0u : qv.z, z1 ? 0u : qv.w};
                    sd[e][y] = MFMA16(*(const bf16x8*)kb, __builtin_bit_cast(bf16x8, qv), md);
                    so[e][y] = MFMA16(*(const bf16x8*)(kb + 8192), __builtin_bit_cast(bf16x8, qv), mo);
                }
            const bf16x8 one = __builtin_bit_cast(bf16x8, (u32x4){0x3F803F80u, 0x3F803F80u, 0x3F803F80u, 0x3F803F80u});
            float m[4];
#pragma unroll
            for (int i = 0; i < 4; ++i) {
                const f32x4 a = sd[i >> 1][i & 1], c = so[i >> 1][i & 1];
                m[i] = fmx(fmx(fmx(fmx(a[0], a[1]), a[2]), fmx(fmx(a[3], c[0]), c[1])), fmx(c[2], c[3]));
            }
#pragma unroll
            for (int i = 0; i < 4; ++i) {
                const unsigned mu = __builtin_bit_cast(unsigned, m[i]);
                const auto r = __builtin_amdgcn_permlane32_swap(mu, mu, false, false);
                const unsigned r0 = r[0], r1 = r[1];
                m[i] = fmx(__builtin_bit_cast(float, r0), __builtin_bit_cast(float, r1));
            }
#pragma unroll
            for (int i = 0; i < 4; ++i) {
                const unsigned mu = __builtin_bit_cast(unsigned, m[i]);
                const auto r = __builtin_amdgcn_permlane16_swap(mu, mu, false, false);
                const unsigned r0 = r[0], r1 = r[1];
                m[i] = fmx(__builtin_bit_cast(float, r0), __builtin_bit_cast(float, r1));
            }
            SCHED();
            f32x4 sm[4];
#pragma unroll
            for (int i = 0; i < 4; ++i) {
                const f32x4 a = sd[i >> 1][i & 1], c = so[i >> 1][i & 1];
                f32x8 x = {a[0], a[1], a[2], a[3], c[0], c[1], c[2], c[3]};
                x -= m[i];
#pragma unroll
                for (int r = 0; r < 8; ++r) x[r] = __builtin_amdgcn_exp2f(x[r]);
                pe[i >> 1][i & 1] = __builtin_convertvector(x, bf16x8);
                sm[i] = MFMA16(one, pe[i >> 1][i & 1], (f32x4){});
                SCHED();
            }
#pragma unroll
            for (int i = 0; i < 4; ++i) ls[i >> 1][i & 1] = __builtin_amdgcn_rcpf(sm[i][0]);
#pragma unroll
            for (int i = 0; i < 4; ++i) { PIN(pe[i >> 1][i & 1]); PIN(ls[i >> 1][i & 1]); }
        }
        STAMP(10);
        asm volatile("s_waitcnt vmcnt(0) lgkmcnt(0)" ::: "memory");
        STAMP(7);
        __builtin_amdgcn_s_setprio(2);
        if (wave == 4) { poll<true>(f1 + 8192, mall, mall, tag); tell(2); }
        else {
            BAR(); BAR();
            dma_rows<14>(rwo, lbase + LA + w4 * 1024, vwo, w4 * 7168 + (ty & 6) * 128);
            hear(2);
        }
        STAMP(14);
        fetch_a(1);
        if (!local) fetch_b(1);
        if (!YEARLY) dma_rows<YB0>(rwo, lbase + LA + w4 * 1024, vwo, w4 * 7168 + (ty & 6) * 128);
        if (!local && (x7 || w4 == 0)) WAITV(4); else WAITV(0);
        ones(0, true); ones(2, false); WAITL0(); arrive(0);
        WAITV(0);
        if (!YEARLY) dma_rows<14 - YB0>(rwo, lbase + LA + (w4 + 4 * YB0) * 1024, vwo, (w4 + 4 * YB0) * 7168 + (ty & 6) * 128);
        else if (wave == 4) dma_rows<14>(rwo, lbase + LA, vwo, (ty & 6) * 128);
        STAMP(9);
        if (!local) {
            u32x4 ov[2][2];
#pragma unroll
            for (int e = 0; e < 2; ++e)
#pragma unroll
                for (int y = 0; y < 2; ++y) {
                    const char* v0 = smem + vo[e][0] + y * 16; const char* v1 = smem + vo[e][1] + y * 16;
                    const f32x4 a = MFMA16(tr_frag(v0, v0 + 8192), pe[e][y], (f32x4){}), c = MFMA16(tr_frag(v1, v1 + 8192), pe[e][y], (f32x4){});
                    f32x8 x = {a[0], a[1], a[2], a[3], c[0], c[1], c[2], c[3]};
                    x *= ls[e][y];
                    ov[e][y] = __builtin_bit_cast(u32x4, __builtin_convertvector(x, bf16x8));
                }
#pragma unroll
            for (int e = 0; e < 2; ++e)
#pragma unroll
                for (int y = 0; y < 2; ++y) store_dev(ov[e][y], ros, g4 * 512 + c16 * 16 + y * 256, ((64 + T) * 16 + hd[e]) * 2048);
        }
        STAMP(11);
        __builtin_amdgcn_s_setprio(0);
        if (!local) { arrive(8); WAITV(4); arrive(16); await4(8); } else { WAITV(0); arrive(16); }
        dma_rows<6>(rwo, lbase + LW + w4 * 1024, vwo, ty < 6 ? w4 * 7168 + (ty | 1) * 128 : OOB);
        if (!local) { WAITV(6); if (lane < 2) raise(ws + WS_F2 + (64 + T) * 128 + (lane ? hd[1] : hd[0]) * 8, tag); }
        if (ty < 6) dma_rows<8>(rwo, lbase + LW + (w4 + 24) * 1024, vwo, (w4 + 24) * 7168 + (ty | 1) * 128);
        if (b == 0) {
            poll<false>(ws + WS_F1 + w4 * 2048 + lane * 32, ~0ull, ~0ull, tag);
            arrive(12);
            if (wave == 4) { await4(12); if (lane == 0) __hip_atomic_store((gu64*)ws, tag, RLX); }
        }
        WAITV(0);
    } else {
        mask_bits();
        if (ty < 7) {
            int xo[2][4], wo_[2][2];
#pragma unroll
            for (int g = 0; g < 2; ++g) {
#pragma unroll
                for (int ks = 0; ks < 4; ++ks) { xo[g][ks] = RING + g * 65536 + (w4 * 32 + t) * 128 + (((2 * ks + hh) ^ ((t >> 1) & 7)) << 4); PIN(xo[g][ks]); }
                wo_[g][0] = RING + g * 65536 + wb0; wo_[g][1] = RING + g * 65536 + wb1; PIN(wo_[g][0]); PIN(wo_[g][1]);
            }
            int es[2], el[2][2], g0;
            unsigned pq, pk;
            char* f1;
            auto put = [&](const f32x16& a0, const f32x16& a1, int sec, unsigned pub, u32x4 (&d)[2][2]) {
#pragma unroll
                for (int e = 0; e < 2; ++e)
#pragma unroll
                    for (int s = 0; s < 2; ++s) {
                        d[e][s] = __builtin_bit_cast(u32x4, pack8(e ? a1 : a0, s));
                        if ((pub >> (2 * e + s)) & 1) store_dev(d[e][s], rxs, es[s] + e * 2048, g0 + sec * (56 * 512));
                    }
            };
            auto keep = [&](int lo, const u32x4 (&d)[2][2]) {
#pragma unroll
                for (int e = 0; e < 2; ++e)
#pragma unroll
                    for (int s = 0; s < 2; ++s) if (el[e][s] >= 0) *(u32x4*)(smem + el[e][s] + lo) = d[e][s];
            };
            f32x16 acc[4];
            bf16x8 xf[28], wf[2][4];
            const f32x16 zero = {};
            WAITV(0); BAR();
            STAMP(1); CST(20);
            auto rd = [&](int n) {
                const int sg = (n >> 2) % 3, ks = n & 3;
                const char* st = smem + (sg & 1) * STAGE;
#pragma unroll
                for (int i = 3; i >= 0; --i) {
                    const char* a = st + wo_[sg >> 1][i & 1] + 16384 + (i >> 1) * 8192 + ks * 2048;
                    wf[n & 1][i] = tr_frag(a, a + 512);
                    if (i == 3) xf[n] = *(const bf16x8*)(st + xo[sg >> 1][ks]);
                }
            };
            rd(0);
            SCHED();
#pragma unroll
            for (int n = 0; n < 28; ++n) {
                if (n == 11) {
                    make_tag(); use_tab();
#pragma unroll
            for (int s = 0; s < 2; ++s) {
                const int sw16 = ((((t >> 2) ^ (2 * s + hh)) << 2) + (t & 3)) * 16;
                es[s] = (2 * s + hh) * 512 + sw16; PIN(es[s]);
#pragma unroll
                for (int e = 0; e < 2; ++e) {
                    const int ps = tb8[E_PS - 16] + 4 * e + 2 * s + hh;
                    el[e][s] = (unsigned)ps < 8u ? ps * 512 + sw16 : -1; PIN(el[e][s]);
                }
            }
            pq = tb8[E_PQ - 16]; pk = tb8[E_PK - 16];
            g0 = (br * 64 + T) * XT + ty * 4096;
            f1 = ws + WS_F1 + (br * 16 + j) * 256 + ty * 32 + w4 * 8;
            asm volatile("" : "+s"(g0), "+s"(f1));
                    SCHED();
                }
                if ((n & 3) == 3) { WAITL0(); BAR(); SCHED(); }
                if (n < 27) rd(n + 1);
#pragma unroll
                for (int i = 3; i >= 0; --i) acc[i] = MFMA(wf[n & 1][i], xf[n], n ? acc[i] : zero);
                if (n < 27) { SG(0x8, 1); SG(0x100, 3); SG(0x8, 1); SG(0x100, 3); SG(0x8, 1); SG(0x100, 3); SG(0x8, 1); }
                SCHED();
            }
            STAMP(2);
            const float qs = 0.18898223650461361f * 1.4426950408889634f;
            {
                u32x4 dk[2][2], dq[2][2];
                put(acc[2], acc[3], 1, pk, dk);
                put(acc[0] * qs, acc[1] * qs, 0, pq, dq);
                if (br) { keep(LW + 40960 - w4 * 8192, dk); keep(LW + w4 * 4096, dq); }
            }
            STAMP(3);
            BAR();
            bf16x8 wv[8];
            u32x4 dv[2][2];
            auto rv = [&](int n) { const char* a = smem + LA + (n % 28) * 2048 + (n >= 28 ? wb1 : wb0); wv[n & 7] = tr_frag(a, a + 512); };
            auto putv = [&](int e, int s) {
                dv[e][s] = __builtin_bit_cast(u32x4, pack8(acc[2 + e], s));
                store_dev(dv[e][s], rxs, es[s] + e * 2048, ((pk >> (2 * e + s)) & 1) ? g0 + 112 * 512 : OOB);
            };
#pragma unroll
            for (int n = 0; n < 6; ++n) rv(n);
            PIN(nd); PIN(no); PIN(lm);
            SCHED();
#pragma unroll
            for (int n = 0; n < 56; ++n) {
                if (n == BV1) { BAR(); SCHED(); }
                if (n == RAISE_B || n == RAISE_A) { if ((n == RAISE_B) == (br != 0)) { WAITV(0); if (lane == 0) raise(f1, tag); STAMP(9); } SCHED(); }
                acc[2 + n / 28] = MFMA(wv[n & 7], xf[n % 28], n % 28 ? acc[2 + n / 28] : zero);
                if (n + 6 < 56) rv(n + 6);
                if (n >= 36 && n < 52) mask_reg(n - 36);
                SG(0x8, 1); SG(0x100, 2); SG(0x2, 8);
                SCHED();
                if (n == 30 || n == 32) { putv(0, (n - 30) >> 1); SCHED(); }
            }
            STAMP(4); CST(21);
            putv(1, 0); putv(1, 1);
            if (br) keep(LW + 45056 - w4 * 8192, dv);
            STAMP(5);
            WAITV(0);
            if (lane == 0) raise(f1 + 8192, tag);
        } else {
            make_tag(); use_tab();
            BAR();
            if (threadIdx.x == 0) raise(ws + WS_F1 + (br * 16 + j) * 256 + 7 * 32, tag);
#pragma unroll
            for (int r = 0; r < 16; ++r) mask_reg(r);
        }
        const char* tl = smem + LR + w4 * 4096;
        int fo[2] = {koff(c0, 0), koff(c0, 1)}, vo[2] = {voff(h, c0, 0) + 2048, voff(h, c0, 1) + 2048};
        PIN(fo[0]); PIN(fo[1]); PIN(vo[0]); PIN(vo[1]);
        STAMP(6);
        WAITL0(); BAR();
        STAMP(7);
        __builtin_amdgcn_s_setprio(1);
        u32x4 qf[2];
        qf[0] = *(const u32x4*)(tl + 2048 + fo[0]); qf[1] = *(const u32x4*)(tl + 2048 + fo[1]);
        auto kfrag = [&](int n, int s) { return *(const bf16x8*)(tl + (8 - n) * 4096 + fo[s]); };
        auto vfrag = [&](int n, int s) { const char* vb = tl + (8 - n) * 4096 + s * 256; return tr_frag(vb + vo[0], vb + vo[1]); };
        bf16x8 k0 = kfrag(0, 0), k1 = kfrag(0, 1), k2 = kfrag(8, 0), k3 = kfrag(8, 1);
        WAITL0(); BAR();
        CST(19); STAMP(10);
        qfix(h, qf[0], qf[1]);
        const bf16x8 q0 = __builtin_bit_cast(bf16x8, qf[0]), q1 = __builtin_bit_cast(bf16x8, qf[1]);
        auto qk = [&](int n, const f32x16& c) { f32x16 r = MFMA(kfrag(n, 0), q0, c); return MFMA(kfrag(n, 1), q1, r); };
        f32x16 cm, sa, sb;
        bf16x8 pp[8][2];
        cd = MFMA(k0, q0, cd); co = MFMA(k2, q0, co); cd = MFMA(k1, q1, cd); co = MFMA(k3, q1, co);
        k0 = kfrag(1, 0); k1 = kfrag(1, 1);
#pragma unroll
        for (int r = 0; r < 16; ++r) cd[r] = fmx(cd[r], co[r]);
        {
            const float m = -rowmax(cd);
#pragma unroll
            for (int r = 0; r < 16; ++r) cm[r] = m;
        }
        cd += cm;
        sa = cd;
        SCHED();
#define STG(n, cur, nxt) do { \
            if (n < 7) { nxt = MFMA(k0, q0, cm); nxt = MFMA(k1, q1, nxt); } \
            if (n < 6) { k0 = kfrag(n + 2, 0); k1 = kfrag(n + 2, 1); } \
            ex(cur); pp[n][0] = pack8(cur, 0); pp[n][1] = pack8(cur, 1); \
            if (n < 7) SG(0x8, 1); SG(0x400, 4); if (n < 7) SG(0x8, 1); if (n < 6) SG(0x100, 2); SG(0x400, 4); SG(0x2, 2); SG(0x400, 4); SG(0x2, 2); SG(0x400, 4); SG(0x2, 4); SCHED(); } while (0)
        STG(0, sa, sb); STG(1, sb, sa); STG(2, sa, sb); STG(3, sb, sa); STG(4, sa, sb); STG(5, sb, sa); STG(6, sa, sb); STG(7, sb, sa);
        bf16x8 pz[2];
#pragma unroll
        for (int s = 0; s < 2; ++s) {
            const u32x4 e = __builtin_bit_cast(u32x4, pp[0][s]);
            pp[0][s] = __builtin_bit_cast(bf16x8, e & pm[s]); pz[s] = __builtin_bit_cast(bf16x8, e & ~pm[s]); PIN(pz[s]);
        }
#pragma unroll
        for (int n = 0; n < 8; ++n) { PIN(pp[n][0]); PIN(pp[n][1]); }
        char* f2 = ws + WS_F2 + T * 128 + h * 8;
        int orec = (T * 16 + h) * 2048;
        const char* va = tl + vo[0]; const char* vb = tl + vo[1];
        asm volatile("" : "+s"(f2), "+s"(orec));
        STAMP(8);
        await4(0);
        STAMP(14);
        f32x16 o = {};
        auto pv = [&](int n, bf16x8 p0, bf16x8 p1) { o = MFMA(vfrag(n, 0), p0, o); o = MFMA(vfrag(n, 1), p1, o); };
        {
            bf16x8 vf[18];
            auto rdv = [&](int i) { const int off = (8 - (i >> 1)) * 4096 + (i & 1) * 256; vf[i] = tr_frag(va + off, vb + off); };
#pragma unroll
            for (int i = 0; i < 5; ++i) { rdv(i); SCHED(); }
#pragma unroll
            for (int i = 0; i < 18; ++i) {
                if (PPV && i == 5) { await4(20); SCHED(); }
                o = MFMA(vf[i], i < 16 ? pp[i >> 1][i & 1] : pz[i & 1], o);
                if (i + 5 < 18) rdv(i + 5);
                SCHED();
            }
        }
        auto publish = [&]() {
            const float lf = o[12];
            const unsigned lu = __builtin_bit_cast(unsigned, lf);
            const unsigned lx = __builtin_amdgcn_permlane32_swap(lu, lu, false, false)[1];
            put_o(o, __builtin_bit_cast(float, lx), orec);
        };
        publish();
        STAMP(11); CST(22);
        float big = fmx(__builtin_fabsf(o[0]), __builtin_fabsf(o[1]));
#pragma unroll
        for (int r = 2; r < 16; r += 2) big = fmx(fmx(big, __builtin_fabsf(o[r])), __builtin_fabsf(o[r + 1]));
        if (__builtin_expect(!__all(big < 1.0e36f), 0)) {
            const int nmid = T >= 7 ? 7 : T;
            float m = 0.f;
#pragma unroll 1
            for (int i = 1; i <= nmid; ++i) m = fmx(m, rowmax(qk(i, cm)));
            cd -= m; cm -= m;
            ex(cd);
            o = (f32x16){};
            const u32x4 e0 = __builtin_bit_cast(u32x4, pack8(cd, 0)), e1 = __builtin_bit_cast(u32x4, pack8(cd, 1));
            pv(0, __builtin_bit_cast(bf16x8, e0 & pm[0]), __builtin_bit_cast(bf16x8, e1 & pm[1]));
            pv(8, __builtin_bit_cast(bf16x8, e0 & ~pm[0]), __builtin_bit_cast(bf16x8, e1 & ~pm[1]));
#pragma unroll 1
            for (int i = 1; i <= nmid; ++i) {
                f32x16 s = qk(i, cm);
                ex(s); pv(i, pack8(s, 0), pack8(s, 1));
            }
            WAITV(0);
            publish();
        }
        if (RDFIRST) { await4(16); read_wo(LA); SCHED(); }
        WAITV(0);
        if (lane == 0) raise(f2, tag);
        __builtin_amdgcn_s_setprio(0);
        STAMP(12);
        if (!RDFIRST) { await4(16); read_wo(LA); }
    }
    STAMP(13);
    const int To = br * 64 + 4 * j + 2 * (ty & 1);
    const int tr = br ? 2 * (t & 15) + (t >> 4) : t;
    const int tb = LR + 32768 + ((wave >> 2) * 2 + (w4 & 1)) * 4096;
    int l16 = lane * 16, oa = (w4 & 1) * LR + hh * 2048 + t * 16, tw = tb + tr * 128 + (((4 * (w4 >> 1) + hh) ^ (tr & 7)) << 4), tg = tb + (w4 >> 1) * 2048 + r3 * 128 + ((u ^ r3) << 4);
    int ov = r3 * 1792 + u * 16;
    int oo = ((4 * j + 2 * (ty & 1) + (w4 & 1)) * 32 + (w4 >> 1) * 16) * 1792 + br * 896 + ((ty & 6) + (wave >> 2)) * 128;
    const int os = (To * 16 + w4) * 2048, ol = lbase + w4 * 2048;
    if (F64) { tg = tb + (lane >> 2) * 128 + (((4 * (w4 >> 1) + p) ^ ((lane >> 2) & 7)) << 4); ov = (lane >> 2) * 1792 + p * 16; oo += (w4 >> 1) * (64 - 16 * 1792); }
    asm volatile("" : "+v"(l16), "+v"(oa), "+v"(tw), "+v"(tg), "+v"(ov)); asm volatile("" : "+s"(oo));
    const __amdgpu_buffer_rsrc_t rout = rsrc(out, 2048 * 1792);
    const u32x4 roo = srd(ws + WS_O, O_BYTES);
    const bool idle = aux && ty >= 6;
    if (wave == 0) poll<false>(ws + WS_F2 + To * 128 + lane * 8, 0xFFFFFFFFull, 0xFFFFFFFFull, tag);
    WAITL0(); BAR();
    STAMP(15);
    if (idle) { STAMP_FLUSH(); return; }
    auto geto = [&](int qd) { dma32_dev(roo, ol + qd * 8192, l16, os + qd * 8192); dma32_dev(roo, ol + qd * 8192 + LR, l16, os + qd * 8192 + 32768); };
    f32x16 acc = {};
    {
        bf16x8 of[2][7];
        auto rdo = [&](int qd) {
            const char* a = smem + oa + qd * 8192;
#pragma unroll
            for (int i = 0; i < 6; ++i) of[qd & 1][i] = *(const bf16x8*)(a + (i / 3) * 4096 + (i % 3) * 512);
            const u32x2 lo = *(const u32x2*)(a + 1536), hi = *(const u32x2*)(a + 4096 + 1536);
            of[qd & 1][6] = __builtin_bit_cast(bf16x8, (u32x4){lo.x, lo.y, hi.x, hi.y});
        };
        auto mmo = [&](int qd, int i0, int i1) {
#pragma unroll
            for (int i = i0; i < i1; ++i) acc = MFMA(wo[qd * 7 + i], of[qd & 1][i], acc);
            SCHED();
        };
        if (aux) read_wo(LW); else { geto(0); geto(1); geto(2); WAITV(8); }
        BAR(); rdo(0); SCHED();
        if (!aux) geto(3);
        mmo(0, 0, 3);
        WAITV(8);
        BAR(); rdo(1); SCHED();
        mmo(0, 3, 7); mmo(1, 0, 3);
        WAITV(4);
        BAR(); rdo(2); SCHED();
        mmo(1, 3, 7); mmo(2, 0, 3);
        WAITV(0);
        BAR(); rdo(3); SCHED();
        mmo(2, 3, 7); mmo(3, 0, 7);
    }
    STAMP(16);
    {
#pragma unroll
        for (int s = 0; s < 2; ++s) *(bf16x8*)(smem + (tw ^ (s << 5))) = pack8(acc, s);
        WAITL0(); if (!F64) BAR();
        const u32x4 v0 = *(const u32x4*)(smem + tg), v1 = *(const u32x4*)(smem + tg + (F64 ? 2048 : 1024));
        SCHED();
        store_dev(v0, rout, ov, oo); store_dev(v1, rout, ov, oo + (F64 ? 16 : 8) * 1792);
    }
    STAMP(17);
    STAMP_FLUSH();
}

extern "C" __device__ __attribute__((noinline, used)) void fused_end() {}

extern "C" void kernel_launch(void* const* d_in, const int* in_sizes, int n_in, void* d_out, int out_size, void* d_ws, size_t ws_size, hipStream_t stream) {
    fused<<<256, 512, 0, stream>>>((const char*)d_in[0], (const char*)d_in[1], (const char*)d_in[2], (const char*)d_in[3], (const char*)d_in[4], (const char*)d_in[5], (char*)d_out, (char*)d_ws);
}
```

```cpp
#include <hip/hip_runtime.h>
#include <hip/hip_bf16.h>

typedef __bf16 bf16x8 __attribute__((ext_vector_type(8)));
typedef short s16x4 __attribute__((ext_vector_type(4)));
typedef float f32x16 __attribute__((ext_vector_type(16)));
typedef float f32x8 __attribute__((ext_vector_type(8)));
typedef float f32x4 __attribute__((ext_vector_type(4)));
typedef unsigned u32x4 __attribute__((ext_vector_type(4)));
typedef unsigned u32x2 __attribute__((ext_vector_type(2)));
typedef unsigned long long u64;
typedef __attribute__((address_space(1))) u64 gu64;
#define LAS __attribute__((address_space(3)))
#define DI __device__ __forceinline__
#define MFMA(a, b, c) __builtin_amdgcn_mfma_f32_32x32x16_bf16((a), (b), (c), 0, 0, 0)
#define MFMA16(a, b, c) __builtin_amdgcn_mfma_f32_16x16x32_bf16((a), (b), (c), 0, 0, 0)
#define RLX __ATOMIC_RELAXED, __HIP_MEMORY_SCOPE_AGENT

constexpr int XB = 2048 * 896, WB = 448 * 2688, OB = 448 * 896;
constexpr int STAGE = 32768, RING = 65536, LA = 0, LW = 57344, LR = 114688, LX = 106496, LDS_BYTES = 163840;
#define FS 4352
constexpr int WS_F1 = 4096, WS_F2 = WS_F1 + 64 * FS, WS_X = 1 << 20, XT = 168 * 512, X_BYTES = 2 * 64 * XT, WS_O = 16 << 20, O_BYTES = 2 * 64 * 16 * 2048;
constexpr int OOB = 0x40000000;
#define ROT 2
#define PF0 2
#define RAISE_A 27
#define RAISE_B 4
#define BV1 10
#define KH_DELAY 8
#define OPRE 10
#define OROT 0
#define XB0 5
#define YB0 3
#define YEARLY 1
#define HSL 0
#define F64 0
#define PPV 0
#define RDFIRST 1
constexpr int SPIN_MAX = 1 << 18;

enum { E_RA0, E_RA1, E_RA2, E_RB0, E_RB1, E_RQ, E_LB0, E_LB1, E_MA, E_MB, E_PG0, E_PGD, E_HD0, E_HD1, E_CF0, E_CF1, E_PO0, E_PO1, E_SL, E_H, E_C0, E_PS, E_PQ, E_PK };
struct Tab {
    int e[192][32];
    constexpr Tab() : e() {
        for (int i = 0; i < 192; ++i) {
            const int w4 = i & 3, ty = (i >> 2) & 7, br = (i >> 5) & 1, jc = i >> 6;
            const bool x7 = ty == 7, local = br == 0 && !x7;
            const int ha = (0x4ECA7530u >> (4 * ty)) & 15, hb = (0xBFD98621u >> (4 * ty)) & 15;
            const int ca = (7 * ha) >> 1, cb = (7 * hb) >> 1, cs = ca < cb ? ca : cb;
            const int h = br ? hb : ha, c0 = br ? cb : ca;
            const int nb = x7 ? 15 : (0xF63F30Fu >> (4 * ty)) & 15;
            const int sl = (ty == 2 || ty == 5) ? 3 : 0;
            const int two = (x7 || (br && nb != 15)) ? 0xFF : 0x0F;
            const int rt = (64 + w4) * XT;
            int* v = e[i];
            v[E_HD0] = x7 ? h : ha; v[E_HD1] = x7 ? h : hb; v[E_CF0] = x7 ? c0 : ca; v[E_CF1] = x7 ? c0 : cb;
            v[E_PO0] = x7 ? 0 : (ca - cs) * 512; v[E_PO1] = x7 ? 0 : (cb - cs) * 512;
            v[E_RA0] = w4 * XT + (56 + c0) * 512; v[E_RA1] = jc >= 1 ? v[E_RA0] - 4 * XT : OOB; v[E_RA2] = jc >= 2 ? v[E_RA0] - 8 * XT : OOB;
            v[E_RB0] = rt + (56 + (x7 ? c0 : cb + sl)) * 512;
            v[E_RB1] = (jc == 0 && w4 == 0) ? OOB : ((!x7 && w4 == 0) ? rt + (56 + cs) * 512 : v[E_RB0]) - XT;
            v[E_RQ] = local ? OOB : rt + v[E_CF1] * 512;
            v[E_LB0] = LW + 40960 - w4 * 8192 + (x7 ? 0 : v[E_PO1] + sl * 512); v[E_LB1] = (!x7 && w4 == 0) ? LX : v[E_LB0] + 8192;
            v[E_MA] = two | (jc >= 1 ? two << 8 : 0) | (jc >= 2 ? two << 16 : 0);
            v[E_MB] = local ? 0 : (x7 ? 0xFF : two & 0xF0) | (jc ? (two & 0x88) << 8 : 0);
            v[E_PG0] = (x7 ? 1 + 3 * br : ty) * 32; v[E_PGD] = (x7 ? 1 : (nb == 15 ? 0 : nb - ty)) * 8;
            v[E_SL] = nb == 15 ? 8 : sl;
            v[E_H] = h; v[E_C0] = c0; v[E_PS] = 8 * ty - cs;
            v[E_PQ] = br ? (0x1189188u >> (4 * ty)) & 15 : 15; v[E_PK] = w4 != 3 ? v[E_PQ] : 15;
        }
    }
};
__device__ const Tab g_tab = Tab();

#define STAMP(i)
#define CST(i)
#define STAMP_DECL
#define STAMP_FLUSH()

DI u32x4 srd(const void* p, unsigned bytes) { const u64 a = (u64)p; return (u32x4){(unsigned)a, (unsigned)(a >> 32) & 0xffffu, bytes, 0x00020000u}; }
DI __amdgpu_buffer_rsrc_t rsrc(const void* p, int bytes) { return __builtin_amdgcn_make_buffer_rsrc((void*)p, 0, bytes, 0x00020000); }
DI void dma16(u32x4 r, int lds, int voff, int soff) {
    asm volatile("s_mov_b32 m0, %0\n\ts_nop 0\n\tbuffer_load_dwordx4 %1, %2, %3 offen lds" :: "s"(lds), "v"(voff), "s"(r), "s"(soff) : "memory");
}
DI void dma16_dev(u32x4 r, int lds, int voff, int soff) {
    asm volatile("s_mov_b32 m0, %0\n\ts_nop 0\n\tbuffer_load_dwordx4 %1, %2, %3 offen sc1 lds" :: "s"(lds), "v"(voff), "s"(r), "s"(soff) : "memory");
}
DI void dma64_dev(u32x4 r, int lds, int voff, int soff) {
    asm volatile("s_mov_b32 m0, %0\n\ts_nop 0\n\tbuffer_load_dwordx4 %1, %2, %3 offen sc1 lds\n\tbuffer_load_dwordx4 %1, %2, %3 offen offset:1024 sc1 lds\n\t"
                 "buffer_load_dwordx4 %1, %2, %3 offen offset:2048 sc1 lds\n\tbuffer_load_dwordx4 %1, %2, %3 offen offset:3072 sc1 lds" :: "s"(lds), "v"(voff), "s"(r), "s"(soff) : "memory");
}
DI void dma32_dev(u32x4 r, int lds, int voff, int soff) {
    asm volatile("s_mov_b32 m0, %0\n\ts_nop 0\n\tbuffer_load_dwordx4 %1, %2, %3 offen sc1 lds\n\tbuffer_load_dwordx4 %1, %2, %3 offen offset:1024 sc1 lds"
                 :: "s"(lds), "v"(voff), "s"(r), "s"(soff) : "memory");
}
DI void store_dev(u32x4 v, __amdgpu_buffer_rsrc_t r, int voff, int soff) {
    __builtin_amdgcn_raw_buffer_store_b128(v, r, voff, soff, 16);
    asm volatile("s_nop 1" :: "v"(v) : "memory");
}
template <int N>
DI void dma_rows(u32x4 r, int lds, int voff, int soff) {
    asm volatile("s_mov_b32 m0, %0\n\ts_nop 0\n\tbuffer_load_dwordx4 %2, %3, %1 offen lds\n.rept %4\n\ts_add_u32 m0, m0, 0x1000\n\ts_add_u32 %1, %1, 0x7000\n\tbuffer_load_dwordx4 %2, %3, %1 offen lds\n.endr"
                 : "+s"(lds), "+s"(soff) : "v"(voff), "s"(r), "i"(N - 1) : "scc", "memory");
}
DI bf16x8 tr_frag(const char* a0, const char* a1) {
    s16x4 lo = __builtin_amdgcn_ds_read_tr16_b64_v4i16((LAS s16x4*)a0);
    s16x4 hi = __builtin_amdgcn_ds_read_tr16_b64_v4i16((LAS s16x4*)a1);
    return __builtin_bit_cast(bf16x8, __builtin_shufflevector(lo, hi, 0, 1, 2, 3, 4, 5, 6, 7));
}
DI bf16x8 pack8(const f32x16& v, int s) {
    f32x8 t = {v[8 * s], v[8 * s + 1], v[8 * s + 2], v[8 * s + 3], v[8 * s + 4], v[8 * s + 5], v[8 * s + 6], v[8 * s + 7]};
    return __builtin_convertvector(t, bf16x8);
}
DI float xhalf(float v) {
    const unsigned u = __builtin_bit_cast(unsigned, v);
    const auto r = __builtin_amdgcn_permlane32_swap(u, u, false, false);
    return __builtin_bit_cast(float, (threadIdx.x & 32) ? r[0] : r[1]);
}
#define WAITV(n) asm volatile("s_waitcnt vmcnt(" #n ")" ::: "memory")
#define WAITL0() asm volatile("s_waitcnt lgkmcnt(0)" ::: "memory")
#define BAR() do { asm volatile("" ::: "memory"); __builtin_amdgcn_s_barrier(); asm volatile("" ::: "memory"); } while (0)
#define SCHED() __builtin_amdgcn_sched_barrier(0)

#define SG(m, n) __builtin_amdgcn_sched_group_barrier(m, n, 0)
DI float fmx(float a, float b) { return __builtin_elementwise_maximum(a, b); }
typedef int i32x4 __attribute__((ext_vector_type(4)));
#define PIN(x) asm volatile("" : "+v"(x))
DI void raise(char* p, u64 tag) { (void)__hip_atomic_exchange((gu64*)p, tag, RLX); }
template <bool TWO_BARRIERS>
DI u64 poll(const char* p, u64 all, u64 need, u64 tag) {
    u64 v, zero, sv, left; unsigned cnt = SPIN_MAX;
#define POLL_LOOP ".Lpw%=:\n\ts_waitcnt vmcnt(0)\n\tv_cmp_ne_u64 %4, %6, %0\n\ts_and_b64 vcc, %4, %8\n\ts_cbranch_scc0 .Lpd%=\n\ts_sub_u32 %3, %3, 1\n\ts_cbranch_scc1 .Lpd%=\n\t" \
                  "global_atomic_add_x2 %0, %5, %1, off sc0\n\ts_branch .Lpw%=\n.Lpd%=:\n\ts_mov_b64 exec, %2"
#define POLL_OPS : "=&v"(v), "=&v"(zero), "=&s"(sv), "+s"(cnt), "=&s"(left) : "v"(p), "s"(tag), "s"(all), "s"(need) : "vcc", "scc", "memory"
    if (TWO_BARRIERS)
        asm volatile("s_mov_b64 %2, exec\n\ts_mov_b64 exec, %7\n\tv_mov_b64 %1, 0\n\tglobal_atomic_add_x2 %0, %5, %1, off sc0\n\ts_barrier\n\ts_barrier\n" POLL_LOOP POLL_OPS);
    else
        asm volatile("s_mov_b64 %2, exec\n\ts_mov_b64 exec, %7\n\tv_mov_b64 %1, 0\n\tglobal_atomic_add_x2 %0, %5, %1, off sc0\n" POLL_LOOP POLL_OPS);
    return left;
}

extern "C" __global__ void __launch_bounds__(512, 2)
fused(const char* xa, const char* wa, const char* woa, const char* xb, const char* wb, const char* wob, char* out, char* ws) {
    __shared__ __attribute__((aligned(16))) char smem[LDS_BYTES];
    STAMP_DECL;
    STAMP(0);
    const int b = blockIdx.x, xcd = b & 7, idx = b >> 3;
    const int br = xcd & 1, jj = idx >> 3, ty = idx & 7, j = (xcd >> 1) * 4 + jj;
    unsigned d0 = threadIdx.x * 128;
    {
        u64 pc;
        asm volatile("s_cmp_lg_u32 %2, 7\n\ts_cbranch_scc1 .Lskip%=\n\tv_cmp_gt_u32 vcc, fused_end-.Lhere%=, %1\n\ts_cbranch_vccz .Lskip%=\n\ts_mov_b64 exec, vcc\n\t"
                     "s_getpc_b64 %0\n.Lhere%=:\n\tglobal_load_dword %1, %1, %0\n\ts_mov_b64 exec, -1\n.Lskip%=:"
                     : "=&s"(pc), "+v"(d0) : "s"(ty) : "vcc", "scc", "memory");
    }
    const int wave = __builtin_amdgcn_readfirstlane(threadIdx.x >> 6), lane = threadIdx.x & 63;
    const int r3 = lane >> 3, u = lane & 7;
    const int lbase = (int)(unsigned)(size_t)(LAS char*)smem;
    int vx0 = r3 * 896 + ((u ^ (r3 >> 1)) << 4), vx1 = vx0 ^ 64;
    int vw = r3 * 2688 + ((u ^ (((r3 >> 1) & 1) << 2)) << 4);
    const int rot = ROT == 2 ? ((ty + j) & 1) * 4 : 0;
    const int sx = j * (128 * 896), sw = ty * 128;
    int e0 = sx + wave * 14336 + rot * 128, e1 = sw + (rot * 64 + wave * 8) * 2688, e2 = lbase + RING + wave * 2048, e3 = lbase + RING + 16384 + wave * 1024;
    asm volatile("" : "+s"(e0), "+s"(e1), "+s"(e2), "+s"(e3)); asm volatile("" : "+v"(vx0), "+v"(vx1), "+v"(vw));
    SCHED();
    const u32x4 rx = srd(br ? xb : xa, XB), rw = srd(br ? wb : wa, WB);
    if (ty < 7) {
        dma16(rx, e2, vx0, e0);
        dma16(rw, e3, vw, e1);
        dma16(rx, e2 + 1024, vx1, e0 + 7168);
        dma16(rw, e3 + 8192, vw, e1 + 896);
    } else {
        {
            const int ln = jj * 392 + (threadIdx.x < 392 ? threadIdx.x : 0), row = (ln * 9363) >> 16;
            const int vl = row * 2688 + 1792 + (ln - row * 7) * 128;
            asm volatile("global_load_dword %0, %1, %2" : "+v"(d0) : "v"(vl), "s"(br ? wb : wa) : "memory");
            asm volatile("global_load_dword %0, %1, %2" : "+v"(d0) : "v"(vl + 224 * 2688), "s"(br ? wb : wa) : "memory");
        }
        const int wl = (jj * 784 + (threadIdx.x < 392 ? threadIdx.x : 0)) * 128;
        asm volatile("global_load_dword %0, %1, %2" : "+v"(d0) : "v"(wl), "s"(br ? wob : woa) : "memory");
        asm volatile("global_load_dword %0, %1, %2" : "+v"(d0) : "v"(wl + 392 * 128), "s"(br ? wob : woa) : "memory");
        asm volatile("s_waitcnt vmcnt(0)" : "+v"(d0) :: "memory");
    }
    u64 tag = *(const __attribute__((address_space(4))) u64*)ws;
    auto make_tag = [&]() { asm volatile("" : "+s"(tag)); tag = tag * 0x9E3779B97F4A7C15ull + 0x7F4A7C159E3779B9ull; asm volatile("" : "+s"(tag)); };
    const int w4 = wave & 3;
    const bool aux = wave >= 4;
    const int hh = lane >> 5, t = lane & 31, blk = (lane >> 4) & 1, q = (lane >> 2) & 3, p = lane & 3;
    const int T = 4 * j + w4;
    const bool local = br == 0 && ty < 7;
    typedef int i32x8 __attribute__((ext_vector_type(8)));
    typedef int i32x16 __attribute__((ext_vector_type(16)));
    const __attribute__((address_space(4))) char* te = (const __attribute__((address_space(4))) char*)g_tab.e[(min(j, 2) * 2 + br) * 32 + ty * 4 + w4];
    i32x8 tb8 = *(const __attribute__((address_space(4))) i32x8*)(te + 64);
    int h, c0;
    auto use_tab = [&]() { asm volatile("" : "+s"(tb8)); h = tb8[E_H - 16]; c0 = tb8[E_C0 - 16]; };
    const __amdgpu_buffer_rsrc_t rxs = rsrc(ws + WS_X, X_BYTES);
    const int wb0 = (8 * hh + q) * 128 + ((blk * 2 + (p & 1)) << 4) + (p >> 1) * 8 + ((q >> 1) << 6), wb1 = wb0 ^ 64;

    const unsigned NEGB = 0xF149F2CAu;
    f32x16 cd, co;
    u32x4 pm[2];
    int nd, no, lm;
    auto mask_bits = [&]() {
        const unsigned lo = (2u << t) - 1u;
        nd = (int)(~lo >> (4 * hh)); lm = (int)(lo >> (4 * hh)); no = T < 8 ? -1 : lm;
    };
    auto mask_reg = [&](int r) {
        const int bit = (r & 3) + 8 * (r >> 2);
        float a = __builtin_bit_cast(float, __builtin_amdgcn_sbfe(nd, bit, 1) & NEGB), c = __builtin_bit_cast(float, __builtin_amdgcn_sbfe(no, bit, 1) & NEGB);
        PIN(a); PIN(c); cd[r] = a; co[r] = c;
        if (r & 1) { unsigned m = __builtin_amdgcn_perm(__builtin_amdgcn_sbfe(lm, bit, 1), __builtin_amdgcn_sbfe(lm, bit - 1, 1), 0x05040100u); PIN(m); pm[r >> 3][(r >> 1) & 3] = m; }
    };
    auto koff = [&](int cf, int s) { return (2 * s + hh) * 512 + ((((t >> 2) ^ ((cf + 2 * s + hh) & 3)) << 2) + (t & 3)) * 16; };
    auto voff = [&](int hd, int cf, int tt) {
        const int pc8 = (4 * blk + 2 * (p & 1) + (p >> 1) + (hd & 1)) & 7, cc = pc8 >> 1;
        return cc * 512 + ((((2 * tt + hh) ^ ((cf + cc) & 3)) << 2) + q) * 16 + (pc8 & 1) * 8;
    };
    auto qfix = [&](int hd, u32x4& q0, u32x4& q1) {
        const bool z0 = (hd & 1) && !hh, z1 = !(hd & 1) && hh;
        q0.x = z0 ? 0u : q0.x; q0.y = z0 ? 0u : q0.y; q1.z = z1 ? 0u : q1.z; q1.w = z1 ? 0u : q1.w;
    };
    auto ex = [&](f32x16& s) {
#pragma unroll
        for (int r = 0; r < 16; ++r) s[r] = __builtin_amdgcn_exp2f(s[r]);
    };
    auto rowmax = [&](const f32x16& s) {
        float m = fmx(s[0], s[1]);
#pragma unroll
        for (int r = 2; r < 16; r += 2) m = fmx(fmx(m, s[r]), s[r + 1]);
        const unsigned mu = __builtin_bit_cast(unsigned, m);
        const auto r = __builtin_amdgcn_permlane32_swap(mu, mu, false, false);
        const unsigned r0 = r[0], r1 = r[1];
        return fmx(__builtin_bit_cast(float, r0), __builtin_bit_cast(float, r1));
    };
    const __amdgpu_buffer_rsrc_t ros = rsrc(ws + WS_O, O_BYTES);
    auto put_o = [&](f32x16 o, float l, int rec) {
        o = o * __builtin_amdgcn_rcpf(l);
        store_dev(__builtin_bit_cast(u32x4, pack8(o, 0)), ros, lane * 16, rec);
        store_dev(__builtin_bit_cast(u32x4, pack8(o, 1)), ros, lane * 16 + 1024, rec);
    };
    auto ones = [&](int back, bool two) {
        const int bb = back + (two ? hh : 0);
        if (two || !hh) *(u32x2*)(smem + LR + w4 * 4096 + (2 - bb) * 16384 + 2048 + ((h & 1) ? 0 : 1544) + t * 16) = (u32x2){j >= bb ? 0x3F80u : 0u, 0u};
    };
    LAS char* wd = (LAS char*)(smem + LW + 7 * 512 + 8);

    #define wk ((LAS char*)(smem + LR + 32768 + ((h & 1) ? 0 : 1544)))
#define FTAG ((tag | 1) & 0xBFFFBFFFBFFFBFFFull)
    auto arrive = [&](int n) { if (lane == 0) *(volatile LAS u64*)(wk + (n + w4) * 16) = FTAG; };
    auto await4 = [&](int n) {
#pragma unroll 1
        for (int i = 0; i < SPIN_MAX * 4; ++i) if (__all(*(volatile LAS u64*)(wk + (n + p) * 16) == FTAG)) break;
        asm volatile("" ::: "memory");
    };
    bf16x8 wo[28];
    auto read_wo = [&](int base) {
        int a = base + ((w4 >> 1) ? wb1 : wb0) + hh * (20 * 128);
        PIN(a);
#pragma unroll
        for (int ks = 0; ks < 28; ++ks) {
            const int i = ks % 7, row = (ks / 7) * 112 + (i < 6 ? (i / 3) * 56 + (i % 3) * 8 : 24);
            wo[ks] = tr_frag(smem + a + row * 128, smem + a + (row + (i < 6 ? 4 : 56)) * 128);
        }
    };
    const int vwo = r3 * 896 + ((u ^ (((r3 >> 1) & 1) << 2)) << 4);
    const u32x4 rwo = srd(br ? wob : woa, OB);

    if (__builtin_expect(aux, 1)) {
        const bool x7 = ty == 7;
        i32x16 tb = *(const __attribute__((address_space(4))) i32x16*)te;
        if (ty < 7) {
            int ch = rot, sg = 0;
#pragma unroll 1
            for (int c = 1; c < 7; ++c) {
                ++ch; if (ch == 7) ch = 0;
                ++sg; if (sg == 3) sg = 0;
                const int st = lbase + RING + sg * STAGE;
#pragma unroll
                for (int i = 0; i < 4; ++i) dma16(rx, st + w4 * 4096 + i * 1024, (i & 1) ? vx1 : vx0, sx + (w4 * 4 + i) * 7168 + ch * 128);
#pragma unroll
                for (int cb = 0; cb < 2; ++cb)
#pragma unroll
                    for (int i = 0; i < 2; ++i) dma16(rw, st + 16384 + cb * 8192 + w4 * 2048 + i * 1024, vw, sw + (ch * 64 + w4 * 16 + i * 8) * 2688 + cb * 896);
                WAITV(8); BAR();
            }
#pragma unroll 1
            for (int c = 0; c < 7; ++c) {
                ++ch; if (ch == 7) ch = 0;
#pragma unroll
                for (int i = 0; i < 2; ++i) dma16(rw, lbase + LA + c * 8192 + w4 * 2048 + i * 1024, vw, sw + (ch * 64 + w4 * 16 + i * 8) * 2688 + 1792);
                if (c == 3) { WAITV(8); BAR(); }
            }
            BAR();
        }
        make_tag(); use_tab();
        asm volatile("" : "+s"(tb));
        const int jo = j * (4 * XT), sl = tb8[E_SL - 16];
        int ra0 = tb[E_RA0] + jo, ra1 = tb[E_RA1] + jo, ra2 = tb[E_RA2] + jo, rb0 = tb[E_RB0] + jo, rb1 = tb[E_RB1] + jo, rq = tb[E_RQ] + jo, lh = lbase + LR + w4 * 4096;
        asm volatile("" : "+s"(ra0), "+s"(ra1), "+s"(ra2), "+s"(rb0), "+s"(rb1), "+s"(rq), "+s"(lh));
        const int lb0 = lbase + tb[E_LB0], lb1 = lbase + tb[E_LB1];
        const int hd[2] = {tb[E_HD0], tb[E_HD1]}, cf[2] = {tb[E_CF0], tb[E_CF1]}, po[2] = {tb8[E_PO0 - 16], tb8[E_PO1 - 16]};
        const u64 mb = (u64)(unsigned)tb[E_MB] << 32, mall = mb | (unsigned)tb[E_MA];
        const char* f1 = ws + (WS_F1 + j * FS + tb[E_PG0]) + ((hh * 16 - (r3 & 3)) * FS + (lane & 4) * tb[E_PGD] + p * 8);
        int l16 = lane * 16;
        PIN(f1); PIN(l16);
        int lz = lane; PIN(lz);
        const int g4 = lz >> 4, c16 = lz & 15;
        int fo[2], ko[2], qo[2], vo[2][2];
        f32x4 md, mo;
        const bool pt = x7 || g4 == sl;
#pragma unroll
        for (int e = 0; e < 2; ++e) fo[e] = g4 * 512 + (((c16 >> 1) ^ ((cf[e] + g4) & 3)) << 6) + (c16 & 1) * 32;
        int qs = pt ? fo[1] : OOB; PIN(qs);
        if (!local) {
#pragma unroll
            for (int e = 0; e < 2; ++e) { ko[e] = LW + 40960 - w4 * 8192 + po[e] + fo[e]; qo[e] = LW + w4 * 4096 + po[e] + fo[e]; PIN(ko[e]); PIN(qo[e]); }
#pragma unroll
            for (int r = 0; r < 4; ++r) {
                const int d = c16 - 4 * g4 - r;
                md[r] = (unsigned)d < 8u ? 0.f : -1.0e30f; mo[r] = (d < -8 && T) ? 0.f : -1.0e30f;
            }
            PIN(md); PIN(mo);
        }
        if (ty < 7) { WAITV(6); BAR(); }
        if (!local) {
            PIN(lz);
#pragma unroll
            for (int e = 0; e < 2; ++e)
#pragma unroll
                for (int s = 0; s < 2; ++s) {
                    const int pc8 = (2 * (lz & 3) + s + (hd[e] & 1)) & 7, cc = pc8 >> 1;
                    vo[e][s] = LW + 40960 - w4 * 8192 + 4096 + po[e] + cc * 512 + (((lz >> 3) ^ ((cf[e] + cc) & 3)) << 6) + (q & 1) * 32 + (pc8 & 1) * 8; PIN(vo[e][s]);
                }
        }

        if (wave == 4 && lane < 2) *(volatile LAS u64*)(wd + lane * 16) = 0;
        asm volatile("s_waitcnt vmcnt(0) lgkmcnt(0)" ::: "memory"); BAR();
        STAMP(4);
        const u32x4 rxx = srd(ws + WS_X, X_BYTES);
        auto tell = [&](int n) { if (lane == 0) *(volatile LAS u64*)(n < 2 ? wd + n * 16 : wk + 64) = FTAG; };
        auto hear = [&](int n) {
#pragma unroll 1
            for (int i = 0; i < SPIN_MAX * 4; ++i) { if (__all(*(volatile LAS u64*)(n < 2 ? wd + n * 16 : wk + 64) == FTAG)) break; if (HSL) __builtin_amdgcn_s_sleep(HSL); }
            asm volatile("" ::: "memory");
        };
        auto fetch_b = [&](int v) {
            const int go = v * (56 * 512), lo = v * 4096;
            if (x7) { dma32_dev(rxx, lb0 + lo, l16, rb0 + go); dma32_dev(rxx, lb1 + lo, l16, rb1 + go); }
            else {
                if (w4 == 0) dma64_dev(rxx, lb1 + lo, l16, rb1 + go);
                if (sl != 8 && lane < 32) { dma16_dev(rxx, lb0 + lo, l16, rb0 + go); if (w4) dma16_dev(rxx, lb1 + lo, l16, rb1 + go); }
            }
        };
        auto fetch_a = [&](int v) {
            const int go = v * (56 * 512), lo = v * 2048;
            dma32_dev(rxx, lh + 32768 + lo, l16, ra0 + go);
            dma32_dev(rxx, lh + 16384 + lo, l16, ra1 + go);
            dma32_dev(rxx, lh + lo, l16, ra2 + go);
        };
        STAMP(5);
        unsigned left = 1;
        if (!local) { if (wave == 4) { left = __builtin_amdgcn_readfirstlane((unsigned)poll<false>(f1, mall, mb, tag)); tell(0); if (!left) tell(1); } else hear(0); }
        STAMP(2);
        u32x4 qp[2];
        asm volatile("buffer_load_dwordx4 %0, %2, %3, %4 offen sc1\n\tbuffer_load_dwordx4 %1, %2, %3, %4 offen offset:16 sc1"
                     : "=&v"(qp[0]), "=&v"(qp[1]) : "v"(qs), "s"(rxx), "s"(rq) : "memory");
        if (!local) fetch_b(0);
        if (wave == 4) { if (left) { poll<false>(f1, mall ^ mb, mall ^ mb, tag); tell(1); } } else hear(1);
        STAMP(6);
        dma32_dev(rxx, lh + 2048, l16, ra0 - 56 * 512);
        fetch_a(0);
        WAITV(8);
        PIN(qp[0]); PIN(qp[1]);
        STAMP(3);
        bf16x8 pe[2][2];
        float ls[2][2];
        if (!local) {
            f32x4 sd[2][2], so[2][2];
#pragma unroll
            for (int e = 0; e < 2; ++e)
#pragma unroll
                for (int y = 0; y < 2; ++y) {
                    const char* kb = smem + ko[e] + y * 16;
                    u32x4 qv = *(const u32x4*)(smem + qo[e] + y * 16);
                    const bool a = pt && (e || x7), z0 = (hd[e] & 1) && g4 == 0, z1 = !(hd[e] & 1) && g4 == 3;
                    qv = (u32x4){a ? qp[y].x : qv.x, a ? qp[y].y : qv.y, a ? qp[y].z : qv.z, a ? qp[y].w : qv.w};
                    qv = (u32x4){z0 ? 0u : qv.x, z0 ? 0u : qv.y, z1 ? 0u : qv.z, z1 ? 0u : qv.w};
                    sd[e][y] = MFMA16(*(const bf16x8*)kb, __builtin_bit_cast(bf16x8, qv), md);
                    so[e][y] = MFMA16(*(const bf16x8*)(kb + 8192), __builtin_bit_cast(bf16x8, qv), mo);
                }
            const bf16x8 one = __builtin_bit_cast(bf16x8, (u32x4){0x3F803F80u, 0x3F803F80u, 0x3F803F80u, 0x3F803F80u});
            float m[4];
#pragma unroll
            for (int i = 0; i < 4; ++i) {
                const f32x4 a = sd[i >> 1][i & 1], c = so[i >> 1][i & 1];
                m[i] = fmx(fmx(fmx(fmx(a[0], a[1]), a[2]), fmx(fmx(a[3], c[0]), c[1])), fmx(c[2], c[3]));
            }
#pragma unroll
            for (int i = 0; i < 4; ++i) {
                const unsigned mu = __builtin_bit_cast(unsigned, m[i]);
                const auto r = __builtin_amdgcn_permlane32_swap(mu, mu, false, false);
                const unsigned r0 = r[0], r1 = r[1];
                m[i] = fmx(__builtin_bit_cast(float, r0), __builtin_bit_cast(float, r1));
            }
#pragma unroll
            for (int i = 0; i < 4; ++i) {
                const unsigned mu = __builtin_bit_cast(unsigned, m[i]);
                const auto r = __builtin_amdgcn_permlane16_swap(mu, mu, false, false);
                const unsigned r0 = r[0], r1 = r[1];
                m[i] = fmx(__builtin_bit_cast(float, r0), __builtin_bit_cast(float, r1));
            }
            SCHED();
            f32x4 sm[4];
#pragma unroll
            for (int i = 0; i < 4; ++i) {
                const f32x4 a = sd[i >> 1][i & 1], c = so[i >> 1][i & 1];
                f32x8 x = {a[0], a[1], a[2], a[3], c[0], c[1], c[2], c[3]};
                x -= m[i];
#pragma unroll
                for (int r = 0; r < 8; ++r) x[r] = __builtin_amdgcn_exp2f(x[r]);
                pe[i >> 1][i & 1] = __builtin_convertvector(x, bf16x8);
                sm[i] = MFMA16(one, pe[i >> 1][i & 1], (f32x4){});
                SCHED();
            }
#pragma unroll
            for (int i = 0; i < 4; ++i) ls[i >> 1][i & 1] = __builtin_amdgcn_rcpf(sm[i][0]);
#pragma unroll
            for (int i = 0; i < 4; ++i) { PIN(pe[i >> 1][i & 1]); PIN(ls[i >> 1][i & 1]); }
        }
        STAMP(10);
        asm volatile("s_waitcnt vmcnt(0) lgkmcnt(0)" ::: "memory");
        STAMP(7);
        __builtin_amdgcn_s_setprio(2);
        if (wave == 4) { poll<true>(f1 + 32 * FS, mall, mall, tag); tell(2); }
        else {
            BAR(); BAR();
            dma_rows<14>(rwo, lbase + LA + w4 * 1024, vwo, w4 * 7168 + (ty & 6) * 128);
            hear(2);
        }
        STAMP(14);
        fetch_a(1);
        if (!local) fetch_b(1);
        if (!YEARLY) dma_rows<YB0>(rwo, lbase + LA + w4 * 1024, vwo, w4 * 7168 + (ty & 6) * 128);
        if (!local && (x7 || w4 == 0)) WAITV(4); else WAITV(0);
        ones(0, true); ones(2, false); WAITL0(); arrive(0);
        WAITV(0);
        if (!YEARLY) dma_rows<14 - YB0>(rwo, lbase + LA + (w4 + 4 * YB0) * 1024, vwo, (w4 + 4 * YB0) * 7168 + (ty & 6) * 128);
        else if (wave == 4) dma_rows<14>(rwo, lbase + LA, vwo, (ty & 6) * 128);
        STAMP(9);
        if (!local) {
            u32x4 ov[2][2];
#pragma unroll
            for (int e = 0; e < 2; ++e)
#pragma unroll
                for (int y = 0; y < 2; ++y) {
                    const char* v0 = smem + vo[e][0] + y * 16; const char* v1 = smem + vo[e][1] + y * 16;
                    const f32x4 a = MFMA16(tr_frag(v0, v0 + 8192), pe[e][y], (f32x4){}), c = MFMA16(tr_frag(v1, v1 + 8192), pe[e][y], (f32x4){});
                    f32x8 x = {a[0], a[1], a[2], a[3], c[0], c[1], c[2], c[3]};
                    x *= ls[e][y];
                    ov[e][y] = __builtin_bit_cast(u32x4, __builtin_convertvector(x, bf16x8));
                }
#pragma unroll
            for (int e = 0; e < 2; ++e)
#pragma unroll
                for (int y = 0; y < 2; ++y) store_dev(ov[e][y], ros, g4 * 512 + c16 * 16 + y * 256, ((64 + T) * 16 + hd[e]) * 2048);
        }
        STAMP(11);
        __builtin_amdgcn_s_setprio(0);
        if (!local) { arrive(8); WAITV(4); arrive(16); await4(8); } else { WAITV(0); arrive(16); }
        dma_rows<6>(rwo, lbase + LW + w4 * 1024, vwo, ty < 6 ? w4 * 7168 + (ty | 1) * 128 : OOB);
        if (!local) { WAITV(6); if (lane < 2) raise(ws + WS_F2 + (32 + (T >> 1)) * FS + (T & 1) * 128 + (lane ? hd[1] : hd[0]) * 8, tag); }
        if (ty < 6) dma_rows<8>(rwo, lbase + LW + (w4 + 24) * 1024, vwo, (w4 + 24) * 7168 + (ty | 1) * 128);
        if (b == 0) {
            poll<false>(ws + WS_F1 + (w4 * 8 + r3) * FS + u * 32, ~0ull, ~0ull, tag);
            arrive(12);
            if (wave == 4) { await4(12); if (lane == 0) __hip_atomic_store((gu64*)ws, tag, RLX); }
        }
        WAITV(0);
    } else {
        mask_bits();
        if (ty < 7) {
            int xo[2][4], wo_[2][2];
#pragma unroll
            for (int g = 0; g < 2; ++g) {
#pragma unroll
                for (int ks = 0; ks < 4; ++ks) { xo[g][ks] = RING + g * 65536 + (w4 * 32 + t) * 128 + (((2 * ks + hh) ^ ((t >> 1) & 7)) << 4); PIN(xo[g][ks]); }
                wo_[g][0] = RING + g * 65536 + wb0; wo_[g][1] = RING + g * 65536 + wb1; PIN(wo_[g][0]); PIN(wo_[g][1]);
            }
            int es[2], el[2][2], g0;
            unsigned pq, pk;
            char* f1;
            auto put = [&](const f32x16& a0, const f32x16& a1, int sec, unsigned pub, u32x4 (&d)[2][2]) {
#pragma unroll
                for (int e = 0; e < 2; ++e)
#pragma unroll
                    for (int s = 0; s < 2; ++s) {
                        d[e][s] = __builtin_bit_cast(u32x4, pack8(e ? a1 : a0, s));
                        if ((pub >> (2 * e + s)) & 1) store_dev(d[e][s], rxs, es[s] + e * 2048, g0 + sec * (56 * 512));
                    }
            };
            auto keep = [&](int lo, const u32x4 (&d)[2][2]) {
#pragma unroll
                for (int e = 0; e < 2; ++e)
#pragma unroll
                    for (int s = 0; s < 2; ++s) if (el[e][s] >= 0) *(u32x4*)(smem + el[e][s] + lo) = d[e][s];
            };
            f32x16 acc[4];
            bf16x8 xf[28], wf[2][4];
            const f32x16 zero = {};
            WAITV(0); BAR();
            STAMP(1); CST(20);
            auto rd = [&](int n) {
                const int sg = (n >> 2) % 3, ks = n & 3;
                const char* st = smem + (sg & 1) * STAGE;
#pragma unroll
                for (int i = 3; i >= 0; --i) {
                    const char* a = st + wo_[sg >> 1][i & 1] + 16384 + (i >> 1) * 8192 + ks * 2048;
                    wf[n & 1][i] = tr_frag(a, a + 512);
                    if (i == 3) xf[n] = *(const bf16x8*)(st + xo[sg >> 1][ks]);
                }
            };
            rd(0);
            SCHED();
#pragma unroll
            for (int n = 0; n < 28; ++n) {
                if (n == 11) {
                    make_tag(); use_tab();
#pragma unroll
            for (int s = 0; s < 2; ++s) {
                const int sw16 = ((((t >> 2) ^ (2 * s + hh)) << 2) + (t & 3)) * 16;
                es[s] = (2 * s + hh) * 512 + sw16; PIN(es[s]);
#pragma unroll
                for (int e = 0; e < 2; ++e) {
                    const int ps = tb8[E_PS - 16] + 4 * e + 2 * s + hh;
                    el[e][s] = (unsigned)ps < 8u ? ps * 512 + sw16 : -1; PIN(el[e][s]);
                }
            }
            pq = tb8[E_PQ - 16]; pk = tb8[E_PK - 16];
            g0 = (br * 64 + T) * XT + ty * 4096;
            f1 = ws + WS_F1 + (br * 16 + j) * FS + ty * 32 + w4 * 8;
            asm volatile("" : "+s"(g0), "+s"(f1));
                    SCHED();
                }
                if ((n & 3) == 3) { WAITL0(); BAR(); SCHED(); }
                if (n < 27) rd(n + 1);
#pragma unroll
                for (int i = 3; i >= 0; --i) acc[i] = MFMA(wf[n & 1][i], xf[n], n ? acc[i] : zero);
                if (n < 27) { SG(0x8, 1); SG(0x100, 3); SG(0x8, 1); SG(0x100, 3); SG(0x8, 1); SG(0x100, 3); SG(0x8, 1); }
                SCHED();
            }
            STAMP(2);
            const float qs = 0.18898223650461361f * 1.4426950408889634f;
            {
                u32x4 dk[2][2], dq[2][2];
                put(acc[2], acc[3], 1, pk, dk);
                put(acc[0] * qs, acc[1] * qs, 0, pq, dq);
                if (br) { keep(LW + 40960 - w4 * 8192, dk); keep(LW + w4 * 4096, dq); }
            }
            STAMP(3);
            BAR();
            bf16x8 wv[8];
            u32x4 dv[2][2];
            auto rv = [&](int n) { const char* a = smem + LA + (n % 28) * 2048 + (n >= 28 ? wb1 : wb0); wv[n & 7] = tr_frag(a, a + 512); };
            auto putv = [&](int e, int s) {
                dv[e][s] = __builtin_bit_cast(u32x4, pack8(acc[2 + e], s));
                store_dev(dv[e][s], rxs, es[s] + e * 2048, ((pk >> (2 * e + s)) & 1) ? g0 + 112 * 512 : OOB);
            };
#pragma unroll
            for (int n = 0; n < 6; ++n) rv(n);
            PIN(nd); PIN(no); PIN(lm);
            SCHED();
#pragma unroll
            for (int n = 0; n < 56; ++n) {
                if (n == BV1) { BAR(); SCHED(); }
                if (n == RAISE_B || n == RAISE_A) { if ((n == RAISE_B) == (br != 0)) { WAITV(0); if (lane == 0) raise(f1, tag); STAMP(9); } SCHED(); }
                acc[2 + n / 28] = MFMA(wv[n & 7], xf[n % 28], n % 28 ? acc[2 + n / 28] : zero);
                if (n + 6 < 56) rv(n + 6);
                if (n >= 36 && n < 52) mask_reg(n - 36);
                SG(0x8, 1); SG(0x100, 2); SG(0x2, 8);
                SCHED();
                if (n == 30 || n == 32) { putv(0, (n - 30) >> 1); SCHED(); }
            }
            STAMP(4); CST(21);
            putv(1, 0); putv(1, 1);
            if (br) keep(LW + 45056 - w4 * 8192, dv);
            STAMP(5);
            WAITV(0);
            if (lane == 0) raise(f1 + 32 * FS, tag);
        } else {
            make_tag(); use_tab();
            BAR();
            if (threadIdx.x == 0) raise(ws + WS_F1 + (br * 16 + j) * FS + 7 * 32, tag);
#pragma unroll
            for (int r = 0; r < 16; ++r) mask_reg(r);
        }
        const char* tl = smem + LR + w4 * 4096;
        int fo[2] = {koff(c0, 0), koff(c0, 1)}, vo[2] = {voff(h, c0, 0) + 2048, voff(h, c0, 1) + 2048};
        PIN(fo[0]); PIN(fo[1]); PIN(vo[0]); PIN(vo[1]);
        STAMP(6);
        WAITL0(); BAR();
        STAMP(7);
        __builtin_amdgcn_s_setprio(1);
        u32x4 qf[2];
        qf[0] = *(const u32x4*)(tl + 2048 + fo[0]); qf[1] = *(const u32x4*)(tl + 2048 + fo[1]);
        auto kfrag = [&](int n, int s) { return *(const bf16x8*)(tl + (8 - n) * 4096 + fo[s]); };
        auto vfrag = [&](int n, int s) { const char* vb = tl + (8 - n) * 4096 + s * 256; return tr_frag(vb + vo[0], vb + vo[1]); };
        bf16x8 k0 = kfrag(0, 0), k1 = kfrag(0, 1), k2 = kfrag(8, 0), k3 = kfrag(8, 1);
        WAITL0(); BAR();
        CST(19); STAMP(10);
        qfix(h, qf[0], qf[1]);
        const bf16x8 q0 = __builtin_bit_cast(bf16x8, qf[0]), q1 = __builtin_bit_cast(bf16x8, qf[1]);
        auto qk = [&](int n, const f32x16& c) { f32x16 r = MFMA(kfrag(n, 0), q0, c); return MFMA(kfrag(n, 1), q1, r); };
        f32x16 cm, sa, sb;
        bf16x8 pp[8][2];
        cd = MFMA(k0, q0, cd); co = MFMA(k2, q0, co); cd = MFMA(k1, q1, cd); co = MFMA(k3, q1, co);
        k0 = kfrag(1, 0); k1 = kfrag(1, 1);
#pragma unroll
        for (int r = 0; r < 16; ++r) cd[r] = fmx(cd[r], co[r]);
        {
            const float m = -rowmax(cd);
#pragma unroll
            for (int r = 0; r < 16; ++r) cm[r] = m;
        }
        cd += cm;
        sa = cd;
        SCHED();
#define STG(n, cur, nxt) do { \
            if (n < 7) { nxt = MFMA(k0, q0, cm); nxt = MFMA(k1, q1, nxt); } \
            if (n < 6) { k0 = kfrag(n + 2, 0); k1 = kfrag(n + 2, 1); } \
            ex(cur); pp[n][0] = pack8(cur, 0); pp[n][1] = pack8(cur, 1); \
            if (n < 7) SG(0x8, 1); SG(0x400, 4); if (n < 7) SG(0x8, 1); if (n < 6) SG(0x100, 2); SG(0x400, 4); SG(0x2, 2); SG(0x400, 4); SG(0x2, 2); SG(0x400, 4); SG(0x2, 4); SCHED(); } while (0)
        STG(0, sa, sb); STG(1, sb, sa); STG(2, sa, sb); STG(3, sb, sa); STG(4, sa, sb); STG(5, sb, sa); STG(6, sa, sb); STG(7, sb, sa);
        bf16x8 pz[2];
#pragma unroll
        for (int s = 0; s < 2; ++s) {
            const u32x4 e = __builtin_bit_cast(u32x4, pp[0][s]);
            pp[0][s] = __builtin_bit_cast(bf16x8, e & pm[s]); pz[s] = __builtin_bit_cast(bf16x8, e & ~pm[s]); PIN(pz[s]);
        }
#pragma unroll
        for (int n = 0; n < 8; ++n) { PIN(pp[n][0]); PIN(pp[n][1]); }
        char* f2 = ws + WS_F2 + (T >> 1) * FS + (T & 1) * 128 + h * 8;
        int orec = (T * 16 + h) * 2048;
        const char* va = tl + vo[0]; const char* vb = tl + vo[1];
        asm volatile("" : "+s"(f2), "+s"(orec));
        STAMP(8);
        await4(0);
        STAMP(14);
        f32x16 o = {};
        auto pv = [&](int n, bf16x8 p0, bf16x8 p1) { o = MFMA(vfrag(n, 0), p0, o); o = MFMA(vfrag(n, 1), p1, o); };
        {
            bf16x8 vf[18];
            auto rdv = [&](int i) { const int off = (8 - (i >> 1)) * 4096 + (i & 1) * 256; vf[i] = tr_frag(va + off, vb + off); };
#pragma unroll
            for (int i = 0; i < 5; ++i) { rdv(i); SCHED(); }
#pragma unroll
            for (int i = 0; i < 18; ++i) {
                if (PPV && i == 5) { await4(20); SCHED(); }
                o = MFMA(vf[i], i < 16 ? pp[i >> 1][i & 1] : pz[i & 1], o);
                if (i + 5 < 18) rdv(i + 5);
                SCHED();
            }
        }
        auto publish = [&]() {
            const float lf = o[12];
            const unsigned lu = __builtin_bit_cast(unsigned, lf);
            const unsigned lx = __builtin_amdgcn_permlane32_swap(lu, lu, false, false)[1];
            put_o(o, __builtin_bit_cast(float, lx), orec);
        };
        publish();
        STAMP(11); CST(22);
        float big = fmx(__builtin_fabsf(o[0]), __builtin_fabsf(o[1]));
#pragma unroll
        for (int r = 2; r < 16; r += 2) big = fmx(fmx(big, __builtin_fabsf(o[r])), __builtin_fabsf(o[r + 1]));
        if (__builtin_expect(!__all(big < 1.0e36f), 0)) {
            const int nmid = T >= 7 ? 7 : T;
            float m = 0.f;
#pragma unroll 1
            for (int i = 1; i <= nmid; ++i) m = fmx(m, rowmax(qk(i, cm)));
            cd -= m; cm -= m;
            ex(cd);
            o = (f32x16){};
            const u32x4 e0 = __builtin_bit_cast(u32x4, pack8(cd, 0)), e1 = __builtin_bit_cast(u32x4, pack8(cd, 1));
            pv(0, __builtin_bit_cast(bf16x8, e0 & pm[0]), __builtin_bit_cast(bf16x8, e1 & pm[1]));
            pv(8, __builtin_bit_cast(bf16x8, e0 & ~pm[0]), __builtin_bit_cast(bf16x8, e1 & ~pm[1]));
#pragma unroll 1
            for (int i = 1; i <= nmid; ++i) {
                f32x16 s = qk(i, cm);
                ex(s); pv(i, pack8(s, 0), pack8(s, 1));
            }
            WAITV(0);
            publish();
        }
        if (RDFIRST) { await4(16); read_wo(LA); SCHED(); }
        WAITV(0);
        if (lane == 0) raise(f2, tag);
        __builtin_amdgcn_s_setprio(0);
        STAMP(12);
        if (!RDFIRST) { await4(16); read_wo(LA); }
    }
    STAMP(13);
    const int To = br * 64 + 4 * j + 2 * (ty & 1);
    const int tr = br ? 2 * (t & 15) + (t >> 4) : t;
    const int tb = LR + 32768 + ((wave >> 2) * 2 + (w4 & 1)) * 4096;
    int l16 = lane * 16, oa = (w4 & 1) * LR + hh * 2048 + t * 16, tw = tb + tr * 128 + (((4 * (w4 >> 1) + hh) ^ (tr & 7)) << 4), tg = tb + (w4 >> 1) * 2048 + r3 * 128 + ((u ^ r3) << 4);
    int ov = r3 * 1792 + u * 16;
    int oo = ((4 * j + 2 * (ty & 1) + (w4 & 1)) * 32 + (w4 >> 1) * 16) * 1792 + br * 896 + ((ty & 6) + (wave >> 2)) * 128;
    const int os = (To * 16 + w4) * 2048, ol = lbase + w4 * 2048;
    if (F64) { tg = tb + (lane >> 2) * 128 + (((4 * (w4 >> 1) + p) ^ ((lane >> 2) & 7)) << 4); ov = (lane >> 2) * 1792 + p * 16; oo += (w4 >> 1) * (64 - 16 * 1792); }
    asm volatile("" : "+v"(l16), "+v"(oa), "+v"(tw), "+v"(tg), "+v"(ov)); asm volatile("" : "+s"(oo));
    const __amdgpu_buffer_rsrc_t rout = rsrc(out, 2048 * 1792);
    const u32x4 roo = srd(ws + WS_O, O_BYTES);
    const bool idle = aux && ty >= 6;
    if (wave == 0) poll<false>(ws + WS_F2 + (To >> 1) * FS + lane * 8, 0xFFFFFFFFull, 0xFFFFFFFFull, tag);
    WAITL0(); BAR();
    STAMP(15);
    if (idle) { STAMP_FLUSH(); return; }
    auto geto = [&](int qd) { dma32_dev(roo, ol + qd * 8192, l16, os + qd * 8192); dma32_dev(roo, ol + qd * 8192 + LR, l16, os + qd * 8192 + 32768); };
    f32x16 acc = {};
    {
        bf16x8 of[2][7];
        auto rdo = [&](int qd) {
            const char* a = smem + oa + qd * 8192;
#pragma unroll
            for (int i = 0; i < 6; ++i) of[qd & 1][i] = *(const bf16x8*)(a + (i / 3) * 4096 + (i % 3) * 512);
            const u32x2 lo = *(const u32x2*)(a + 1536), hi = *(const u32x2*)(a + 4096 + 1536);
            of[qd & 1][6] = __builtin_bit_cast(bf16x8, (u32x4){lo.x, lo.y, hi.x, hi.y});
        };
        auto mmo = [&](int qd, int i0, int i1) {
#pragma unroll
            for (int i = i0; i < i1; ++i) acc = MFMA(wo[qd * 7 + i], of[qd & 1][i], acc);
            SCHED();
        };
        if (aux) read_wo(LW); else { geto(0); geto(1); geto(2); WAITV(8); }
        BAR(); rdo(0); SCHED();
        if (!aux) geto(3);
        mmo(0, 0, 3);
        WAITV(8);
        BAR(); rdo(1); SCHED();
        mmo(0, 3, 7); mmo(1, 0, 3);
        WAITV(4);
        BAR(); rdo(2); SCHED();
        mmo(1, 3, 7); mmo(2, 0, 3);
        WAITV(0);
        BAR(); rdo(3); SCHED();
        mmo(2, 3, 7); mmo(3, 0, 7);
    }
    STAMP(16);
    {
#pragma unroll
        for (int s = 0; s < 2; ++s) *(bf16x8*)(smem + (tw ^ (s << 5))) = pack8(acc, s);
        WAITL0(); if (!F64) BAR();
        const u32x4 v0 = *(const u32x4*)(smem + tg), v1 = *(const u32x4*)(smem + tg + (F64 ? 2048 : 1024));
        SCHED();
        store_dev(v0, rout, ov, oo); store_dev(v1, rout, ov, oo + (F64 ? 16 : 8) * 1792);
    }
    STAMP(17);
    STAMP_FLUSH();
}

extern "C" __device__ __attribute__((noinline, used)) void fused_end() {}

extern "C" void kernel_launch(void* const* d_in, const int* in_sizes, int n_in, void* d_out, int out_size, void* d_ws, size_t ws_size, hipStream_t stream) {
    fused<<<256, 512, 0, stream>>>((const char*)d_in[0], (const char*)d_in[1], (const char*)d_in[2], (const char*)d_in[3], (const char*)d_in[4], (const char*)d_in[5], (char*)d_out, (char*)d_ws);
}
```

```cpp
#include <hip/hip_runtime.h>
#include <hip/hip_bf16.h>

typedef __bf16 bf16x8 __attribute__((ext_vector_type(8)));
typedef short s16x4 __attribute__((ext_vector_type(4)));
typedef float f32x16 __attribute__((ext_vector_type(16)));
typedef float f32x8 __attribute__((ext_vector_type(8)));
typedef float f32x4 __attribute__((ext_vector_type(4)));
typedef unsigned u32x4 __attribute__((ext_vector_type(4)));
typedef unsigned u32x2 __attribute__((ext_vector_type(2)));
typedef unsigned long long u64;
typedef __attribute__((address_space(1))) u64 gu64;
#define LAS __attribute__((address_space(3)))
#define DI __device__ __forceinline__
#define MFMA(a, b, c) __builtin_amdgcn_mfma_f32_32x32x16_bf16((a), (b), (c), 0, 0, 0)
#define MFMA16(a, b, c) __builtin_amdgcn_mfma_f32_16x16x32_bf16((a), (b), (c), 0, 0, 0)
#define RLX __ATOMIC_RELAXED, __HIP_MEMORY_SCOPE_AGENT

constexpr int XB = 2048 * 896, WB = 448 * 2688, OB = 448 * 896;
constexpr int STAGE = 32768, RING = 65536, LA = 0, LW = 57344, LR = 114688, LX = 106496, LDS_BYTES = 163840;
#define FS 4352
constexpr int WS_F1 = 4096, WS_F2 = WS_F1 + 64 * FS, WS_X = 1 << 20, XT = 168 * 512, X_BYTES = 2 * 64 * XT, WS_O = 16 << 20, O_BYTES = 2 * 64 * 16 * 2048;
constexpr int OOB = 0x40000000;
#define ROT 2
#define PF0 2
#define RAISE_A 27
#define RAISE_B 4
#define BV1 10
#define KH_DELAY 8
#define OPRE 10
#define OROT 0
#define XB0 5
#define YB0 3
#define YEARLY 1
#define HSL 0
#define F64 0
#define PPV 0
#define RDFIRST 1
#define XROT 0
#define PROT 1
constexpr int SPIN_MAX = 1 << 18;

enum { E_RA0, E_RA1, E_RA2, E_RB0, E_RB1, E_RQ, E_LB0, E_LB1, E_MA, E_MB, E_PG0, E_PGD, E_HD0, E_HD1, E_CF0, E_CF1, E_PO0, E_PO1, E_SL, E_H, E_C0, E_PS, E_PQ, E_PK };
struct Tab {
    int e[192][32];
    constexpr Tab() : e() {
        for (int i = 0; i < 192; ++i) {
            const int w4 = i & 3, ty = (i >> 2) & 7, br = (i >> 5) & 1, jc = i >> 6;
            const bool x7 = ty == 7, local = br == 0 && !x7;
            const int ha = (0x4ECA7530u >> (4 * ty)) & 15, hb = (0xBFD98621u >> (4 * ty)) & 15;
            const int ca = (7 * ha) >> 1, cb = (7 * hb) >> 1, cs = ca < cb ? ca : cb;
            const int h = br ? hb : ha, c0 = br ? cb : ca;
            const int nb = x7 ? 15 : (0xF63F30Fu >> (4 * ty)) & 15;
            const int sl = (ty == 2 || ty == 5) ? 3 : 0;
            const int two = (x7 || (br && nb != 15)) ? 0xFF : 0x0F;
            const int rt = (64 + w4) * XT;
            int* v = e[i];
            v[E_HD0] = x7 ? h : ha; v[E_HD1] = x7 ? h : hb; v[E_CF0] = x7 ? c0 : ca; v[E_CF1] = x7 ? c0 : cb;
            v[E_PO0] = x7 ? 0 : (ca - cs) * 512; v[E_PO1] = x7 ? 0 : (cb - cs) * 512;
            v[E_RA0] = w4 * XT + (56 + c0) * 512; v[E_RA1] = jc >= 1 ? v[E_RA0] - 4 * XT : OOB; v[E_RA2] = jc >= 2 ? v[E_RA0] - 8 * XT : OOB;
            v[E_RB0] = rt + (56 + (x7 ? c0 : cb + sl)) * 512;
            v[E_RB1] = (jc == 0 && w4 == 0) ? OOB : ((!x7 && w4 == 0) ? rt + (56 + cs) * 512 : v[E_RB0]) - XT;
            v[E_RQ] = local ? OOB : rt + v[E_CF1] * 512;
            v[E_LB0] = LW + 40960 - w4 * 8192 + (x7 ? 0 : v[E_PO1] + sl * 512); v[E_LB1] = (!x7 && w4 == 0) ? LX : v[E_LB0] + 8192;
            v[E_MA] = two | (jc >= 1 ? two << 8 : 0) | (jc >= 2 ? two << 16 : 0);
            v[E_MB] = local ? 0 : (x7 ? 0xFF : two & 0xF0) | (jc ? (two & 0x88) << 8 : 0);
            v[E_PG0] = (x7 ? 1 + 3 * br : ty) * 32; v[E_PGD] = (x7 ? 1 : (nb == 15 ? 0 : nb - ty)) * 8;
            v[E_SL] = nb == 15 ? 8 : sl;
            v[E_H] = h; v[E_C0] = c0; v[E_PS] = 8 * ty - cs;
            v[E_PQ] = br ? (0x1189188u >> (4 * ty)) & 15 : 15; v[E_PK] = w4 != 3 ? v[E_PQ] : 15;
        }
    }
};
__device__ const Tab g_tab = Tab();

#define STAMP(i)
#define CST(i)
#define STAMP_DECL
#define STAMP_FLUSH()

DI u32x4 srd(const void* p, unsigned bytes) { const u64 a = (u64)p; return (u32x4){(unsigned)a, (unsigned)(a >> 32) & 0xffffu, bytes, 0x00020000u}; }
DI __amdgpu_buffer_rsrc_t rsrc(const void* p, int bytes) { return __builtin_amdgcn_make_buffer_rsrc((void*)p, 0, bytes, 0x00020000); }
DI void dma16(u32x4 r, int lds, int voff, int soff) {
    asm volatile("s_mov_b32 m0, %0\n\ts_nop 0\n\tbuffer_load_dwordx4 %1, %2, %3 offen lds" :: "s"(lds), "v"(voff), "s"(r), "s"(soff) : "memory");
}
DI void dma16_dev(u32x4 r, int lds, int voff, int soff) {
    asm volatile("s_mov_b32 m0, %0\n\ts_nop 0\n\tbuffer_load_dwordx4 %1, %2, %3 offen sc1 lds" :: "s"(lds), "v"(voff), "s"(r), "s"(soff) : "memory");
}
DI void dma64_dev(u32x4 r, int lds, int voff, int soff) {
    asm volatile("s_mov_b32 m0, %0\n\ts_nop 0\n\tbuffer_load_dwordx4 %1, %2, %3 offen sc1 lds\n\tbuffer_load_dwordx4 %1, %2, %3 offen offset:1024 sc1 lds\n\t"
                 "buffer_load_dwordx4 %1, %2, %3 offen offset:2048 sc1 lds\n\tbuffer_load_dwordx4 %1, %2, %3 offen offset:3072 sc1 lds" :: "s"(lds), "v"(voff), "s"(r), "s"(soff) : "memory");
}
DI void dma32_dev(u32x4 r, int lds, int voff, int soff) {
    asm volatile("s_mov_b32 m0, %0\n\ts_nop 0\n\tbuffer_load_dwordx4 %1, %2, %3 offen sc1 lds\n\tbuffer_load_dwordx4 %1, %2, %3 offen offset:1024 sc1 lds"
                 :: "s"(lds), "v"(voff), "s"(r), "s"(soff) : "memory");
}
DI void store_dev(u32x4 v, __amdgpu_buffer_rsrc_t r, int voff, int soff) {
    __builtin_amdgcn_raw_buffer_store_b128(v, r, voff, soff, 16);
    asm volatile("s_nop 1" :: "v"(v) : "memory");
}
template <int N>
DI void dma_rows(u32x4 r, int lds, int voff, int soff) {
    asm volatile("s_mov_b32 m0, %0\n\ts_nop 0\n\tbuffer_load_dwordx4 %2, %3, %1 offen lds\n.rept %4\n\ts_add_u32 m0, m0, 0x1000\n\ts_add_u32 %1, %1, 0x7000\n\tbuffer_load_dwordx4 %2, %3, %1 offen lds\n.endr"
                 : "+s"(lds), "+s"(soff) : "v"(voff), "s"(r), "i"(N - 1) : "scc", "memory");
}
DI bf16x8 tr_frag(const char* a0, const char* a1) {
    s16x4 lo = __builtin_amdgcn_ds_read_tr16_b64_v4i16((LAS s16x4*)a0);
    s16x4 hi = __builtin_amdgcn_ds_read_tr16_b64_v4i16((LAS s16x4*)a1);
    return __builtin_bit_cast(bf16x8, __builtin_shufflevector(lo, hi, 0, 1, 2, 3, 4, 5, 6, 7));
}
DI bf16x8 pack8(const f32x16& v, int s) {
    f32x8 t = {v[8 * s], v[8 * s + 1], v[8 * s + 2], v[8 * s + 3], v[8 * s + 4], v[8 * s + 5], v[8 * s + 6], v[8 * s + 7]};
    return __builtin_convertvector(t, bf16x8);
}
DI float xhalf(float v) {
    const unsigned u = __builtin_bit_cast(unsigned, v);
    const auto r = __builtin_amdgcn_permlane32_swap(u, u, false, false);
    return __builtin_bit_cast(float, (threadIdx.x & 32) ? r[0] : r[1]);
}
#define WAITV(n) asm volatile("s_waitcnt vmcnt(" #n ")" ::: "memory")
#define WAITL0() asm volatile("s_waitcnt lgkmcnt(0)" ::: "memory")
#define BAR() do { asm volatile("" ::: "memory"); __builtin_amdgcn_s_barrier(); asm volatile("" ::: "memory"); } while (0)
#define SCHED() __builtin_amdgcn_sched_barrier(0)

#define SG(m, n) __builtin_amdgcn_sched_group_barrier(m, n, 0)
DI float fmx(float a, float b) { return __builtin_elementwise_maximum(a, b); }
typedef int i32x4 __attribute__((ext_vector_type(4)));
#define PIN(x) asm volatile("" : "+v"(x))
DI void raise(char* p, u64 tag) { (void)__hip_atomic_exchange((gu64*)p, tag, RLX); }
template <bool TWO_BARRIERS>
DI u64 poll(const char* p, u64 all, u64 need, u64 tag) {
    u64 v, zero, sv, left; unsigned cnt = SPIN_MAX;
#define POLL_LOOP ".Lpw%=:\n\ts_waitcnt vmcnt(0)\n\tv_cmp_ne_u64 %4, %6, %0\n\ts_and_b64 vcc, %4, %8\n\ts_cbranch_scc0 .Lpd%=\n\ts_sub_u32 %3, %3, 1\n\ts_cbranch_scc1 .Lpd%=\n\t" \
                  "global_atomic_add_x2 %0, %5, %1, off sc0\n\ts_branch .Lpw%=\n.Lpd%=:\n\ts_mov_b64 exec, %2"
#define POLL_OPS : "=&v"(v), "=&v"(zero), "=&s"(sv), "+s"(cnt), "=&s"(left) : "v"(p), "s"(tag), "s"(all), "s"(need) : "vcc", "scc", "memory"
    if (TWO_BARRIERS)
        asm volatile("s_mov_b64 %2, exec\n\ts_mov_b64 exec, %7\n\tv_mov_b64 %1, 0\n\tglobal_atomic_add_x2 %0, %5, %1, off sc0\n\ts_barrier\n\ts_barrier\n" POLL_LOOP POLL_OPS);
    else
        asm volatile("s_mov_b64 %2, exec\n\ts_mov_b64 exec, %7\n\tv_mov_b64 %1, 0\n\tglobal_atomic_add_x2 %0, %5, %1, off sc0\n" POLL_LOOP POLL_OPS);
    return left;
}

extern "C" __global__ void __launch_bounds__(512, 2)
fused(const char* xa, const char* wa, const char* woa, const char* xb, const char* wb, const char* wob, char* out, char* ws) {
    __shared__ __attribute__((aligned(16))) char smem[LDS_BYTES];
    STAMP_DECL;
    STAMP(0);
    const int b = blockIdx.x, xcd = b & 7, idx = b >> 3;
    const int br = xcd & 1, jj = idx >> 3, ty = idx & 7, j = (xcd >> 1) * 4 + jj;
    unsigned d0 = threadIdx.x * 128;
    {
        u64 pc;
        asm volatile("s_cmp_lg_u32 %2, 7\n\ts_cbranch_scc1 .Lskip%=\n\tv_cmp_gt_u32 vcc, fused_end-.Lhere%=, %1\n\ts_cbranch_vccz .Lskip%=\n\ts_mov_b64 exec, vcc\n\t"
                     "s_getpc_b64 %0\n.Lhere%=:\n\tglobal_load_dword %1, %1, %0\n\ts_mov_b64 exec, -1\n.Lskip%=:"
                     : "=&s"(pc), "+v"(d0) : "s"(ty) : "vcc", "scc", "memory");
    }
    const int wave = __builtin_amdgcn_readfirstlane(threadIdx.x >> 6), lane = threadIdx.x & 63;
    const int r3 = lane >> 3, u = lane & 7;
    const int lbase = (int)(unsigned)(size_t)(LAS char*)smem;
    int vx0 = r3 * 896 + ((u ^ (r3 >> 1)) << 4), vx1 = vx0 ^ 64;
    int vw = r3 * 2688 + ((u ^ (((r3 >> 1) & 1) << 2)) << 4);
    int rot = (ROT == 2 ? ((ty + j) & 1) * 4 : 0) + XROT * (xcd >> 1); if (rot >= 7) rot -= 7;
    const int sx = j * (128 * 896), sw = ty * 128;
    int e0 = sx + wave * 14336 + rot * 128, e1 = sw + (rot * 64 + wave * 8) * 2688, e2 = lbase + RING + wave * 2048, e3 = lbase + RING + 16384 + wave * 1024;
    asm volatile("" : "+s"(e0), "+s"(e1), "+s"(e2), "+s"(e3)); asm volatile("" : "+v"(vx0), "+v"(vx1), "+v"(vw));
    SCHED();
    const u32x4 rx = srd(br ? xb : xa, XB), rw = srd(br ? wb : wa, WB);
    if (ty < 7) {
        dma16(rx, e2, vx0, e0);
        dma16(rw, e3, vw, e1);
        dma16(rx, e2 + 1024, vx1, e0 + 7168);
        dma16(rw, e3 + 8192, vw, e1 + 896);
    } else {
        int ln[2];
#pragma unroll
        for (int i = 0; i < 2; ++i) { ln[i] = (i * 392 + (threadIdx.x < 392 ? threadIdx.x : 0)) * 4 + jj + PROT * (xcd >> 1) * 784; if (ln[i] >= 3136) ln[i] -= 3136; }
#pragma unroll
        for (int i = 0; i < 2; ++i) { const int row = (ln[i] * 18725) >> 17; asm volatile("global_load_dword %0, %1, %2" : "+v"(d0) : "v"(row * 1792 + 1792 + ln[i] * 128), "s"(br ? wb : wa) : "memory"); }
#pragma unroll
        for (int i = 0; i < 2; ++i) asm volatile("global_load_dword %0, %1, %2" : "+v"(d0) : "v"(ln[i] * 128), "s"(br ? wob : woa) : "memory");
        asm volatile("s_waitcnt vmcnt(0)" : "+v"(d0) :: "memory");
    }
    u64 tag = *(const __attribute__((address_space(4))) u64*)ws;
    auto make_tag = [&]() { asm volatile("" : "+s"(tag)); tag = tag * 0x9E3779B97F4A7C15ull + 0x7F4A7C159E3779B9ull; asm volatile("" : "+s"(tag)); };
    const int w4 = wave & 3;
    const bool aux = wave >= 4;
    const int hh = lane >> 5, t = lane & 31, blk = (lane >> 4) & 1, q = (lane >> 2) & 3, p = lane & 3;
    const int T = 4 * j + w4;
    const bool local = br == 0 && ty < 7;
    typedef int i32x8 __attribute__((ext_vector_type(8)));
    typedef int i32x16 __attribute__((ext_vector_type(16)));
    const __attribute__((address_space(4))) char* te = (const __attribute__((address_space(4))) char*)g_tab.e[(min(j, 2) * 2 + br) * 32 + ty * 4 + w4];
    i32x8 tb8 = *(const __attribute__((address_space(4))) i32x8*)(te + 64);
    int h, c0;
    auto use_tab = [&]() { asm volatile("" : "+s"(tb8)); h = tb8[E_H - 16]; c0 = tb8[E_C0 - 16]; };
    const __amdgpu_buffer_rsrc_t rxs = rsrc(ws + WS_X, X_BYTES);
    const int wb0 = (8 * hh + q) * 128 + ((blk * 2 + (p & 1)) << 4) + (p >> 1) * 8 + ((q >> 1) << 6), wb1 = wb0 ^ 64;

    const unsigned NEGB = 0xF149F2CAu;
    f32x16 cd, co;
    u32x4 pm[2];
    int nd, no, lm;
    auto mask_bits = [&]() {
        const unsigned lo = (2u << t) - 1u;
        nd = (int)(~lo >> (4 * hh)); lm = (int)(lo >> (4 * hh)); no = T < 8 ? -1 : lm;
    };
    auto mask_reg = [&](int r) {
        const int bit = (r & 3) + 8 * (r >> 2);
        float a = __builtin_bit_cast(float, __builtin_amdgcn_sbfe(nd, bit, 1) & NEGB), c = __builtin_bit_cast(float, __builtin_amdgcn_sbfe(no, bit, 1) & NEGB);
        PIN(a); PIN(c); cd[r] = a; co[r] = c;
        if (r & 1) { unsigned m = __builtin_amdgcn_perm(__builtin_amdgcn_sbfe(lm, bit, 1), __builtin_amdgcn_sbfe(lm, bit - 1, 1), 0x05040100u); PIN(m); pm[r >> 3][(r >> 1) & 3] = m; }
    };
    auto koff = [&](int cf, int s) { return (2 * s + hh) * 512 + ((((t >> 2) ^ ((cf + 2 * s + hh) & 3)) << 2) + (t & 3)) * 16; };
    auto voff = [&](int hd, int cf, int tt) {
        const int pc8 = (4 * blk + 2 * (p & 1) + (p >> 1) + (hd & 1)) & 7, cc = pc8 >> 1;
        return cc * 512 + ((((2 * tt + hh) ^ ((cf + cc) & 3)) << 2) + q) * 16 + (pc8 & 1) * 8;
    };
    auto qfix = [&](int hd, u32x4& q0, u32x4& q1) {
        const bool z0 = (hd & 1) && !hh, z1 = !(hd & 1) && hh;
        q0.x = z0 ? 0u : q0.x; q0.y = z0 ? 0u : q0.y; q1.z = z1 ? 0u : q1.z; q1.w = z1 ? 0u : q1.w;
    };
    auto ex = [&](f32x16& s) {
#pragma unroll
        for (int r = 0; r < 16; ++r) s[r] = __builtin_amdgcn_exp2f(s[r]);
    };
    auto rowmax = [&](const f32x16& s) {
        float m = fmx(s[0], s[1]);
#pragma unroll
        for (int r = 2; r < 16; r += 2) m = fmx(fmx(m, s[r]), s[r + 1]);
        const unsigned mu = __builtin_bit_cast(unsigned, m);
        const auto r = __builtin_amdgcn_permlane32_swap(mu, mu, false, false);
        const unsigned r0 = r[0], r1 = r[1];
        return fmx(__builtin_bit_cast(float, r0), __builtin_bit_cast(float, r1));
    };
    const __amdgpu_buffer_rsrc_t ros = rsrc(ws + WS_O, O_BYTES);
    auto put_o = [&](f32x16 o, float l, int rec) {
        o = o * __builtin_amdgcn_rcpf(l);
        store_dev(__builtin_bit_cast(u32x4, pack8(o, 0)), ros, lane * 16, rec);
        store_dev(__builtin_bit_cast(u32x4, pack8(o, 1)), ros, lane * 16 + 1024, rec);
    };
    auto ones = [&](int back, bool two) {
        const int bb = back + (two ? hh : 0);
        if (two || !hh) *(u32x2*)(smem + LR + w4 * 4096 + (2 - bb) * 16384 + 2048 + ((h & 1) ? 0 : 1544) + t * 16) = (u32x2){j >= bb ? 0x3F80u : 0u, 0u};
    };
    LAS char* wd = (LAS char*)(smem + LW + 7 * 512 + 8);

    #define wk ((LAS char*)(smem + LR + 32768 + ((h & 1) ? 0 : 1544)))
#define FTAG ((tag | 1) & 0xBFFFBFFFBFFFBFFFull)
    auto arrive = [&](int n) { if (lane == 0) *(volatile LAS u64*)(wk + (n + w4) * 16) = FTAG; };
    auto await4 = [&](int n) {
#pragma unroll 1
        for (int i = 0; i < SPIN_MAX * 4; ++i) if (__all(*(volatile LAS u64*)(wk + (n + p) * 16) == FTAG)) break;
        asm volatile("" ::: "memory");
    };
    bf16x8 wo[28];
    auto read_wo = [&](int base) {
        int a = base + ((w4 >> 1) ? wb1 : wb0) + hh * (20 * 128);
        PIN(a);
#pragma unroll
        for (int ks = 0; ks < 28; ++ks) {
            const int i = ks % 7, row = (ks / 7) * 112 + (i < 6 ? (i / 3) * 56 + (i % 3) * 8 : 24);
            wo[ks] = tr_frag(smem + a + row * 128, smem + a + (row + (i < 6 ? 4 : 56)) * 128);
        }
    };
    const int vwo = r3 * 896 + ((u ^ (((r3 >> 1) & 1) << 2)) << 4);
    const u32x4 rwo = srd(br ? wob : woa, OB);

    if (__builtin_expect(aux, 1)) {
        const bool x7 = ty == 7;
        i32x16 tb = *(const __attribute__((address_space(4))) i32x16*)te;
        if (ty < 7) {
            int ch = rot, sg = 0;
#pragma unroll 1
            for (int c = 1; c < 7; ++c) {
                ++ch; if (ch == 7) ch = 0;
                ++sg; if (sg == 3) sg = 0;
                const int st = lbase + RING + sg * STAGE;
#pragma unroll
                for (int i = 0; i < 4; ++i) dma16(rx, st + w4 * 4096 + i * 1024, (i & 1) ? vx1 : vx0, sx + (w4 * 4 + i) * 7168 + ch * 128);
#pragma unroll
                for (int cb = 0; cb < 2; ++cb)
#pragma unroll
                    for (int i = 0; i < 2; ++i) dma16(rw, st + 16384 + cb * 8192 + w4 * 2048 + i * 1024, vw, sw + (ch * 64 + w4 * 16 + i * 8) * 2688 + cb * 896);
                WAITV(8); BAR();
            }
#pragma unroll 1
            for (int c = 0; c < 7; ++c) {
                ++ch; if (ch == 7) ch = 0;
#pragma unroll
                for (int i = 0; i < 2; ++i) dma16(rw, lbase + LA + c * 8192 + w4 * 2048 + i * 1024, vw, sw + (ch * 64 + w4 * 16 + i * 8) * 2688 + 1792);
                if (c == 3) { WAITV(8); BAR(); }
            }
            BAR();
        }
        make_tag(); use_tab();
        asm volatile("" : "+s"(tb));
        const int jo = j * (4 * XT), sl = tb8[E_SL - 16];
        int ra0 = tb[E_RA0] + jo, ra1 = tb[E_RA1] + jo, ra2 = tb[E_RA2] + jo, rb0 = tb[E_RB0] + jo, rb1 = tb[E_RB1] + jo, rq = tb[E_RQ] + jo, lh = lbase + LR + w4 * 4096;
        asm volatile("" : "+s"(ra0), "+s"(ra1), "+s"(ra2), "+s"(rb0), "+s"(rb1), "+s"(rq), "+s"(lh));
        const int lb0 = lbase + tb[E_LB0], lb1 = lbase + tb[E_LB1];
        const int hd[2] = {tb[E_HD0], tb[E_HD1]}, cf[2] = {tb[E_CF0], tb[E_CF1]}, po[2] = {tb8[E_PO0 - 16], tb8[E_PO1 - 16]};
        const u64 mb = (u64)(unsigned)tb[E_MB] << 32, mall = mb | (unsigned)tb[E_MA];
        const char* f1 = ws + (WS_F1 + j * FS + tb[E_PG0]) + ((hh * 16 - (r3 & 3)) * FS + (lane & 4) * tb[E_PGD] + p * 8);
        int l16 = lane * 16;
        PIN(f1); PIN(l16);
        int lz = lane; PIN(lz);
        const int g4 = lz >> 4, c16 = lz & 15;
        int fo[2], ko[2], qo[2], vo[2][2];
        f32x4 md, mo;
        const bool pt = x7 || g4 == sl;
#pragma unroll
        for (int e = 0; e < 2; ++e) fo[e] = g4 * 512 + (((c16 >> 1) ^ ((cf[e] + g4) & 3)) << 6) + (c16 & 1) * 32;
        int qs = pt ? fo[1] : OOB; PIN(qs);
        if (!local) {
#pragma unroll
            for (int e = 0; e < 2; ++e) { ko[e] = LW + 40960 - w4 * 8192 + po[e] + fo[e]; qo[e] = LW + w4 * 4096 + po[e] + fo[e]; PIN(ko[e]); PIN(qo[e]); }
#pragma unroll
            for (int r = 0; r < 4; ++r) {
                const int d = c16 - 4 * g4 - r;
                md[r] = (unsigned)d < 8u ? 0.f : -1.0e30f; mo[r] = (d < -8 && T) ? 0.f : -1.0e30f;
            }
            PIN(md); PIN(mo);
        }
        if (ty < 7) { WAITV(6); BAR(); }
        if (!local) {
            PIN(lz);
#pragma unroll
            for (int e = 0; e < 2; ++e)
#pragma unroll
                for (int s = 0; s < 2; ++s) {
                    const int pc8 = (2 * (lz & 3) + s + (hd[e] & 1)) & 7, cc = pc8 >> 1;
                    vo[e][s] = LW + 40960 - w4 * 8192 + 4096 + po[e] + cc * 512 + (((lz >> 3) ^ ((cf[e] + cc) & 3)) << 6) + (q & 1) * 32 + (pc8 & 1) * 8; PIN(vo[e][s]);
                }
        }

        if (wave == 4 && lane < 2) *(volatile LAS u64*)(wd + lane * 16) = 0;
        asm volatile("s_waitcnt vmcnt(0) lgkmcnt(0)" ::: "memory"); BAR();
        STAMP(4);
        const u32x4 rxx = srd(ws + WS_X, X_BYTES);
        auto tell = [&](int n) { if (lane == 0) *(volatile LAS u64*)(n < 2 ? wd + n * 16 : wk + 64) = FTAG; };
        auto hear = [&](int n) {
#pragma unroll 1
            for (int i = 0; i < SPIN_MAX * 4; ++i) { if (__all(*(volatile LAS u64*)(n < 2 ? wd + n * 16 : wk + 64) == FTAG)) break; if (HSL) __builtin_amdgcn_s_sleep(HSL); }
            asm volatile("" ::: "memory");
        };
        auto fetch_b = [&](int v) {
            const int go = v * (56 * 512), lo = v * 4096;
            if (x7) { dma32_dev(rxx, lb0 + lo, l16, rb0 + go); dma32_dev(rxx, lb1 + lo, l16, rb1 + go); }
            else {
                if (w4 == 0) dma64_dev(rxx, lb1 + lo, l16, rb1 + go);
                if (sl != 8 && lane < 32) { dma16_dev(rxx, lb0 + lo, l16, rb0 + go); if (w4) dma16_dev(rxx, lb1 + lo, l16, rb1 + go); }
            }
        };
        auto fetch_a = [&](int v) {
            const int go = v * (56 * 512), lo = v * 2048;
            dma32_dev(rxx, lh + 32768 + lo, l16, ra0 + go);
            dma32_dev(rxx, lh + 16384 + lo, l16, ra1 + go);
            dma32_dev(rxx, lh + lo, l16, ra2 + go);
        };
        STAMP(5);
        unsigned left = 1;
        if (!local) { if (wave == 4) { left = __builtin_amdgcn_readfirstlane((unsigned)poll<false>(f1, mall, mb, tag)); tell(0); if (!left) tell(1); } else hear(0); }
        STAMP(2);
        u32x4 qp[2];
        asm volatile("buffer_load_dwordx4 %0, %2, %3, %4 offen sc1\n\tbuffer_load_dwordx4 %1, %2, %3, %4 offen offset:16 sc1"
                     : "=&v"(qp[0]), "=&v"(qp[1]) : "v"(qs), "s"(rxx), "s"(rq) : "memory");
        if (!local) fetch_b(0);
        if (wave == 4) { if (left) { poll<false>(f1, mall ^ mb, mall ^ mb, tag); tell(1); } } else hear(1);
        STAMP(6);
        dma32_dev(rxx, lh + 2048, l16, ra0 - 56 * 512);
        fetch_a(0);
        WAITV(8);
        PIN(qp[0]); PIN(qp[1]);
        STAMP(3);
        bf16x8 pe[2][2];
        float ls[2][2];
        if (!local) {
            f32x4 sd[2][2], so[2][2];
#pragma unroll
            for (int e = 0; e < 2; ++e)
#pragma unroll
                for (int y = 0; y < 2; ++y) {
                    const char* kb = smem + ko[e] + y * 16;
                    u32x4 qv = *(const u32x4*)(smem + qo[e] + y * 16);
                    const bool a = pt && (e || x7), z0 = (hd[e] & 1) && g4 == 0, z1 = !(hd[e] & 1) && g4 == 3;
                    qv = (u32x4){a ? qp[y].x : qv.x, a ? qp[y].y : qv.y, a ? qp[y].z : qv.z, a ? qp[y].w : qv.w};
                    qv = (u32x4){z0 ? 0u : qv.x, z0 ? 0u : qv.y, z1 ? 0u : qv.z, z1 ? 0u : qv.w};
                    sd[e][y] = MFMA16(*(const bf16x8*)kb, __builtin_bit_cast(bf16x8, qv), md);
                    so[e][y] = MFMA16(*(const bf16x8*)(kb + 8192), __builtin_bit_cast(bf16x8, qv), mo);
                }
            const bf16x8 one = __builtin_bit_cast(bf16x8, (u32x4){0x3F803F80u, 0x3F803F80u, 0x3F803F80u, 0x3F803F80u});
            float m[4];
#pragma unroll
            for (int i = 0; i < 4; ++i) {
                const f32x4 a = sd[i >> 1][i & 1], c = so[i >> 1][i & 1];
                m[i] = fmx(fmx(fmx(fmx(a[0], a[1]), a[2]), fmx(fmx(a[3], c[0]), c[1])), fmx(c[2], c[3]));
            }
#pragma unroll
            for (int i = 0; i < 4; ++i) {
                const unsigned mu = __builtin_bit_cast(unsigned, m[i]);
                const auto r = __builtin_amdgcn_permlane32_swap(mu, mu, false, false);
                const unsigned r0 = r[0], r1 = r[1];
                m[i] = fmx(__builtin_bit_cast(float, r0), __builtin_bit_cast(float, r1));
            }
#pragma unroll
            for (int i = 0; i < 4; ++i) {
                const unsigned mu = __builtin_bit_cast(unsigned, m[i]);
                const auto r = __builtin_amdgcn_permlane16_swap(mu, mu, false, false);
                const unsigned r0 = r[0], r1 = r[1];
                m[i] = fmx(__builtin_bit_cast(float, r0), __builtin_bit_cast(float, r1));
            }
            SCHED();
            f32x4 sm[4];
#pragma unroll
            for (int i = 0; i < 4; ++i) {
                const f32x4 a = sd[i >> 1][i & 1], c = so[i >> 1][i & 1];
                f32x8 x = {a[0], a[1], a[2], a[3], c[0], c[1], c[2], c[3]};
                x -= m[i];
#pragma unroll
                for (int r = 0; r < 8; ++r) x[r] = __builtin_amdgcn_exp2f(x[r]);
                pe[i >> 1][i & 1] = __builtin_convertvector(x, bf16x8);
                sm[i] = MFMA16(one, pe[i >> 1][i & 1], (f32x4){});
                SCHED();
            }
#pragma unroll
            for (int i = 0; i < 4; ++i) ls[i >> 1][i & 1] = __builtin_amdgcn_rcpf(sm[i][0]);
#pragma unroll
            for (int i = 0; i < 4; ++i) { PIN(pe[i >> 1][i & 1]); PIN(ls[i >> 1][i & 1]); }
        }
        STAMP(10);
        asm volatile("s_waitcnt vmcnt(0) lgkmcnt(0)" ::: "memory");
        STAMP(7);
        __builtin_amdgcn_s_setprio(2);
        if (wave == 4) { poll<true>(f1 + 32 * FS, mall, mall, tag); tell(2); }
        else {
            BAR(); BAR();
            dma_rows<14>(rwo, lbase + LA + w4 * 1024, vwo, w4 * 7168 + (ty & 6) * 128);
            hear(2);
        }
        STAMP(14);
        fetch_a(1);
        if (!local) fetch_b(1);
        if (!YEARLY) dma_rows<YB0>(rwo, lbase + LA + w4 * 1024, vwo, w4 * 7168 + (ty & 6) * 128);
        if (!local && (x7 || w4 == 0)) WAITV(4); else WAITV(0);
        ones(0, true); ones(2, false); WAITL0(); arrive(0);
        WAITV(0);
        if (!YEARLY) dma_rows<14 - YB0>(rwo, lbase + LA + (w4 + 4 * YB0) * 1024, vwo, (w4 + 4 * YB0) * 7168 + (ty & 6) * 128);
        else if (wave == 4) dma_rows<14>(rwo, lbase + LA, vwo, (ty & 6) * 128);
        STAMP(9);
        if (!local) {
            u32x4 ov[2][2];
#pragma unroll
            for (int e = 0; e < 2; ++e)
#pragma unroll
                for (int y = 0; y < 2; ++y) {
                    const char* v0 = smem + vo[e][0] + y * 16; const char* v1 = smem + vo[e][1] + y * 16;
                    const f32x4 a = MFMA16(tr_frag(v0, v0 + 8192), pe[e][y], (f32x4){}), c = MFMA16(tr_frag(v1, v1 + 8192), pe[e][y], (f32x4){});
                    f32x8 x = {a[0], a[1], a[2], a[3], c[0], c[1], c[2], c[3]};
                    x *= ls[e][y];
                    ov[e][y] = __builtin_bit_cast(u32x4, __builtin_convertvector(x, bf16x8));
                }
#pragma unroll
            for (int e = 0; e < 2; ++e)
#pragma unroll
                for (int y = 0; y < 2; ++y) store_dev(ov[e][y], ros, g4 * 512 + c16 * 16 + y * 256, ((64 + T) * 16 + hd[e]) * 2048);
        }
        STAMP(11);
        __builtin_amdgcn_s_setprio(0);
        if (!local) { arrive(8); WAITV(4); arrive(16); await4(8); } else { WAITV(0); arrive(16); }
        dma_rows<6>(rwo, lbase + LW + w4 * 1024, vwo, ty < 6 ? w4 * 7168 + (ty | 1) * 128 : OOB);
        if (!local) { WAITV(6); if (lane < 2) raise(ws + WS_F2 + (32 + (T >> 1)) * FS + (T & 1) * 128 + (lane ? hd[1] : hd[0]) * 8, tag); }
        if (ty < 6) dma_rows<8>(rwo, lbase + LW + (w4 + 24) * 1024, vwo, (w4 + 24) * 7168 + (ty | 1) * 128);
        if (b == 0) {
            poll<false>(ws + WS_F1 + (w4 * 8 + r3) * FS + u * 32, ~0ull, ~0ull, tag);
            arrive(12);
            if (wave == 4) { await4(12); if (lane == 0) __hip_atomic_store((gu64*)ws, tag, RLX); }
        }
        WAITV(0);
    } else {
        mask_bits();
        if (ty < 7) {
            int xo[2][4], wo_[2][2];
#pragma unroll
            for (int g = 0; g < 2; ++g) {
#pragma unroll
                for (int ks = 0; ks < 4; ++ks) { xo[g][ks] = RING + g * 65536 + (w4 * 32 + t) * 128 + (((2 * ks + hh) ^ ((t >> 1) & 7)) << 4); PIN(xo[g][ks]); }
                wo_[g][0] = RING + g * 65536 + wb0; wo_[g][1] = RING + g * 65536 + wb1; PIN(wo_[g][0]); PIN(wo_[g][1]);
            }
            int es[2], el[2][2], g0;
            unsigned pq, pk;
            char* f1;
            auto put = [&](const f32x16& a0, const f32x16& a1, int sec, unsigned pub, u32x4 (&d)[2][2]) {
#pragma unroll
                for (int e = 0; e < 2; ++e)
#pragma unroll
                    for (int s = 0; s < 2; ++s) {
                        d[e][s] = __builtin_bit_cast(u32x4, pack8(e ? a1 : a0, s));
                        if ((pub >> (2 * e + s)) & 1) store_dev(d[e][s], rxs, es[s] + e * 2048, g0 + sec * (56 * 512));
                    }
            };
            auto keep = [&](int lo, const u32x4 (&d)[2][2]) {
#pragma unroll
                for (int e = 0; e < 2; ++e)
#pragma unroll
                    for (int s = 0; s < 2; ++s) if (el[e][s] >= 0) *(u32x4*)(smem + el[e][s] + lo) = d[e][s];
            };
            f32x16 acc[4];
            bf16x8 xf[28], wf[2][4];
            const f32x16 zero = {};
            WAITV(0); BAR();
            STAMP(1); CST(20);
            auto rd = [&](int n) {
                const int sg = (n >> 2) % 3, ks = n & 3;
                const char* st = smem + (sg & 1) * STAGE;
#pragma unroll
                for (int i = 3; i >= 0; --i) {
                    const char* a = st + wo_[sg >> 1][i & 1] + 16384 + (i >> 1) * 8192 + ks * 2048;
                    wf[n & 1][i] = tr_frag(a, a + 512);
                    if (i == 3) xf[n] = *(const bf16x8*)(st + xo[sg >> 1][ks]);
                }
            };
            rd(0);
            SCHED();
#pragma unroll
            for (int n = 0; n < 28; ++n) {
                if (n == 11) {
                    make_tag(); use_tab();
#pragma unroll
            for (int s = 0; s < 2; ++s) {
                const int sw16 = ((((t >> 2) ^ (2 * s + hh)) << 2) + (t & 3)) * 16;
                es[s] = (2 * s + hh) * 512 + sw16; PIN(es[s]);
#pragma unroll
                for (int e = 0; e < 2; ++e) {
                    const int ps = tb8[E_PS - 16] + 4 * e + 2 * s + hh;
                    el[e][s] = (unsigned)ps < 8u ? ps * 512 + sw16 : -1; PIN(el[e][s]);
                }
            }
            pq = tb8[E_PQ - 16]; pk = tb8[E_PK - 16];
            g0 = (br * 64 + T) * XT + ty * 4096;
            f1 = ws + WS_F1 + (br * 16 + j) * FS + ty * 32 + w4 * 8;
            asm volatile("" : "+s"(g0), "+s"(f1));
                    SCHED();
                }
                if ((n & 3) == 3) { WAITL0(); BAR(); SCHED(); }
                if (n < 27) rd(n + 1);
#pragma unroll
                for (int i = 3; i >= 0; --i) acc[i] = MFMA(wf[n & 1][i], xf[n], n ? acc[i] : zero);
                if (n < 27) { SG(0x8, 1); SG(0x100, 3); SG(0x8, 1); SG(0x100, 3); SG(0x8, 1); SG(0x100, 3); SG(0x8, 1); }
                SCHED();
            }
            STAMP(2);
            const float qs = 0.18898223650461361f * 1.4426950408889634f;
            {
                u32x4 dk[2][2], dq[2][2];
                put(acc[2], acc[3], 1, pk, dk);
                put(acc[0] * qs, acc[1] * qs, 0, pq, dq);
                if (br) { keep(LW + 40960 - w4 * 8192, dk); keep(LW + w4 * 4096, dq); }
            }
            STAMP(3);
            BAR();
            bf16x8 wv[8];
            u32x4 dv[2][2];
            auto rv = [&](int n) { const char* a = smem + LA + (n % 28) * 2048 + (n >= 28 ? wb1 : wb0); wv[n & 7] = tr_frag(a, a + 512); };
            auto putv = [&](int e, int s) {
                dv[e][s] = __builtin_bit_cast(u32x4, pack8(acc[2 + e], s));
                store_dev(dv[e][s], rxs, es[s] + e * 2048, ((pk >> (2 * e + s)) & 1) ? g0 + 112 * 512 : OOB);
            };
#pragma unroll
            for (int n = 0; n < 6; ++n) rv(n);
            PIN(nd); PIN(no); PIN(lm);
            SCHED();
#pragma unroll
            for (int n = 0; n < 56; ++n) {
                if (n == BV1) { BAR(); SCHED(); }
                if (n == RAISE_B || n == RAISE_A) { if ((n == RAISE_B) == (br != 0)) { WAITV(0); if (lane == 0) raise(f1, tag); STAMP(9); } SCHED(); }
                acc[2 + n / 28] = MFMA(wv[n & 7], xf[n % 28], n % 28 ? acc[2 + n / 28] : zero);
                if (n + 6 < 56) rv(n + 6);
                if (n >= 36 && n < 52) mask_reg(n - 36);
                SG(0x8, 1); SG(0x100, 2); SG(0x2, 8);
                SCHED();
                if (n == 30 || n == 32) { putv(0, (n - 30) >> 1); SCHED(); }
            }
            STAMP(4); CST(21);
            putv(1, 0); putv(1, 1);
            if (br) keep(LW + 45056 - w4 * 8192, dv);
            STAMP(5);
            WAITV(0);
            if (lane == 0) raise(f1 + 32 * FS, tag);
        } else {
            make_tag(); use_tab();
            BAR();
            if (threadIdx.x == 0) raise(ws + WS_F1 + (br * 16 + j) * FS + 7 * 32, tag);
#pragma unroll
            for (int r = 0; r < 16; ++r) mask_reg(r);
        }
        const char* tl = smem + LR + w4 * 4096;
        int fo[2] = {koff(c0, 0), koff(c0, 1)}, vo[2] = {voff(h, c0, 0) + 2048, voff(h, c0, 1) + 2048};
        PIN(fo[0]); PIN(fo[1]); PIN(vo[0]); PIN(vo[1]);
        STAMP(6);
        WAITL0(); BAR();
        STAMP(7);
        __builtin_amdgcn_s_setprio(1);
        u32x4 qf[2];
        qf[0] = *(const u32x4*)(tl + 2048 + fo[0]); qf[1] = *(const u32x4*)(tl + 2048 + fo[1]);
        auto kfrag = [&](int n, int s) { return *(const bf16x8*)(tl + (8 - n) * 4096 + fo[s]); };
        auto vfrag = [&](int n, int s) { const char* vb = tl + (8 - n) * 4096 + s * 256; return tr_frag(vb + vo[0], vb + vo[1]); };
        bf16x8 k0 = kfrag(0, 0), k1 = kfrag(0, 1), k2 = kfrag(8, 0), k3 = kfrag(8, 1);
        WAITL0(); BAR();
        CST(19); STAMP(10);
        qfix(h, qf[0], qf[1]);
        const bf16x8 q0 = __builtin_bit_cast(bf16x8, qf[0]), q1 = __builtin_bit_cast(bf16x8, qf[1]);
        auto qk = [&](int n, const f32x16& c) { f32x16 r = MFMA(kfrag(n, 0), q0, c); return MFMA(kfrag(n, 1), q1, r); };
        f32x16 cm, sa, sb;
        bf16x8 pp[8][2];
        cd = MFMA(k0, q0, cd); co = MFMA(k2, q0, co); cd = MFMA(k1, q1, cd); co = MFMA(k3, q1, co);
        k0 = kfrag(1, 0); k1 = kfrag(1, 1);
#pragma unroll
        for (int r = 0; r < 16; ++r) cd[r] = fmx(cd[r], co[r]);
        {
            const float m = -rowmax(cd);
#pragma unroll
            for (int r = 0; r < 16; ++r) cm[r] = m;
        }
        cd += cm;
        sa = cd;
        SCHED();
#define STG(n, cur, nxt) do { \
            if (n < 7) { nxt = MFMA(k0, q0, cm); nxt = MFMA(k1, q1, nxt); } \
            if (n < 6) { k0 = kfrag(n + 2, 0); k1 = kfrag(n + 2, 1); } \
            ex(cur); pp[n][0] = pack8(cur, 0); pp[n][1] = pack8(cur, 1); \
            if (n < 7) SG(0x8, 1); SG(0x400, 4); if (n < 7) SG(0x8, 1); if (n < 6) SG(0x100, 2); SG(0x400, 4); SG(0x2, 2); SG(0x400, 4); SG(0x2, 2); SG(0x400, 4); SG(0x2, 4); SCHED(); } while (0)
        STG(0, sa, sb); STG(1, sb, sa); STG(2, sa, sb); STG(3, sb, sa); STG(4, sa, sb); STG(5, sb, sa); STG(6, sa, sb); STG(7, sb, sa);
        bf16x8 pz[2];
#pragma unroll
        for (int s = 0; s < 2; ++s) {
            const u32x4 e = __builtin_bit_cast(u32x4, pp[0][s]);
            pp[0][s] = __builtin_bit_cast(bf16x8, e & pm[s]); pz[s] = __builtin_bit_cast(bf16x8, e & ~pm[s]); PIN(pz[s]);
        }
#pragma unroll
        for (int n = 0; n < 8; ++n) { PIN(pp[n][0]); PIN(pp[n][1]); }
        char* f2 = ws + WS_F2 + (T >> 1) * FS + (T & 1) * 128 + h * 8;
        int orec = (T * 16 + h) * 2048;
        const char* va = tl + vo[0]; const char* vb = tl + vo[1];
        asm volatile("" : "+s"(f2), "+s"(orec));
        STAMP(8);
        await4(0);
        STAMP(14);
        f32x16 o = {};
        auto pv = [&](int n, bf16x8 p0, bf16x8 p1) { o = MFMA(vfrag(n, 0), p0, o); o = MFMA(vfrag(n, 1), p1, o); };
        {
            bf16x8 vf[18];
            auto rdv = [&](int i) { const int off = (8 - (i >> 1)) * 4096 + (i & 1) * 256; vf[i] = tr_frag(va + off, vb + off); };
#pragma unroll
            for (int i = 0; i < 5; ++i) { rdv(i); SCHED(); }
#pragma unroll
            for (int i = 0; i < 18; ++i) {
                if (PPV && i == 5) { await4(20); SCHED(); }
                o = MFMA(vf[i], i < 16 ? pp[i >> 1][i & 1] : pz[i & 1], o);
                if (i + 5 < 18) rdv(i + 5);
                SCHED();
            }
        }
        auto publish = [&]() {
            const float lf = o[12];
            const unsigned lu = __builtin_bit_cast(unsigned, lf);
            const unsigned lx = __builtin_amdgcn_permlane32_swap(lu, lu, false, false)[1];
            put_o(o, __builtin_bit_cast(float, lx), orec);
        };
        publish();
        STAMP(11); CST(22);
        float big = fmx(__builtin_fabsf(o[0]), __builtin_fabsf(o[1]));
#pragma unroll
        for (int r = 2; r < 16; r += 2) big = fmx(fmx(big, __builtin_fabsf(o[r])), __builtin_fabsf(o[r + 1]));
        if (__builtin_expect(!__all(big < 1.0e36f), 0)) {
            const int nmid = T >= 7 ? 7 : T;
            float m = 0.f;
#pragma unroll 1
            for (int i = 1; i <= nmid; ++i) m = fmx(m, rowmax(qk(i, cm)));
            cd -= m; cm -= m;
            ex(cd);
            o = (f32x16){};
            const u32x4 e0 = __builtin_bit_cast(u32x4, pack8(cd, 0)), e1 = __builtin_bit_cast(u32x4, pack8(cd, 1));
            pv(0, __builtin_bit_cast(bf16x8, e0 & pm[0]), __builtin_bit_cast(bf16x8, e1 & pm[1]));
            pv(8, __builtin_bit_cast(bf16x8, e0 & ~pm[0]), __builtin_bit_cast(bf16x8, e1 & ~pm[1]));
#pragma unroll 1
            for (int i = 1; i <= nmid; ++i) {
                f32x16 s = qk(i, cm);
                ex(s); pv(i, pack8(s, 0), pack8(s, 1));
            }
            WAITV(0);
            publish();
        }
        if (RDFIRST) { await4(16); read_wo(LA); SCHED(); }
        WAITV(0);
        if (lane == 0) raise(f2, tag);
        __builtin_amdgcn_s_setprio(0);
        STAMP(12);
        if (!RDFIRST) { await4(16); read_wo(LA); }
    }
    STAMP(13);
    const int To = br * 64 + 4 * j + 2 * (ty & 1);
    const int tr = br ? 2 * (t & 15) + (t >> 4) : t;
    const int tb = LR + 32768 + ((wave >> 2) * 2 + (w4 & 1)) * 4096;
    int l16 = lane * 16, oa = (w4 & 1) * LR + hh * 2048 + t * 16, tw = tb + tr * 128 + (((4 * (w4 >> 1) + hh) ^ (tr & 7)) << 4), tg = tb + (w4 >> 1) * 2048 + r3 * 128 + ((u ^ r3) << 4);
    int ov = r3 * 1792 + u * 16;
    int oo = ((4 * j + 2 * (ty & 1) + (w4 & 1)) * 32 + (w4 >> 1) * 16) * 1792 + br * 896 + ((ty & 6) + (wave >> 2)) * 128;
    const int os = (To * 16 + w4) * 2048, ol = lbase + w4 * 2048;
    if (F64) { tg = tb + (lane >> 2) * 128 + (((4 * (w4 >> 1) + p) ^ ((lane >> 2) & 7)) << 4); ov = (lane >> 2) * 1792 + p * 16; oo += (w4 >> 1) * (64 - 16 * 1792); }
    asm volatile("" : "+v"(l16), "+v"(oa), "+v"(tw), "+v"(tg), "+v"(ov)); asm volatile("" : "+s"(oo));
    const __amdgpu_buffer_rsrc_t rout = rsrc(out, 2048 * 1792);
    const u32x4 roo = srd(ws + WS_O, O_BYTES);
    const bool idle = aux && ty >= 6;
    if (wave == 0) poll<false>(ws + WS_F2 + (To >> 1) * FS + lane * 8, 0xFFFFFFFFull, 0xFFFFFFFFull, tag);
    WAITL0(); BAR();
    STAMP(15);
    if (idle) { STAMP_FLUSH(); return; }
    auto geto = [&](int qd) { dma32_dev(roo, ol + qd * 8192, l16, os + qd * 8192); dma32_dev(roo, ol + qd * 8192 + LR, l16, os + qd * 8192 + 32768); };
    f32x16 acc = {};
    {
        bf16x8 of[2][7];
        auto rdo = [&](int qd) {
            const char* a = smem + oa + qd * 8192;
#pragma unroll
            for (int i = 0; i < 6; ++i) of[qd & 1][i] = *(const bf16x8*)(a + (i / 3) * 4096 + (i % 3) * 512);
            const u32x2 lo = *(const u32x2*)(a + 1536), hi = *(const u32x2*)(a + 4096 + 1536);
            of[qd & 1][6] = __builtin_bit_cast(bf16x8, (u32x4){lo.x, lo.y, hi.x, hi.y});
        };
        auto mmo = [&](int qd, int i0, int i1) {
#pragma unroll
            for (int i = i0; i < i1; ++i) acc = MFMA(wo[qd * 7 + i], of[qd & 1][i], acc);
            SCHED();
        };
        if (aux) read_wo(LW); else { geto(0); geto(1); geto(2); WAITV(8); }
        BAR(); rdo(0); SCHED();
        if (!aux) geto(3);
        mmo(0, 0, 3);
        WAITV(8);
        BAR(); rdo(1); SCHED();
        mmo(0, 3, 7); mmo(1, 0, 3);
        WAITV(4);
        BAR(); rdo(2); SCHED();
        mmo(1, 3, 7); mmo(2, 0, 3);
        WAITV(0);
        BAR(); rdo(3); SCHED();
        mmo(2, 3, 7); mmo(3, 0, 7);
    }
    STAMP(16);
    {
#pragma unroll
        for (int s = 0; s < 2; ++s) *(bf16x8*)(smem + (tw ^ (s << 5))) = pack8(acc, s);
        WAITL0(); if (!F64) BAR();
        const u32x4 v0 = *(const u32x4*)(smem + tg), v1 = *(const u32x4*)(smem + tg + (F64 ? 2048 : 1024));
        SCHED();
        store_dev(v0, rout, ov, oo); store_dev(v1, rout, ov, oo + (F64 ? 16 : 8) * 1792);
    }
    STAMP(17);
    STAMP_FLUSH();
}

extern "C" __device__ __attribute__((noinline, used)) void fused_end() {}

extern "C" void kernel_launch(void* const* d_in, const int* in_sizes, int n_in, void* d_out, int out_size, void* d_ws, size_t ws_size, hipStream_t stream) {
    fused<<<256, 512, 0, stream>>>((const char*)d_in[0], (const char*)d_in[1], (const char*)d_in[2], (const char*)d_in[3], (const char*)d_in[4], (const char*)d_in[5], (char*)d_out, (char*)d_ws);
}
```

```cpp
#include <hip/hip_runtime.h>
#include <hip/hip_bf16.h>

typedef __bf16 bf16x8 __attribute__((ext_vector_type(8)));
typedef short s16x4 __attribute__((ext_vector_type(4)));
typedef float f32x16 __attribute__((ext_vector_type(16)));
typedef float f32x8 __attribute__((ext_vector_type(8)));
typedef float f32x4 __attribute__((ext_vector_type(4)));
typedef unsigned u32x4 __attribute__((ext_vector_type(4)));
typedef unsigned u32x2 __attribute__((ext_vector_type(2)));
typedef unsigned long long u64;
typedef __attribute__((address_space(1))) u64 gu64;
#define LAS __attribute__((address_space(3)))
#define DI __device__ __forceinline__
#define MFMA(a, b, c) __builtin_amdgcn_mfma_f32_32x32x16_bf16((a), (b), (c), 0, 0, 0)
#define MFMA16(a, b, c) __builtin_amdgcn_mfma_f32_16x16x32_bf16((a), (b), (c), 0, 0, 0)
#define RLX __ATOMIC_RELAXED, __HIP_MEMORY_SCOPE_AGENT

constexpr int XB = 2048 * 896, WB = 448 * 2688, OB = 448 * 896;
constexpr int STAGE = 32768, RING = 65536, LA = 0, LW = 57344, LR = 114688, LX = 106496, LDS_BYTES = 163840;
constexpr int WS_F1 = 4096, WS_F2 = 65536, WS_X = 1 << 20, XT = 168 * 512, X_BYTES = 2 * 64 * XT, WS_O = 16 << 20, O_BYTES = 2 * 64 * 16 * 2048;
constexpr int OOB = 0x40000000;
#define ROT 2
#define PF0 2
#define RAISE_A 27
#define RAISE_B 4
#define BV1 10
#define KH_DELAY 8
#define OPRE 10
#define OROT 0
#define XB0 5
#define YB0 3
#define YEARLY 1
constexpr int SPIN_MAX = 1 << 18;

enum { E_RA0, E_RA1, E_RA2, E_RB0, E_RB1, E_RQ, E_LB0, E_LB1, E_MA, E_MB, E_PG0, E_PGD, E_HD0, E_HD1, E_CF0, E_CF1, E_PO0, E_PO1, E_SL, E_H, E_C0, E_PS, E_PQ, E_PK };
struct Tab {
    int e[192][32];
    constexpr Tab() : e() {
        for (int i = 0; i < 192; ++i) {
            const int w4 = i & 3, ty = (i >> 2) & 7, br = (i >> 5) & 1, jc = i >> 6;
            const bool x7 = ty == 7, local = br == 0 && !x7;
            const int ha = (0x4ECA7530u >> (4 * ty)) & 15, hb = (0xBFD98621u >> (4 * ty)) & 15;
            const int ca = (7 * ha) >> 1, cb = (7 * hb) >> 1, cs = ca < cb ? ca : cb;
            const int h = br ? hb : ha, c0 = br ? cb : ca;
            const int nb = x7 ? 15 : (0xF63F30Fu >> (4 * ty)) & 15;
            const int sl = (ty == 2 || ty == 5) ? 3 : 0;
            const int two = (x7 || (br && nb != 15)) ? 0xFF : 0x0F;
            const int rt = (64 + w4) * XT;
            int* v = e[i];
            v[E_HD0] = x7 ? h : ha; v[E_HD1] = x7 ? h : hb; v[E_CF0] = x7 ? c0 : ca; v[E_CF1] = x7 ? c0 : cb;
            v[E_PO0] = x7 ? 0 : (ca - cs) * 512; v[E_PO1] = x7 ? 0 : (cb - cs) * 512;
            v[E_RA0] = w4 * XT + (56 + c0) * 512; v[E_RA1] = jc >= 1 ? v[E_RA0] - 4 * XT : OOB; v[E_RA2] = jc >= 2 ? v[E_RA0] - 8 * XT : OOB;
            v[E_RB0] = rt + (56 + (x7 ? c0 : cb + sl)) * 512;
            v[E_RB1] = (jc == 0 && w4 == 0) ? OOB : ((!x7 && w4 == 0) ? rt + (56 + cs) * 512 : v[E_RB0]) - XT;
            v[E_RQ] = local ? OOB : rt + v[E_CF1] * 512;
            v[E_LB0] = LW + 40960 - w4 * 8192 + (x7 ? 0 : v[E_PO1] + sl * 512); v[E_LB1] = (!x7 && w4 == 0) ? LX : v[E_LB0] + 8192;
            v[E_MA] = two | (jc >= 1 ? two << 8 : 0) | (jc >= 2 ? two << 16 : 0);
            v[E_MB] = local ? 0 : (x7 ? 0xFF : two & 0xF0) | (jc ? (two & 0x88) << 8 : 0);
            v[E_PG0] = (x7 ? 1 + 3 * br : ty) * 32; v[E_PGD] = (x7 ? 1 : (nb == 15 ? 0 : nb - ty)) * 8;
            v[E_SL] = nb == 15 ? 8 : sl;
            v[E_H] = h; v[E_C0] = c0; v[E_PS] = 8 * ty - cs;
            v[E_PQ] = br ? (0x1189188u >> (4 * ty)) & 15 : 15; v[E_PK] = w4 != 3 ? v[E_PQ] : 15;
        }
    }
};
__device__ const Tab g_tab = Tab();

#define STAMP(i)
#define CST(i)
#define STAMP_DECL
#define STAMP_FLUSH()

DI u32x4 srd(const void* p, unsigned bytes) { const u64 a = (u64)p; return (u32x4){(unsigned)a, (unsigned)(a >> 32) & 0xffffu, bytes, 0x00020000u}; }
DI __amdgpu_buffer_rsrc_t rsrc(const void* p, int bytes) { return __builtin_amdgcn_make_buffer_rsrc((void*)p, 0, bytes, 0x00020000); }
DI void dma16(u32x4 r, int lds, int voff, int soff) {
    asm volatile("s_mov_b32 m0, %0\n\ts_nop 0\n\tbuffer_load_dwordx4 %1, %2, %3 offen lds" :: "s"(lds), "v"(voff), "s"(r), "s"(soff) : "memory");
}
DI void dma16_dev(u32x4 r, int lds, int voff, int soff) {
    asm volatile("s_mov_b32 m0, %0\n\ts_nop 0\n\tbuffer_load_dwordx4 %1, %2, %3 offen sc1 lds" :: "s"(lds), "v"(voff), "s"(r), "s"(soff) : "memory");
}
DI void dma64_dev(u32x4 r, int lds, int voff, int soff) {
    asm volatile("s_mov_b32 m0, %0\n\ts_nop 0\n\tbuffer_load_dwordx4 %1, %2, %3 offen sc1 lds\n\tbuffer_load_dwordx4 %1, %2, %3 offen offset:1024 sc1 lds\n\t"
                 "buffer_load_dwordx4 %1, %2, %3 offen offset:2048 sc1 lds\n\tbuffer_load_dwordx4 %1, %2, %3 offen offset:3072 sc1 lds" :: "s"(lds), "v"(voff), "s"(r), "s"(soff) : "memory");
}
DI void dma32_dev(u32x4 r, int lds, int voff, int soff) {
    asm volatile("s_mov_b32 m0, %0\n\ts_nop 0\n\tbuffer_load_dwordx4 %1, %2, %3 offen sc1 lds\n\tbuffer_load_dwordx4 %1, %2, %3 offen offset:1024 sc1 lds"
                 :: "s"(lds), "v"(voff), "s"(r), "s"(soff) : "memory");
}
DI void store_dev(u32x4 v, __amdgpu_buffer_rsrc_t r, int voff, int soff) {
    __builtin_amdgcn_raw_buffer_store_b128(v, r, voff, soff, 16);
    asm volatile("s_nop 1" :: "v"(v) : "memory");
}
template <int N>
DI void dma_rows(u32x4 r, int lds, int voff, int soff) {
    asm volatile("s_mov_b32 m0, %0\n\ts_nop 0\n\tbuffer_load_dwordx4 %2, %3, %1 offen lds\n.rept %4\n\ts_add_u32 m0, m0, 0x1000\n\ts_add_u32 %1, %1, 0x7000\n\tbuffer_load_dwordx4 %2, %3, %1 offen lds\n.endr"
                 : "+s"(lds), "+s"(soff) : "v"(voff), "s"(r), "i"(N - 1) : "scc", "memory");
}
DI bf16x8 tr_frag(const char* a0, const char* a1) {
    s16x4 lo = __builtin_amdgcn_ds_read_tr16_b64_v4i16((LAS s16x4*)a0);
    s16x4 hi = __builtin_amdgcn_ds_read_tr16_b64_v4i16((LAS s16x4*)a1);
    return __builtin_bit_cast(bf16x8, __builtin_shufflevector(lo, hi, 0, 1, 2, 3, 4, 5, 6, 7));
}
DI bf16x8 pack8(const f32x16& v, int s) {
    f32x8 t = {v[8 * s], v[8 * s + 1], v[8 * s + 2], v[8 * s + 3], v[8 * s + 4], v[8 * s + 5], v[8 * s + 6], v[8 * s + 7]};
    return __builtin_convertvector(t, bf16x8);
}
DI float xhalf(float v) {
    const unsigned u = __builtin_bit_cast(unsigned, v);
    const auto r = __builtin_amdgcn_permlane32_swap(u, u, false, false);
    return __builtin_bit_cast(float, (threadIdx.x & 32) ? r[0] : r[1]);
}
#define WAITV(n) asm volatile("s_waitcnt vmcnt(" #n ")" ::: "memory")
#define WAITL0() asm volatile("s_waitcnt lgkmcnt(0)" ::: "memory")
#define BAR() do { asm volatile("" ::: "memory"); __builtin_amdgcn_s_barrier(); asm volatile("" ::: "memory"); } while (0)
#define SCHED() __builtin_amdgcn_sched_barrier(0)

#define SG(m, n) __builtin_amdgcn_sched_group_barrier(m, n, 0)
DI float fmx(float a, float b) { return __builtin_elementwise_maximum(a, b); }
typedef int i32x4 __attribute__((ext_vector_type(4)));
#define PIN(x) asm volatile("" : "+v"(x))
DI void raise(char* p, u64 tag) { (void)__hip_atomic_exchange((gu64*)p, tag, RLX); }
template <bool TWO_BARRIERS>
DI u64 poll(const char* p, u64 all, u64 need, u64 tag) {
    u64 v, zero, sv, left; unsigned cnt = SPIN_MAX;
#define POLL_LOOP ".Lpw%=:\n\ts_waitcnt vmcnt(0)\n\tv_cmp_ne_u64 %4, %6, %0\n\ts_and_b64 vcc, %4, %8\n\ts_cbranch_scc0 .Lpd%=\n\ts_sub_u32 %3, %3, 1\n\ts_cbranch_scc1 .Lpd%=\n\t" \
                  "global_atomic_add_x2 %0, %5, %1, off sc0\n\ts_branch .Lpw%=\n.Lpd%=:\n\ts_mov_b64 exec, %2"
#define POLL_OPS : "=&v"(v), "=&v"(zero), "=&s"(sv), "+s"(cnt), "=&s"(left) : "v"(p), "s"(tag), "s"(all), "s"(need) : "vcc", "scc", "memory"
    if (TWO_BARRIERS)
        asm volatile("s_mov_b64 %2, exec\n\ts_mov_b64 exec, %7\n\tv_mov_b64 %1, 0\n\tglobal_atomic_add_x2 %0, %5, %1, off sc0\n\ts_barrier\n\ts_barrier\n" POLL_LOOP POLL_OPS);
    else
        asm volatile("s_mov_b64 %2, exec\n\ts_mov_b64 exec, %7\n\tv_mov_b64 %1, 0\n\tglobal_atomic_add_x2 %0, %5, %1, off sc0\n" POLL_LOOP POLL_OPS);
    return left;
}

extern "C" __global__ void __launch_bounds__(512, 2)
fused(const char* xa, const char* wa, const char* woa, const char* xb, const char* wb, const char* wob, char* out, char* ws) {
    __shared__ __attribute__((aligned(16))) char smem[LDS_BYTES];
    STAMP_DECL;
    STAMP(0);
    const int b = blockIdx.x, xcd = b & 7, idx = b >> 3;
    const int br = xcd & 1, jj = idx >> 3, ty = idx & 7, j = (xcd >> 1) * 4 + jj;
    unsigned d0 = threadIdx.x * 128;
    {
        u64 pc;
        asm volatile("s_cmp_lg_u32 %2, 7\n\ts_cbranch_scc1 .Lskip%=\n\tv_cmp_gt_u32 vcc, fused_end-.Lhere%=, %1\n\ts_cbranch_vccz .Lskip%=\n\ts_mov_b64 exec, vcc\n\t"
                     "s_getpc_b64 %0\n.Lhere%=:\n\tglobal_load_dword %1, %1, %0\n\ts_mov_b64 exec, -1\n.Lskip%=:"
                     : "=&s"(pc), "+v"(d0) : "s"(ty) : "vcc", "scc", "memory");
    }
    const int wave = __builtin_amdgcn_readfirstlane(threadIdx.x >> 6), lane = threadIdx.x & 63;
    const int r3 = lane >> 3, u = lane & 7;
    const int lbase = (int)(unsigned)(size_t)(LAS char*)smem;
    int vx0 = r3 * 896 + ((u ^ (r3 >> 1)) << 4), vx1 = vx0 ^ 64;
    int vw = r3 * 2688 + ((u ^ (((r3 >> 1) & 1) << 2)) << 4);
    const int rot = ROT == 2 ? ((ty + j) & 1) * 4 : 0;
    const int sx = j * (128 * 896), sw = ty * 128;
    int e0 = sx + wave * 14336 + rot * 128, e1 = sw + (rot * 64 + wave * 8) * 2688, e2 = lbase + RING + wave * 2048, e3 = lbase + RING + 16384 + wave * 1024;
    asm volatile("" : "+s"(e0), "+s"(e1), "+s"(e2), "+s"(e3)); asm volatile("" : "+v"(vx0), "+v"(vx1), "+v"(vw));
    SCHED();
    const u32x4 rx = srd(br ? xb : xa, XB), rw = srd(br ? wb : wa, WB);
    if (ty < 7) {
        dma16(rx, e2, vx0, e0);
        dma16(rw, e3, vw, e1);
        dma16(rx, e2 + 1024, vx1, e0 + 7168);
        dma16(rw, e3 + 8192, vw, e1 + 896);
    } else {
        {
            const int ln = jj * 392 + (threadIdx.x < 392 ? threadIdx.x : 0), row = (ln * 9363) >> 16;
            const int vl = row * 2688 + 1792 + (ln - row * 7) * 128;
            asm volatile("global_load_dword %0, %1, %2" : "+v"(d0) : "v"(vl), "s"(br ? wb : wa) : "memory");
            asm volatile("global_load_dword %0, %1, %2" : "+v"(d0) : "v"(vl + 224 * 2688), "s"(br ? wb : wa) : "memory");
        }
        const int wl = (jj * 784 + (threadIdx.x < 392 ? threadIdx.x : 0)) * 128;
        asm volatile("global_load_dword %0, %1, %2" : "+v"(d0) : "v"(wl), "s"(br ? wob : woa) : "memory");
        asm volatile("global_load_dword %0, %1, %2" : "+v"(d0) : "v"(wl + 392 * 128), "s"(br ? wob : woa) : "memory");
        asm volatile("s_waitcnt vmcnt(0)" : "+v"(d0) :: "memory");
    }
    u64 tag = *(const __attribute__((address_space(4))) u64*)ws;
    auto make_tag = [&]() { asm volatile("" : "+s"(tag)); tag = tag * 0x9E3779B97F4A7C15ull + 0x7F4A7C159E3779B9ull; asm volatile("" : "+s"(tag)); };
    const int w4 = wave & 3;
    const bool aux = wave >= 4;
    const int hh = lane >> 5, t = lane & 31, blk = (lane >> 4) & 1, q = (lane >> 2) & 3, p = lane & 3;
    const int T = 4 * j + w4;
    const bool local = br == 0 && ty < 7;
    typedef int i32x8 __attribute__((ext_vector_type(8)));
    typedef int i32x16 __attribute__((ext_vector_type(16)));
    const __attribute__((address_space(4))) char* te = (const __attribute__((address_space(4))) char*)g_tab.e[(min(j, 2) * 2 + br) * 32 + ty * 4 + w4];
    i32x8 tb8 = *(const __attribute__((address_space(4))) i32x8*)(te + 64);
    int h, c0;
    auto use_tab = [&]() { asm volatile("" : "+s"(tb8)); h = tb8[E_H - 16]; c0 = tb8[E_C0 - 16]; };
    const __amdgpu_buffer_rsrc_t rxs = rsrc(ws + WS_X, X_BYTES);
    const int wb0 = (8 * hh + q) * 128 + ((blk * 2 + (p & 1)) << 4) + (p >> 1) * 8 + ((q >> 1) << 6), wb1 = wb0 ^ 64;

    const unsigned NEGB = 0xF149F2CAu;
    f32x16 cd, co;
    u32x4 pm[2];
    int nd, no, lm;
    auto mask_bits = [&]() {
        const unsigned lo = (2u << t) - 1u;
        nd = (int)(~lo >> (4 * hh)); lm = (int)(lo >> (4 * hh)); no = T < 8 ? -1 : lm;
    };
    auto mask_reg = [&](int r) {
        const int bit = (r & 3) + 8 * (r >> 2);
        float a = __builtin_bit_cast(float, __builtin_amdgcn_sbfe(nd, bit, 1) & NEGB), c = __builtin_bit_cast(float, __builtin_amdgcn_sbfe(no, bit, 1) & NEGB);
        PIN(a); PIN(c); cd[r] = a; co[r] = c;
        if (r & 1) { unsigned m = __builtin_amdgcn_perm(__builtin_amdgcn_sbfe(lm, bit, 1), __builtin_amdgcn_sbfe(lm, bit - 1, 1), 0x05040100u); PIN(m); pm[r >> 3][(r >> 1) & 3] = m; }
    };
    auto koff = [&](int cf, int s) { return (2 * s + hh) * 512 + ((((t >> 2) ^ ((cf + 2 * s + hh) & 3)) << 2) + (t & 3)) * 16; };
    auto voff = [&](int hd, int cf, int tt) {
        const int pc8 = (4 * blk + 2 * (p & 1) + (p >> 1) + (hd & 1)) & 7, cc = pc8 >> 1;
        return cc * 512 + ((((2 * tt + hh) ^ ((cf + cc) & 3)) << 2) + q) * 16 + (pc8 & 1) * 8;
    };
    auto qfix = [&](int hd, u32x4& q0, u32x4& q1) {
        const bool z0 = (hd & 1) && !hh, z1 = !(hd & 1) && hh;
        q0.x = z0 ? 0u : q0.x; q0.y = z0 ? 0u : q0.y; q1.z = z1 ? 0u : q1.z; q1.w = z1 ? 0u : q1.w;
    };
    auto ex = [&](f32x16& s) {
#pragma unroll
        for (int r = 0; r < 16; ++r) s[r] = __builtin_amdgcn_exp2f(s[r]);
    };
    auto rowmax = [&](const f32x16& s) {
        float m = fmx(s[0], s[1]);
#pragma unroll
        for (int r = 2; r < 16; r += 2) m = fmx(fmx(m, s[r]), s[r + 1]);
        const unsigned mu = __builtin_bit_cast(unsigned, m);
        const auto r = __builtin_amdgcn_permlane32_swap(mu, mu, false, false);
        const unsigned r0 = r[0], r1 = r[1];
        return fmx(__builtin_bit_cast(float, r0), __builtin_bit_cast(float, r1));
    };
    const __amdgpu_buffer_rsrc_t ros = rsrc(ws + WS_O, O_BYTES);
    auto put_o = [&](f32x16 o, float l, int rec) {
        o = o * __builtin_amdgcn_rcpf(l);
        store_dev(__builtin_bit_cast(u32x4, pack8(o, 0)), ros, lane * 16, rec);
        store_dev(__builtin_bit_cast(u32x4, pack8(o, 1)), ros, lane * 16 + 1024, rec);
    };
    auto ones = [&](int back, bool two) {
        const int bb = back + (two ? hh : 0);
        if (two || !hh) *(u32x2*)(smem + LR + w4 * 4096 + (2 - bb) * 16384 + 2048 + ((h & 1) ? 0 : 1544) + t * 16) = (u32x2){j >= bb ? 0x3F80u : 0u, 0u};
    };
    LAS char* wd = (LAS char*)(smem + LW + 7 * 512 + 8);

    #define wk ((LAS char*)(smem + LR + 32768 + ((h & 1) ? 0 : 1544)))
#define FTAG ((tag | 1) & 0xBFFFBFFFBFFFBFFFull)
    auto arrive = [&](int n) { if (lane == 0) *(volatile LAS u64*)(wk + (n + w4) * 16) = FTAG; };
    auto await4 = [&](int n) {
#pragma unroll 1
        for (int i = 0; i < SPIN_MAX * 4; ++i) if (__all(*(volatile LAS u64*)(wk + (n + p) * 16) == FTAG)) break;
        asm volatile("" ::: "memory");
    };
    bf16x8 wo[28];
    auto read_wo = [&](int base) {
        int a = base + ((w4 >> 1) ? wb1 : wb0) + hh * (20 * 128);
        PIN(a);
#pragma unroll
        for (int ks = 0; ks < 28; ++ks) {
            const int i = ks % 7, row = (ks / 7) * 112 + (i < 6 ? (i / 3) * 56 + (i % 3) * 8 : 24);
            wo[ks] = tr_frag(smem + a + row * 128, smem + a + (row + (i < 6 ? 4 : 56)) * 128);
        }
    };
    const int vwo = r3 * 896 + ((u ^ (((r3 >> 1) & 1) << 2)) << 4);
    const u32x4 rwo = srd(br ? wob : woa, OB);

    if (__builtin_expect(aux, 1)) {
        const bool x7 = ty == 7;
        i32x16 tb = *(const __attribute__((address_space(4))) i32x16*)te;
        if (ty < 7) {
            int ch = rot, sg = 0;
#pragma unroll 1
            for (int c = 1; c < 7; ++c) {
                ++ch; if (ch == 7) ch = 0;
                ++sg; if (sg == 3) sg = 0;
                const int st = lbase + RING + sg * STAGE;
#pragma unroll
                for (int i = 0; i < 4; ++i) dma16(rx, st + w4 * 4096 + i * 1024, (i & 1) ? vx1 : vx0, sx + (w4 * 4 + i) * 7168 + ch * 128);
#pragma unroll
                for (int cb = 0; cb < 2; ++cb)
#pragma unroll
                    for (int i = 0; i < 2; ++i) dma16(rw, st + 16384 + cb * 8192 + w4 * 2048 + i * 1024, vw, sw + (ch * 64 + w4 * 16 + i * 8) * 2688 + cb * 896);
                WAITV(8); BAR();
            }
#pragma unroll 1
            for (int c = 0; c < 7; ++c) {
                ++ch; if (ch == 7) ch = 0;
#pragma unroll
                for (int i = 0; i < 2; ++i) dma16(rw, lbase + LA + c * 8192 + w4 * 2048 + i * 1024, vw, sw + (ch * 64 + w4 * 16 + i * 8) * 2688 + 1792);
                if (c == 3) { WAITV(8); BAR(); }
            }
            BAR();
        }
        make_tag(); use_tab();
        asm volatile("" : "+s"(tb));
        const int jo = j * (4 * XT), sl = tb8[E_SL - 16];
        int ra0 = tb[E_RA0] + jo, ra1 = tb[E_RA1] + jo, ra2 = tb[E_RA2] + jo, rb0 = tb[E_RB0] + jo, rb1 = tb[E_RB1] + jo, rq = tb[E_RQ] + jo, lh = lbase + LR + w4 * 4096;
        asm volatile("" : "+s"(ra0), "+s"(ra1), "+s"(ra2), "+s"(rb0), "+s"(rb1), "+s"(rq), "+s"(lh));
        const int lb0 = lbase + tb[E_LB0], lb1 = lbase + tb[E_LB1];
        const int hd[2] = {tb[E_HD0], tb[E_HD1]}, cf[2] = {tb[E_CF0], tb[E_CF1]}, po[2] = {tb8[E_PO0 - 16], tb8[E_PO1 - 16]};
        const u64 mb = (u64)(unsigned)tb[E_MB] << 32, mall = mb | (unsigned)tb[E_MA];
        const char* f1 = ws + (WS_F1 + j * 256 + tb[E_PG0]) + (hh * 4096 - (r3 & 3) * 256 + (lane & 4) * tb[E_PGD] + p * 8);
        int l16 = lane * 16;
        PIN(f1); PIN(l16);
        int lz = lane; PIN(lz);
        const int g4 = lz >> 4, c16 = lz & 15;
        int fo[2], ko[2], qo[2], vo[2][2];
        f32x4 md, mo;
        const bool pt = x7 || g4 == sl;
#pragma unroll
        for (int e = 0; e < 2; ++e) fo[e] = g4 * 512 + (((c16 >> 1) ^ ((cf[e] + g4) & 3)) << 6) + (c16 & 1) * 32;
        int qs = pt ? fo[1] : OOB; PIN(qs);
        if (!local) {
#pragma unroll
            for (int e = 0; e < 2; ++e) { ko[e] = LW + 40960 - w4 * 8192 + po[e] + fo[e]; qo[e] = LW + w4 * 4096 + po[e] + fo[e]; PIN(ko[e]); PIN(qo[e]); }
#pragma unroll
            for (int r = 0; r < 4; ++r) {
                const int d = c16 - 4 * g4 - r;
                md[r] = (unsigned)d < 8u ? 0.f : -1.0e30f; mo[r] = (d < -8 && T) ? 0.f : -1.0e30f;
            }
            PIN(md); PIN(mo);
        }
        if (ty < 7) { WAITV(6); BAR(); }
        if (wave == 4 && lane < 2) *(volatile LAS u64*)(wd + lane * 16) = 0;
        asm volatile("s_waitcnt vmcnt(0) lgkmcnt(0)" ::: "memory"); BAR();
        STAMP(4);
        const u32x4 rxx = srd(ws + WS_X, X_BYTES);
        auto tell = [&](int n) { if (lane == 0) *(volatile LAS u64*)(n < 2 ? wd + n * 16 : wk + 64) = FTAG; };
        auto hear = [&](int n) {
#pragma unroll 1
            for (int i = 0; i < SPIN_MAX * 4; ++i) { if (__all(*(volatile LAS u64*)(n < 2 ? wd + n * 16 : wk + 64) == FTAG)) break; __builtin_amdgcn_s_sleep(1); }
            asm volatile("" ::: "memory");
        };
        auto fetch_b = [&](int v) {
            const int go = v * (56 * 512), lo = v * 4096;
            if (x7) { dma32_dev(rxx, lb0 + lo, l16, rb0 + go); dma32_dev(rxx, lb1 + lo, l16, rb1 + go); }
            else {
                if (w4 == 0) dma64_dev(rxx, lb1 + lo, l16, rb1 + go);
                if (sl != 8 && lane < 32) { dma16_dev(rxx, lb0 + lo, l16, rb0 + go); if (w4) dma16_dev(rxx, lb1 + lo, l16, rb1 + go); }
            }
        };
        auto fetch_a = [&](int v) {
            const int go = v * (56 * 512), lo = v * 2048;
            dma32_dev(rxx, lh + 32768 + lo, l16, ra0 + go);
            dma32_dev(rxx, lh + 16384 + lo, l16, ra1 + go);
            dma32_dev(rxx, lh + lo, l16, ra2 + go);
        };
        STAMP(5);
        unsigned left = 1;
        if (!local) { if (wave == 4) { left = __builtin_amdgcn_readfirstlane((unsigned)poll<false>(f1, mall, mb, tag)); tell(0); if (!left) tell(1); } else hear(0); }
        STAMP(2);
        u32x4 qp[2];
        asm volatile("buffer_load_dwordx4 %0, %2, %3, %4 offen sc1\n\tbuffer_load_dwordx4 %1, %2, %3, %4 offen offset:16 sc1"
                     : "=&v"(qp[0]), "=&v"(qp[1]) : "v"(qs), "s"(rxx), "s"(rq) : "memory");
        if (!local) fetch_b(0);
        if (wave == 4) { if (left) { poll<false>(f1, mall ^ mb, mall ^ mb, tag); tell(1); } } else hear(1);
        STAMP(6);
        dma32_dev(rxx, lh + 2048, l16, ra0 - 56 * 512);
        fetch_a(0);
        WAITV(8);
        PIN(qp[0]); PIN(qp[1]);
        STAMP(3);
        bf16x8 pe[2][2];
        float ls[2][2];
        if (!local) {
            f32x4 sd[2][2], so[2][2];
#pragma unroll
            for (int e = 0; e < 2; ++e)
#pragma unroll
                for (int y = 0; y < 2; ++y) {
                    const char* kb = smem + ko[e] + y * 16;
                    u32x4 qv = *(const u32x4*)(smem + qo[e] + y * 16);
                    const bool a = pt && (e || x7), z0 = (hd[e] & 1) && g4 == 0, z1 = !(hd[e] & 1) && g4 == 3;
                    qv = (u32x4){a ? qp[y].x : qv.x, a ? qp[y].y : qv.y, a ? qp[y].z : qv.z, a ? qp[y].w : qv.w};
                    qv = (u32x4){z0 ? 0u : qv.x, z0 ? 0u : qv.y, z1 ? 0u : qv.z, z1 ? 0u : qv.w};
                    sd[e][y] = MFMA16(*(const bf16x8*)kb, __builtin_bit_cast(bf16x8, qv), md);
                    so[e][y] = MFMA16(*(const bf16x8*)(kb + 8192), __builtin_bit_cast(bf16x8, qv), mo);
                }
            const bf16x8 one = __builtin_bit_cast(bf16x8, (u32x4){0x3F803F80u, 0x3F803F80u, 0x3F803F80u, 0x3F803F80u});
            float m[4];
#pragma unroll
            for (int i = 0; i < 4; ++i) {
                const f32x4 a = sd[i >> 1][i & 1], c = so[i >> 1][i & 1];
                m[i] = fmx(fmx(fmx(fmx(a[0], a[1]), a[2]), fmx(fmx(a[3], c[0]), c[1])), fmx(c[2], c[3]));
            }
#pragma unroll
            for (int i = 0; i < 4; ++i) {
                const unsigned mu = __builtin_bit_cast(unsigned, m[i]);
                const auto r = __builtin_amdgcn_permlane32_swap(mu, mu, false, false);
                const unsigned r0 = r[0], r1 = r[1];
                m[i] = fmx(__builtin_bit_cast(float, r0), __builtin_bit_cast(float, r1));
            }
#pragma unroll
            for (int i = 0; i < 4; ++i) {
                const unsigned mu = __builtin_bit_cast(unsigned, m[i]);
                const auto r = __builtin_amdgcn_permlane16_swap(mu, mu, false, false);
                const unsigned r0 = r[0], r1 = r[1];
                m[i] = fmx(__builtin_bit_cast(float, r0), __builtin_bit_cast(float, r1));
            }
            SCHED();
            f32x4 sm[4];
#pragma unroll
            for (int i = 0; i < 4; ++i) {
                const f32x4 a = sd[i >> 1][i & 1], c = so[i >> 1][i & 1];
                f32x8 x = {a[0], a[1], a[2], a[3], c[0], c[1], c[2], c[3]};
                x -= m[i];
#pragma unroll
                for (int r = 0; r < 8; ++r) x[r] = __builtin_amdgcn_exp2f(x[r]);
                pe[i >> 1][i & 1] = __builtin_convertvector(x, bf16x8);
                sm[i] = MFMA16(one, pe[i >> 1][i & 1], (f32x4){});
                SCHED();
            }
#pragma unroll
            for (int i = 0; i < 4; ++i) ls[i >> 1][i & 1] = __builtin_amdgcn_rcpf(sm[i][0]);
#pragma unroll
            for (int i = 0; i < 4; ++i) { PIN(pe[i >> 1][i & 1]); PIN(ls[i >> 1][i & 1]); }
        }
        STAMP(10);
        asm volatile("s_waitcnt vmcnt(0) lgkmcnt(0)" ::: "memory");
        STAMP(7);
        __builtin_amdgcn_s_setprio(2);
        if (wave == 4) { poll<true>(f1 + 8192, mall, mall, tag); tell(2); }
        else {
            BAR(); BAR();
            dma_rows<14>(rwo, lbase + LA + w4 * 1024, vwo, w4 * 7168 + (ty & 6) * 128);
            hear(2);
        }
        STAMP(14);
        if (!local) fetch_b(1);
        fetch_a(1);
        if (!YEARLY) dma_rows<YB0>(rwo, lbase + LA + w4 * 1024, vwo, w4 * 7168 + (ty & 6) * 128);
        if (!local) {
            PIN(lz);
#pragma unroll
            for (int e = 0; e < 2; ++e)
#pragma unroll
                for (int s = 0; s < 2; ++s) {
                    const int pc8 = (2 * (lz & 3) + s + (hd[e] & 1)) & 7, cc = pc8 >> 1;
                    vo[e][s] = LW + 40960 - w4 * 8192 + 4096 + po[e] + cc * 512 + (((lz >> 3) ^ ((cf[e] + cc) & 3)) << 6) + (q & 1) * 32 + (pc8 & 1) * 8; PIN(vo[e][s]);
                }
        }
        asm volatile("s_waitcnt vmcnt(%0)" :: "i"(YEARLY ? 0 : YB0) : "memory");
        ones(0, true); ones(2, false);
        WAITL0();
        arrive(0);
        if (!YEARLY) dma_rows<14 - YB0>(rwo, lbase + LA + (w4 + 4 * YB0) * 1024, vwo, (w4 + 4 * YB0) * 7168 + (ty & 6) * 128);
        else if (wave == 4) dma_rows<14>(rwo, lbase + LA, vwo, (ty & 6) * 128);
        STAMP(9);
        if (!local) {
            u32x4 ov[2][2];
#pragma unroll
            for (int e = 0; e < 2; ++e)
#pragma unroll
                for (int y = 0; y < 2; ++y) {
                    const char* v0 = smem + vo[e][0] + y * 16; const char* v1 = smem + vo[e][1] + y * 16;
                    const f32x4 a = MFMA16(tr_frag(v0, v0 + 8192), pe[e][y], (f32x4){}), c = MFMA16(tr_frag(v1, v1 + 8192), pe[e][y], (f32x4){});
                    f32x8 x = {a[0], a[1], a[2], a[3], c[0], c[1], c[2], c[3]};
                    x *= ls[e][y];
                    ov[e][y] = __builtin_bit_cast(u32x4, __builtin_convertvector(x, bf16x8));
                }
#pragma unroll
            for (int e = 0; e < 2; ++e)
#pragma unroll
                for (int y = 0; y < 2; ++y) store_dev(ov[e][y], ros, g4 * 512 + c16 * 16 + y * 256, ((64 + T) * 16 + hd[e]) * 2048);
        }
        STAMP(11);
        __builtin_amdgcn_s_setprio(0);
        if (!local) { arrive(8); WAITV(4); arrive(16); await4(8); } else { WAITV(0); arrive(16); }
        dma_rows<6>(rwo, lbase + LW + w4 * 1024, vwo, ty < 6 ? w4 * 7168 + (ty | 1) * 128 : OOB);
        if (!local) { WAITV(6); if (lane < 2) raise(ws + WS_F2 + (64 + T) * 128 + (lane ? hd[1] : hd[0]) * 8, tag); }
        if (ty < 6) dma_rows<8>(rwo, lbase + LW + (w4 + 24) * 1024, vwo, (w4 + 24) * 7168 + (ty | 1) * 128);
        if (b == 0) {
            poll<false>(ws + WS_F1 + w4 * 2048 + lane * 32, ~0ull, ~0ull, tag);
            arrive(12);
            if (wave == 4) { await4(12); if (lane == 0) __hip_atomic_store((gu64*)ws, tag, RLX); }
        }
        WAITV(0);
    } else {
        mask_bits();
        if (ty < 7) {
            int xo[2][4], wo_[2][2];
#pragma unroll
            for (int g = 0; g < 2; ++g) {
#pragma unroll
                for (int ks = 0; ks < 4; ++ks) { xo[g][ks] = RING + g * 65536 + (w4 * 32 + t) * 128 + (((2 * ks + hh) ^ ((t >> 1) & 7)) << 4); PIN(xo[g][ks]); }
                wo_[g][0] = RING + g * 65536 + wb0; wo_[g][1] = RING + g * 65536 + wb1; PIN(wo_[g][0]); PIN(wo_[g][1]);
            }
            int es[2], el[2][2], g0;
            unsigned pq, pk;
            char* f1;
            auto put = [&](const f32x16& a0, const f32x16& a1, int sec, unsigned pub, u32x4 (&d)[2][2]) {
#pragma unroll
                for (int e = 0; e < 2; ++e)
#pragma unroll
                    for (int s = 0; s < 2; ++s) {
                        d[e][s] = __builtin_bit_cast(u32x4, pack8(e ? a1 : a0, s));
                        if ((pub >> (2 * e + s)) & 1) store_dev(d[e][s], rxs, es[s] + e * 2048, g0 + sec * (56 * 512));
                    }
            };
            auto keep = [&](int lo, const u32x4 (&d)[2][2]) {
#pragma unroll
                for (int e = 0; e < 2; ++e)
#pragma unroll
                    for (int s = 0; s < 2; ++s) if (el[e][s] >= 0) *(u32x4*)(smem + el[e][s] + lo) = d[e][s];
            };
            f32x16 acc[4];
            bf16x8 xf[28], wf[2][4];
            const f32x16 zero = {};
            WAITV(0); BAR();
            STAMP(1); CST(20);
            auto rd = [&](int n) {
                const int sg = (n >> 2) % 3, ks = n & 3;
                const char* st = smem + (sg & 1) * STAGE;
#pragma unroll
                for (int i = 3; i >= 0; --i) {
                    const char* a = st + wo_[sg >> 1][i & 1] + 16384 + (i >> 1) * 8192 + ks * 2048;
                    wf[n & 1][i] = tr_frag(a, a + 512);
                    if (i == 3) xf[n] = *(const bf16x8*)(st + xo[sg >> 1][ks]);
                }
            };
            rd(0);
            SCHED();
#pragma unroll
            for (int n = 0; n < 28; ++n) {
                if (n == 11) {
                    make_tag(); use_tab();
#pragma unroll
            for (int s = 0; s < 2; ++s) {
                const int sw16 = ((((t >> 2) ^ (2 * s + hh)) << 2) + (t & 3)) * 16;
                es[s] = (2 * s + hh) * 512 + sw16; PIN(es[s]);
#pragma unroll
                for (int e = 0; e < 2; ++e) {
                    const int ps = tb8[E_PS - 16] + 4 * e + 2 * s + hh;
                    el[e][s] = (unsigned)ps < 8u ? ps * 512 + sw16 : -1; PIN(el[e][s]);
                }
            }
            pq = tb8[E_PQ - 16]; pk = tb8[E_PK - 16];
            g0 = (br * 64 + T) * XT + ty * 4096;
            f1 = ws + WS_F1 + (br * 16 + j) * 256 + ty * 32 + w4 * 8;
            asm volatile("" : "+s"(g0), "+s"(f1));
                    SCHED();
                }
                if ((n & 3) == 3) { WAITL0(); BAR(); SCHED(); }
                if (n < 27) rd(n + 1);
#pragma unroll
                for (int i = 3; i >= 0; --i) acc[i] = MFMA(wf[n & 1][i], xf[n], n ? acc[i] : zero);
                if (n < 27) { SG(0x8, 1); SG(0x100, 3); SG(0x8, 1); SG(0x100, 3); SG(0x8, 1); SG(0x100, 3); SG(0x8, 1); }
                SCHED();
            }
            STAMP(2);
            const float qs = 0.18898223650461361f * 1.4426950408889634f;
            {
                u32x4 dk[2][2], dq[2][2];
                put(acc[2], acc[3], 1, pk, dk);
                put(acc[0] * qs, acc[1] * qs, 0, pq, dq);
                if (br) { keep(LW + 40960 - w4 * 8192, dk); keep(LW + w4 * 4096, dq); }
            }
            STAMP(3);
            BAR();
            bf16x8 wv[8];
            u32x4 dv[2][2];
            auto rv = [&](int n) { const char* a = smem + LA + (n % 28) * 2048 + (n >= 28 ? wb1 : wb0); wv[n & 7] = tr_frag(a, a + 512); };
            auto putv = [&](int e, int s) {
                dv[e][s] = __builtin_bit_cast(u32x4, pack8(acc[2 + e], s));
                store_dev(dv[e][s], rxs, es[s] + e * 2048, ((pk >> (2 * e + s)) & 1) ? g0 + 112 * 512 : OOB);
            };
#pragma unroll
            for (int n = 0; n < 6; ++n) rv(n);
            PIN(nd); PIN(no); PIN(lm);
            SCHED();
#pragma unroll
            for (int n = 0; n < 56; ++n) {
                if (n == BV1) { BAR(); SCHED(); }
                if (n == RAISE_B || n == RAISE_A) { if ((n == RAISE_B) == (br != 0)) { WAITV(0); if (lane == 0) raise(f1, tag); STAMP(9); } SCHED(); }
                acc[2 + n / 28] = MFMA(wv[n & 7], xf[n % 28], n % 28 ? acc[2 + n / 28] : zero);
                if (n + 6 < 56) rv(n + 6);
                if (n >= 36 && n < 52) mask_reg(n - 36);
                SG(0x8, 1); SG(0x100, 2); SG(0x2, 8);
                SCHED();
                if (n == 30 || n == 32) { putv(0, (n - 30) >> 1); SCHED(); }
            }
            STAMP(4); CST(21);
            putv(1, 0); putv(1, 1);
            if (br) keep(LW + 45056 - w4 * 8192, dv);
            STAMP(5);
            WAITV(0);
            if (lane == 0) raise(f1 + 8192, tag);
        } else {
            make_tag(); use_tab();
            BAR();
            if (threadIdx.x == 0) raise(ws + WS_F1 + (br * 16 + j) * 256 + 7 * 32, tag);
#pragma unroll
            for (int r = 0; r < 16; ++r) mask_reg(r);
        }
        const char* tl = smem + LR + w4 * 4096;
        int fo[2] = {koff(c0, 0), koff(c0, 1)}, vo[2] = {voff(h, c0, 0) + 2048, voff(h, c0, 1) + 2048};
        PIN(fo[0]); PIN(fo[1]); PIN(vo[0]); PIN(vo[1]);
        STAMP(6);
        WAITL0(); BAR();
        STAMP(7);
        __builtin_amdgcn_s_setprio(1);
        u32x4 qf[2];
        qf[0] = *(const u32x4*)(tl + 2048 + fo[0]); qf[1] = *(const u32x4*)(tl + 2048 + fo[1]);
        auto kfrag = [&](int n, int s) { return *(const bf16x8*)(tl + (8 - n) * 4096 + fo[s]); };
        auto vfrag = [&](int n, int s) { const char* vb = tl + (8 - n) * 4096 + s * 256; return tr_frag(vb + vo[0], vb + vo[1]); };
        bf16x8 k0 = kfrag(0, 0), k1 = kfrag(0, 1), k2 = kfrag(8, 0), k3 = kfrag(8, 1);
        WAITL0(); BAR();
        CST(19); STAMP(10);
        qfix(h, qf[0], qf[1]);
        const bf16x8 q0 = __builtin_bit_cast(bf16x8, qf[0]), q1 = __builtin_bit_cast(bf16x8, qf[1]);
        auto qk = [&](int n, const f32x16& c) { f32x16 r = MFMA(kfrag(n, 0), q0, c); return MFMA(kfrag(n, 1), q1, r); };
        f32x16 cm, sa, sb;
        bf16x8 pp[8][2];
        cd = MFMA(k0, q0, cd); co = MFMA(k2, q0, co); cd = MFMA(k1, q1, cd); co = MFMA(k3, q1, co);
        k0 = kfrag(1, 0); k1 = kfrag(1, 1);
#pragma unroll
        for (int r = 0; r < 16; ++r) cd[r] = fmx(cd[r], co[r]);
        {
            const float m = -rowmax(cd);
#pragma unroll
            for (int r = 0; r < 16; ++r) cm[r] = m;
        }
        cd += cm;
        sa = cd;
        SCHED();
#define STG(n, cur, nxt) do { \
            if (n < 7) { nxt = MFMA(k0, q0, cm); nxt = MFMA(k1, q1, nxt); } \
            if (n < 6) { k0 = kfrag(n + 2, 0); k1 = kfrag(n + 2, 1); } \
            ex(cur); pp[n][0] = pack8(cur, 0); pp[n][1] = pack8(cur, 1); \
            if (n < 7) SG(0x8, 1); SG(0x400, 4); if (n < 7) SG(0x8, 1); if (n < 6) SG(0x100, 2); SG(0x400, 4); SG(0x2, 2); SG(0x400, 4); SG(0x2, 2); SG(0x400, 4); SG(0x2, 4); SCHED(); } while (0)
        STG(0, sa, sb); STG(1, sb, sa); STG(2, sa, sb); STG(3, sb, sa); STG(4, sa, sb); STG(5, sb, sa); STG(6, sa, sb); STG(7, sb, sa);
        bf16x8 pz[2];
#pragma unroll
        for (int s = 0; s < 2; ++s) {
            const u32x4 e = __builtin_bit_cast(u32x4, pp[0][s]);
            pp[0][s] = __builtin_bit_cast(bf16x8, e & pm[s]); pz[s] = __builtin_bit_cast(bf16x8, e & ~pm[s]); PIN(pz[s]);
        }
#pragma unroll
        for (int n = 0; n < 8; ++n) { PIN(pp[n][0]); PIN(pp[n][1]); }
        char* f2 = ws + WS_F2 + T * 128 + h * 8;
        int orec = (T * 16 + h) * 2048;
        const char* va = tl + vo[0]; const char* vb = tl + vo[1];
        asm volatile("" : "+s"(f2), "+s"(orec));
        STAMP(8);
        await4(0);
        STAMP(14);
        f32x16 o = {};
        auto pv = [&](int n, bf16x8 p0, bf16x8 p1) { o = MFMA(vfrag(n, 0), p0, o); o = MFMA(vfrag(n, 1), p1, o); };
        {
            bf16x8 vf[18];
            auto rdv = [&](int i) { const int off = (8 - (i >> 1)) * 4096 + (i & 1) * 256; vf[i] = tr_frag(va + off, vb + off); };
#pragma unroll
            for (int i = 0; i < 5; ++i) { rdv(i); SCHED(); }
#pragma unroll
            for (int i = 0; i < 18; ++i) {
                o = MFMA(vf[i], i < 16 ? pp[i >> 1][i & 1] : pz[i & 1], o);
                if (i + 5 < 18) rdv(i + 5);
                SCHED();
            }
        }
        auto publish = [&]() {
            const float lf = o[12];
            const unsigned lu = __builtin_bit_cast(unsigned, lf);
            const unsigned lx = __builtin_amdgcn_permlane32_swap(lu, lu, false, false)[1];
            put_o(o, __builtin_bit_cast(float, lx), orec);
        };
        publish();
        STAMP(11); CST(22);
        float big = fmx(__builtin_fabsf(o[0]), __builtin_fabsf(o[1]));
#pragma unroll
        for (int r = 2; r < 16; r += 2) big = fmx(fmx(big, __builtin_fabsf(o[r])), __builtin_fabsf(o[r + 1]));
        if (__builtin_expect(!__all(big < 1.0e36f), 0)) {
            const int nmid = T >= 7 ? 7 : T;
            float m = 0.f;
#pragma unroll 1
            for (int i = 1; i <= nmid; ++i) m = fmx(m, rowmax(qk(i, cm)));
            cd -= m; cm -= m;
            ex(cd);
            o = (f32x16){};
            const u32x4 e0 = __builtin_bit_cast(u32x4, pack8(cd, 0)), e1 = __builtin_bit_cast(u32x4, pack8(cd, 1));
            pv(0, __builtin_bit_cast(bf16x8, e0 & pm[0]), __builtin_bit_cast(bf16x8, e1 & pm[1]));
            pv(8, __builtin_bit_cast(bf16x8, e0 & ~pm[0]), __builtin_bit_cast(bf16x8, e1 & ~pm[1]));
#pragma unroll 1
            for (int i = 1; i <= nmid; ++i) {
                f32x16 s = qk(i, cm);
                ex(s); pv(i, pack8(s, 0), pack8(s, 1));
            }
            WAITV(0);
            publish();
        }
        WAITV(0);
        if (lane == 0) raise(f2, tag);
        __builtin_amdgcn_s_setprio(0);
        STAMP(12);
        await4(16);
        read_wo(LA);
    }
    STAMP(13);
    const int To = br * 64 + 4 * j + 2 * (ty & 1);
    const int tr = br ? 2 * (t & 15) + (t >> 4) : t;
    const int tb = LR + 32768 + ((wave >> 2) * 2 + (w4 & 1)) * 4096;
    int l16 = lane * 16, oa = (w4 & 1) * LR + hh * 2048 + t * 16, tw = tb + tr * 128 + (((4 * (w4 >> 1) + hh) ^ (tr & 7)) << 4), tg = tb + (w4 >> 1) * 2048 + r3 * 128 + ((u ^ r3) << 4);
    int ov = r3 * 1792 + u * 16;
    int oo = ((4 * j + 2 * (ty & 1) + (w4 & 1)) * 32 + (w4 >> 1) * 16) * 1792 + br * 896 + ((ty & 6) + (wave >> 2)) * 128;
    const int os = (To * 16 + w4) * 2048, ol = lbase + w4 * 2048;
    asm volatile("" : "+v"(l16), "+v"(oa), "+v"(tw), "+v"(tg), "+v"(ov)); asm volatile("" : "+s"(oo));
    const __amdgpu_buffer_rsrc_t rout = rsrc(out, 2048 * 1792);
    const u32x4 roo = srd(ws + WS_O, O_BYTES);
    const bool idle = aux && ty >= 6;
    if (wave == 0) poll<false>(ws + WS_F2 + To * 128 + lane * 8, 0xFFFFFFFFull, 0xFFFFFFFFull, tag);
    WAITL0(); BAR();
    STAMP(15);
    if (idle) { STAMP_FLUSH(); return; }
    auto geto = [&](int qd) { dma32_dev(roo, ol + qd * 8192, l16, os + qd * 8192); dma32_dev(roo, ol + qd * 8192 + LR, l16, os + qd * 8192 + 32768); };
    f32x16 acc = {};
    {
        bf16x8 of[2][7];
        auto rdo = [&](int qd) {
            const char* a = smem + oa + qd * 8192;
#pragma unroll
            for (int i = 0; i < 6; ++i) of[qd & 1][i] = *(const bf16x8*)(a + (i / 3) * 4096 + (i % 3) * 512);
            const u32x2 lo = *(const u32x2*)(a + 1536), hi = *(const u32x2*)(a + 4096 + 1536);
            of[qd & 1][6] = __builtin_bit_cast(bf16x8, (u32x4){lo.x, lo.y, hi.x, hi.y});
        };
        auto mmo = [&](int qd, int i0, int i1) {
#pragma unroll
            for (int i = i0; i < i1; ++i) acc = MFMA(wo[qd * 7 + i], of[qd & 1][i], acc);
            SCHED();
        };
        if (aux) read_wo(LW); else { geto(0); geto(1); geto(2); WAITV(8); }
        BAR(); rdo(0); SCHED();
        if (!aux) geto(3);
        mmo(0, 0, 3);
        WAITV(8);
        BAR(); rdo(1); SCHED();
        mmo(0, 3, 7); mmo(1, 0, 3);
        WAITV(4);
        BAR(); rdo(2); SCHED();
        mmo(1, 3, 7); mmo(2, 0, 3);
        WAITV(0);
        BAR(); rdo(3); SCHED();
        mmo(2, 3, 7); mmo(3, 0, 7);
    }
    STAMP(16);
    {
#pragma unroll
        for (int s = 0; s < 2; ++s) *(bf16x8*)(smem + (tw ^ (s << 5))) = pack8(acc, s);
        WAITL0(); BAR();
        const u32x4 v0 = *(const u32x4*)(smem + tg), v1 = *(const u32x4*)(smem + tg + 1024);
        SCHED();
        store_dev(v0, rout, ov, oo); store_dev(v1, rout, ov, oo + 8 * 1792);
    }
    STAMP(17);
    STAMP_FLUSH();
}

extern "C" __device__ __attribute__((noinline, used)) void fused_end() {}

extern "C" void kernel_launch(void* const* d_in, const int* in_sizes, int n_in, void* d_out, int out_size, void* d_ws, size_t ws_size, hipStream_t stream) {
    fused<<<256, 512, 0, stream>>>((const char*)d_in[0], (const char*)d_in[1], (const char*)d_in[2], (const char*)d_in[3], (const char*)d_in[4], (const char*)d_in[5], (char*)d_out, (char*)d_ws);
}
```
